```python
import torch

def _softmax(x, axis=-1):
    return torch.softmax(x, dim=axis)

def _gelu(x):
    return torch.nn.functional.gelu(x, approximate='tanh')

def _silu(x):
    return torch.nn.functional.silu(x)

def _sigmoid(x):
    return torch.sigmoid(x)

def _layernorm(x, axis=-1, eps=1e-05):
    m = x.mean(dim=axis, keepdim=True)
    v = x.var(dim=axis, keepdim=True, unbiased=False)
    return (x - m) / torch.sqrt(v + eps)

def _scatter_add(tgt, idx, src, axis=0):
    return tgt.clone().index_add_(axis, idx, src)

def _segment_sum(src, seg, n):
    out = torch.zeros((n,) + src.shape[1:], dtype=src.dtype)
    return out.index_add_(0, seg, src)

def _pad(x, pw):
    flat = tuple((v for pair in reversed(pw) for v in pair))
    return torch.nn.functional.pad(x, flat)
kernel_name = 'standalone__mixer_gmlp__prompt'

def setup_inputs():

    _w = lambda a: (lambda t: t.to(torch.bfloat16) if t.is_floating_point() else t)(torch.as_tensor(a))
    B, S, D, F = (1, 2048, 448, 1792)
    Fh = F // 2
    return {'x': _w((lambda *s: torch.randn(*s, dtype=torch.bfloat16) * 0.05)(B, S, D)), 'w_in': _w((lambda *s: torch.randn(*s, dtype=torch.bfloat16) * 0.4)(D, 2 * Fh)), 'w_sp': _w(0.01 * (lambda *s: torch.randn(*s, dtype=torch.bfloat16) * 0.4)(S, S)), 'b_sp': _w(1.0 + 0.01 * (lambda *s: torch.randn(*s, dtype=torch.bfloat16) * 0.05)(S)), 'w_out': _w((lambda *s: torch.randn(*s, dtype=torch.bfloat16) * 0.05)(Fh, D))}

def reference(x, w_in, w_sp, b_sp, w_out):
    B, S, D = x.shape
    F = w_in.shape[-1]
    Fh = F // 2
    z = _gelu(torch.einsum('bsd,df->bsf', x, w_in))
    z1 = z[..., :Fh].clone()
    z2 = _layernorm(z[..., Fh:], axis=-1)
    g = torch.einsum('ts,bsf->btf', w_sp, z2) + b_sp[None, :, None]
    return torch.einsum('bsf,fd->bsd', z1 * g, w_out)
```

```cpp
#include <hip/hip_runtime.h>
#include <hip/hip_bf16.h>
#include <stdint.h>

typedef __attribute__((ext_vector_type(8))) short bf16x8;
typedef __attribute__((ext_vector_type(4))) short s16x4;
typedef __attribute__((ext_vector_type(4))) float f32x4;
typedef __attribute__((ext_vector_type(4))) unsigned v4u;
typedef __attribute__((ext_vector_type(2))) unsigned v2u;
typedef __attribute__((address_space(3))) s16x4* lds_s16x4_p;

#define S_ 2048
#define D_ 448
#define F_ 1792
#define FH_ 896

#define WS_Z1 0
#define WS_Z2 (WS_Z1 + 3670016)
#define WS_H  (WS_Z2 + 3670016)
#define WS_WO (WS_H + 3670016)

#define SCHED() __builtin_amdgcn_sched_barrier(0)

__device__ __forceinline__ f32x4 mfma16(v4u a, v4u b, f32x4 c) {
  return __builtin_amdgcn_mfma_f32_16x16x32_bf16(__builtin_bit_cast(bf16x8, a), __builtin_bit_cast(bf16x8, b), c, 0, 0, 0);
}
__device__ __forceinline__ unsigned pack_bf16(float lo, float hi) {
  __hip_bfloat16 a = __float2bfloat16(lo), b = __float2bfloat16(hi);
  return (unsigned)__builtin_bit_cast(unsigned short, a) | ((unsigned)__builtin_bit_cast(unsigned short, b) << 16);
}
__device__ __forceinline__ float bf_lo(unsigned u) { return __builtin_bit_cast(float, u << 16); }
__device__ __forceinline__ float bf_hi(unsigned u) { return __builtin_bit_cast(float, u & 0xffff0000u); }

__device__ __forceinline__ float gelu_f(float x) {
  const float c0 = -2.0f * 0.7978845608028654f * 1.4426950408889634f;
  const float c1 = c0 * 0.044715f;
  float t = __builtin_fmaf(x * x, c1, c0);
  float e = __builtin_amdgcn_exp2f(x * t);
  return x * __builtin_amdgcn_rcpf(1.0f + e);
}

#define K1_XB 57344
#define K1_SLOT 14336
#define K1_LDS (K1_XB + 7 * K1_SLOT)

__global__ __launch_bounds__(256, 1) void k1_gemm_gelu(const unsigned short* __restrict__ x, const unsigned short* __restrict__ win,
                                                      char* __restrict__ ws) {
  extern __shared__ __attribute__((aligned(16))) char smem[];
  const int lane = threadIdx.x & 63;
  const int wid = __builtin_amdgcn_readfirstlane(threadIdx.x >> 6);
  const int xcd = blockIdx.x & 7, idx = blockIdx.x >> 3;
  const int sp = (xcd >> 1) * 8 + (idx & 7);
  const int fp = (xcd & 1) * 4 + (idx >> 3);
  const int g = lane >> 4;

  const unsigned short* xrow = x + (size_t)(sp * 64 + wid * 16) * D_;
  const unsigned short* w2 = win + FH_ + fp * 112 + (size_t)(lane / 14) * F_ + (lane % 14) * 8;
  const unsigned short* w1 = w2 - FH_;
  if (lane < 56) {
#pragma unroll
    for (int r = 0; r < 16; ++r) {
      __builtin_amdgcn_global_load_lds((const unsigned*)(xrow + r * D_ + ((lane ^ ((r >> 1) & 7)) * 8)),
                                       (unsigned*)(smem + (wid * 16 + r) * 896), 16, 0, 0);
    }
#pragma unroll
    for (int c = 0; c < 7; ++c)
#pragma unroll
      for (int i = 0; i < 4; ++i) {
        const int krow = c * 64 + (wid * 4 + i) * 4;
        __builtin_amdgcn_global_load_lds((const unsigned*)(w2 + (size_t)krow * F_), (unsigned*)(smem + K1_XB + krow * 224), 16, 0, 0);
      }
  }
  asm volatile("s_waitcnt vmcnt(28)" ::: "memory");
  v4u xf[14];
  {
    const int row = lane & 15;
    const char* xb = smem + (wid * 16 + row) * 896 + 8 * (g & 1);
    const int sw = (row >> 1) & 7;
#pragma unroll
    for (int ks = 0; ks < 14; ++ks) {
      const int c0 = 4 * ks + 2 * (g >> 1);
      v2u a = *(const v2u*)(xb + ((c0 ^ sw) * 16));
      v2u b = *(const v2u*)(xb + (((c0 + 1) ^ sw) * 16));
      xf[ks] = (v4u){a[0], a[1], b[0], b[1]};
    }
  }
  asm volatile("s_waitcnt vmcnt(24) lgkmcnt(0)" ::: "memory");
  __builtin_amdgcn_s_barrier();
  if (lane < 56) {
#pragma unroll
    for (int c = 0; c < 4; ++c)
#pragma unroll
      for (int i = 0; i < 4; ++i) {
        const int krow = c * 64 + (wid * 4 + i) * 4;
        __builtin_amdgcn_global_load_lds((const unsigned*)(w1 + (size_t)krow * F_), (unsigned*)(smem + krow * 224), 16, 0, 0);
      }
  }
  const int trb = (16 * (g >> 1) + 4 * (g & 1) + ((lane & 15) >> 2)) * 224 + 8 * (lane & 3);
  f32x4 acc[7];
#pragma unroll
  for (int n = 0; n < 7; ++n) acc[n] = (f32x4){0.f, 0.f, 0.f, 0.f};

#pragma unroll
  for (int c = 0; c < 7; ++c) {
    if (c > 0) {
      const int n_after = (6 - c) * 4 + 16 + 4 * ((c - 1) < 3 ? (c - 1) : 3);
      asm volatile("s_waitcnt vmcnt(%0)" ::"i"(n_after) : "memory");
      __builtin_amdgcn_s_barrier();
      if (c <= 3 && lane < 56) {
#pragma unroll
        for (int i = 0; i < 4; ++i) {
          const int krow = (3 + c) * 64 + (wid * 4 + i) * 4;
          __builtin_amdgcn_global_load_lds((const unsigned*)(w1 + (size_t)krow * F_),
                                           (unsigned*)(smem + K1_XB + (c - 1) * K1_SLOT + (wid * 4 + i) * 896), 16, 0, 0);
        }
      }
    }
    const char* sb = smem + K1_XB + c * K1_SLOT + trb;
#pragma unroll
    for (int k2 = 0; k2 < 2; ++k2) {
#pragma unroll
      for (int n = 0; n < 7; ++n) {
        s16x4 t0 = __builtin_amdgcn_ds_read_tr16_b64_v4i16((lds_s16x4_p)(sb + k2 * 7168 + n * 32));
        s16x4 t1 = __builtin_amdgcn_ds_read_tr16_b64_v4i16((lds_s16x4_p)(sb + k2 * 7168 + 1792 + n * 32));
        v2u a = __builtin_bit_cast(v2u, t0), b = __builtin_bit_cast(v2u, t1);
        v4u wf = (v4u){a[0], a[1], b[0], b[1]};
        acc[n] = mfma16(xf[2 * c + k2], wf, acc[n]);
      }
    }
  }
  {
    char* z2 = ws + WS_Z2 + ((size_t)((2 * sp + (wid >> 1)) * 56 + fp * 7) * 64 + lane) * 16 + (wid & 1) * 8;
#pragma unroll
    for (int n = 0; n < 7; ++n) {
      v2u o;
      o[0] = pack_bf16(gelu_f(acc[n][0]), gelu_f(acc[n][1]));
      o[1] = pack_bf16(gelu_f(acc[n][2]), gelu_f(acc[n][3]));
      *(v2u*)(z2 + n * 1024) = o;
      acc[n] = (f32x4){0.f, 0.f, 0.f, 0.f};
    }
  }
  asm volatile("s_waitcnt vmcnt(0)" ::: "memory");
  __builtin_amdgcn_s_barrier();
#pragma unroll
  for (int c = 0; c < 7; ++c) {
    const char* sb = smem + (c < 4 ? c * K1_SLOT : K1_XB + (c - 4) * K1_SLOT) + trb;
#pragma unroll
    for (int k2 = 0; k2 < 2; ++k2) {
#pragma unroll
      for (int n = 0; n < 7; ++n) {
        s16x4 t0 = __builtin_amdgcn_ds_read_tr16_b64_v4i16((lds_s16x4_p)(sb + k2 * 7168 + n * 32));
        s16x4 t1 = __builtin_amdgcn_ds_read_tr16_b64_v4i16((lds_s16x4_p)(sb + k2 * 7168 + 1792 + n * 32));
        v2u a = __builtin_bit_cast(v2u, t0), b = __builtin_bit_cast(v2u, t1);
        v4u wf = (v4u){a[0], a[1], b[0], b[1]};
        acc[n] = mfma16(wf, xf[2 * c + k2], acc[n]);
      }
    }
  }
  {
    char* z1 = ws + WS_Z1 + ((size_t)((4 * sp + wid) * 56 + fp * 7) * 64 + lane) * 8;
#pragma unroll
    for (int n = 0; n < 7; ++n) {
      v2u o;
      o[0] = pack_bf16(gelu_f(acc[n][0]), gelu_f(acc[n][1]));
      o[1] = pack_bf16(gelu_f(acc[n][2]), gelu_f(acc[n][3]));
      *(v2u*)(z1 + n * 512) = o;
    }
  }
}

__global__ __launch_bounds__(256) void k_ln(char* __restrict__ ws, const unsigned short* __restrict__ wout) {
  __shared__ float red[4][4][8];
  const int lane = threadIdx.x & 63, wid = threadIdx.x >> 6, g = lane >> 4;
  if (blockIdx.x >= 64) {
    for (int b = (blockIdx.x - 64) * 4 + wid; b < 784; b += (gridDim.x - 64) * 4) {
      const int kf = b / 28, dt = b % 28;
      unsigned short v[8];
#pragma unroll
      for (int e = 0; e < 8; ++e) v[e] = wout[(size_t)(32 * kf + 16 * (e >> 2) + 4 * g + (e & 3)) * D_ + 16 * dt + (lane & 15)];
      v4u o;
#pragma unroll
      for (int j = 0; j < 4; ++j) o[j] = (unsigned)v[2 * j] | ((unsigned)v[2 * j + 1] << 16);
      *(v4u*)(ws + WS_WO + ((size_t)b * 64 + lane) * 16) = o;
    }
    return;
  }
  v4u* p = (v4u*)(ws + WS_Z2) + ((size_t)blockIdx.x * 56 + wid * 14) * 64 + lane;
  v4u d[14];
#pragma unroll
  for (int i = 0; i < 14; ++i) d[i] = p[i * 64];
  float s[8];
#pragma unroll
  for (int e = 0; e < 8; ++e) s[e] = 0.f;
#pragma unroll
  for (int i = 0; i < 14; ++i)
#pragma unroll
    for (int j = 0; j < 4; ++j) { s[2 * j] += bf_lo(d[i][j]); s[2 * j + 1] += bf_hi(d[i][j]); }
#pragma unroll
  for (int e = 0; e < 8; ++e)
    for (int o = 1; o < 16; o <<= 1) s[e] += __shfl_xor(s[e], o);
  if ((lane & 15) == 0)
#pragma unroll
    for (int e = 0; e < 8; ++e) red[wid][g][e] = s[e];
  __syncthreads();
  float m[8];
#pragma unroll
  for (int e = 0; e < 8; ++e) m[e] = (red[0][g][e] + red[1][g][e] + red[2][g][e] + red[3][g][e]) * (1.0f / FH_);
  __syncthreads();
#pragma unroll
  for (int e = 0; e < 8; ++e) s[e] = 0.f;
#pragma unroll
  for (int i = 0; i < 14; ++i)
#pragma unroll
    for (int j = 0; j < 4; ++j) {
      float a = bf_lo(d[i][j]) - m[2 * j], b = bf_hi(d[i][j]) - m[2 * j + 1];
      s[2 * j] += a * a; s[2 * j + 1] += b * b;
    }
#pragma unroll
  for (int e = 0; e < 8; ++e)
    for (int o = 1; o < 16; o <<= 1) s[e] += __shfl_xor(s[e], o);
  if ((lane & 15) == 0)
#pragma unroll
    for (int e = 0; e < 8; ++e) red[wid][g][e] = s[e];
  __syncthreads();
  float r[8];
#pragma unroll
  for (int e = 0; e < 8; ++e) r[e] = rsqrtf((red[0][g][e] + red[1][g][e] + red[2][g][e] + red[3][g][e]) * (1.0f / FH_) + 1e-5f);
#pragma unroll
  for (int i = 0; i < 14; ++i) {
    v4u o;
#pragma unroll
    for (int j = 0; j < 4; ++j)
      o[j] = pack_bf16((bf_lo(d[i][j]) - m[2 * j]) * r[2 * j], (bf_hi(d[i][j]) - m[2 * j + 1]) * r[2 * j + 1]);
    p[i * 64] = o;
  }
}

#define K2_RED 32768
#define K2_LDS (K2_RED + 4 * 21 * 1024)

__global__ __launch_bounds__(256, 1) void k2_spatial(const unsigned short* __restrict__ wsp, const unsigned short* __restrict__ bsp,
                                                    char* __restrict__ ws) {
  extern __shared__ __attribute__((aligned(16))) char smem[];
  const int lane = threadIdx.x & 63;
  const int wid = __builtin_amdgcn_readfirstlane(threadIdx.x >> 6);
  const int xcd = blockIdx.x & 7, idx = blockIdx.x >> 3;
  const int tp = (xcd >> 1) * 8 + (idx & 7);
  const int fp = (xcd & 1) * 4 + (idx >> 3);
  const int g = lane >> 4;

  const v4u* ap = (const v4u*)(wsp + (size_t)(tp * 64 + (lane >> 3)) * S_ + wid * 512 + (lane & 7) * 8);
  const v4u* bp = (const v4u*)(ws + WS_Z2) + ((size_t)(wid * 16) * 56 + fp * 7) * 64 + lane;
  char* stg = smem + wid * 8192;
  const int rsw = (lane & 15) >> 1;
  const int rbase = (lane & 15) * 128 + 8 * (g & 1);

  v4u As[2][8];
  v4u Bf[2][14];
  v4u Af[2][8];
  f32x4 acc[4][7];
#pragma unroll
  for (int m = 0; m < 4; ++m)
#pragma unroll
    for (int n = 0; n < 7; ++n) acc[m][n] = (f32x4){0.f, 0.f, 0.f, 0.f};

#define LOAD_A(j) _Pragma("unroll") for (int i = 0; i < 8; ++i) As[(j) & 1][i] = ap[(size_t)i * 8 * (S_ / 8) + (j) * 8];
#define LOAD_B(j) _Pragma("unroll") for (int q = 0; q < 14; ++q) Bf[(j) & 1][q] = bp[(size_t)(2 * (j) + q / 7) * 56 * 64 + (q % 7) * 64];
#define WRITE_A(j) _Pragma("unroll") for (int i = 0; i < 8; ++i) \
    *(v4u*)(stg + (8 * i + (lane >> 3)) * 128 + (((lane & 7) ^ ((4 * i + g) & 7)) * 16)) = As[(j) & 1][i];
#define READ_A(j) _Pragma("unroll") for (int m = 0; m < 4; ++m) _Pragma("unroll") for (int ks = 0; ks < 2; ++ks) { \
    const int c0 = 4 * ks + (g >> 1); \
    v2u a = *(const v2u*)(stg + m * 2048 + rbase + ((c0 ^ rsw) * 16)); \
    v2u b = *(const v2u*)(stg + m * 2048 + rbase + (((c0 + 2) ^ rsw) * 16)); \
    Af[(j) & 1][m * 2 + ks] = (v4u){a[0], a[1], b[0], b[1]}; }

  LOAD_A(0) LOAD_B(0) LOAD_A(1) LOAD_B(1)
  SCHED();
  WRITE_A(0)
  LOAD_A(2)
  READ_A(0)
  SCHED();
#pragma unroll
  for (int j = 0; j < 8; ++j) {
    if (j + 1 < 8) {
      WRITE_A(j + 1)
      if (j + 3 < 8) { LOAD_A(j + 3) }
      READ_A(j + 1)
    }
    SCHED();
#pragma unroll
    for (int ks = 0; ks < 2; ++ks) {
#pragma unroll
      for (int n = 0; n < 7; ++n) {
#pragma unroll
        for (int m = 0; m < 4; ++m) acc[m][n] = mfma16(Bf[j & 1][ks * 7 + n], Af[j & 1][m * 2 + ks], acc[m][n]);
        if (j + 2 < 8) Bf[j & 1][ks * 7 + n] = bp[(size_t)(2 * (j + 2) + ks) * 56 * 64 + n * 64];
      }
    }
    SCHED();
  }
  v2u z1v[7];
  {
    const v2u* z1 = (const v2u*)(ws + WS_Z1) + ((size_t)((4 * tp + wid) * 56 + fp * 7)) * 64 + lane;
#pragma unroll
    for (int n = 0; n < 7; ++n) z1v[n] = z1[n * 64];
  }
  const float bias = __builtin_bit_cast(float, (unsigned)bsp[tp * 64 + wid * 16 + (lane & 15)] << 16);
  char* red = smem + K2_RED;
#pragma unroll
  for (int dm = 1; dm < 4; ++dm) {
#pragma unroll
    for (int m = 0; m < 4; ++m) {
      if (m == ((wid + dm) & 3)) {
#pragma unroll
        for (int n = 0; n < 7; ++n) *(f32x4*)(red + ((m * 3 + dm - 1) * 7 + n) * 1024 + lane * 16) = acc[m][n];
      }
    }
  }
  __syncthreads();
  f32x4 o[7];
#pragma unroll
  for (int m = 0; m < 4; ++m)
    if (m == wid) {
#pragma unroll
      for (int n = 0; n < 7; ++n) o[n] = acc[m][n];
    }
#pragma unroll
  for (int dm = 0; dm < 3; ++dm)
#pragma unroll
    for (int n = 0; n < 7; ++n) o[n] += *(const f32x4*)(red + ((wid * 3 + dm) * 7 + n) * 1024 + lane * 16);
  char* hp = ws + WS_H + ((size_t)(4 * tp + wid) * 28 * 64 + lane) * 16;
#pragma unroll
  for (int n = 0; n < 7; ++n) {
    const int ft = fp * 7 + n;
    v2u h;
    h[0] = pack_bf16(bf_lo(z1v[n][0]) * (o[n][0] + bias), bf_hi(z1v[n][0]) * (o[n][1] + bias));
    h[1] = pack_bf16(bf_lo(z1v[n][1]) * (o[n][2] + bias), bf_hi(z1v[n][1]) * (o[n][3] + bias));
    *(v2u*)(hp + (ft >> 1) * 1024 + (ft & 1) * 8) = h;
  }
}

#define K3_LDS (4 * 12 * 1024)
__global__ __launch_bounds__(256, 1) void k3_out(const char* __restrict__ ws, unsigned short* __restrict__ out) {
  extern __shared__ __attribute__((aligned(16))) char smem[];
  const int lane = threadIdx.x & 63;
  const int wid = __builtin_amdgcn_readfirstlane(threadIdx.x >> 6);
  const int xcd = blockIdx.x & 7, idx = blockIdx.x >> 3;
  const int tp = xcd * 4 + (idx & 3);
  const int dp = idx >> 2;
  const int g = lane >> 4;
  const v4u* hp = (const v4u*)(ws + WS_H) + ((size_t)(4 * tp) * 28 + wid * 7) * 64 + lane;
  const v4u* wp = (const v4u*)(ws + WS_WO) + ((size_t)(wid * 7) * 28 + dp * 4) * 64 + lane;
  v4u Hf[7][4], Wf[7][4];
#pragma unroll
  for (int k = 0; k < 7; ++k) {
#pragma unroll
    for (int n = 0; n < 4; ++n) Wf[k][n] = wp[(k * 28 + n) * 64];
#pragma unroll
    for (int m = 0; m < 4; ++m) Hf[k][m] = hp[(m * 28 + k) * 64];
  }
  SCHED();
  f32x4 acc[4][4];
#pragma unroll
  for (int m = 0; m < 4; ++m)
#pragma unroll
    for (int n = 0; n < 4; ++n) acc[m][n] = (f32x4){0.f, 0.f, 0.f, 0.f};
#pragma unroll
  for (int k = 0; k < 7; ++k)
#pragma unroll
    for (int m = 0; m < 4; ++m)
#pragma unroll
      for (int n = 0; n < 4; ++n) acc[m][n] = mfma16(Wf[k][n], Hf[k][m], acc[m][n]);
#pragma unroll
  for (int dm = 1; dm < 4; ++dm)
#pragma unroll
    for (int m = 0; m < 4; ++m)
      if (m == ((wid + dm) & 3)) {
#pragma unroll
        for (int n = 0; n < 4; ++n) *(f32x4*)(smem + ((m * 3 + dm - 1) * 4 + n) * 1024 + lane * 16) = acc[m][n];
      }
  __syncthreads();
  f32x4 o[4];
#pragma unroll
  for (int m = 0; m < 4; ++m)
    if (m == wid) {
#pragma unroll
      for (int n = 0; n < 4; ++n) o[n] = acc[m][n];
    }
#pragma unroll
  for (int dm = 0; dm < 3; ++dm)
#pragma unroll
    for (int n = 0; n < 4; ++n) o[n] += *(const f32x4*)(smem + ((wid * 3 + dm) * 4 + n) * 1024 + lane * 16);
  unsigned short* op = out + (size_t)(tp * 64 + wid * 16 + (lane & 15)) * D_ + dp * 64 + 4 * g;
#pragma unroll
  for (int n = 0; n < 4; ++n) {
    v2u h;
    h[0] = pack_bf16(o[n][0], o[n][1]);
    h[1] = pack_bf16(o[n][2], o[n][3]);
    *(v2u*)(op + n * 16) = h;
  }
}

extern "C" void kernel_launch(void* const* d_in, const int* in_sizes, int n_in,
                              void* d_out, int out_size, void* d_ws, size_t ws_size,
                              hipStream_t stream) {
  const unsigned short* x = (const unsigned short*)d_in[0];
  const unsigned short* win = (const unsigned short*)d_in[1];
  const unsigned short* wsp = (const unsigned short*)d_in[2];
  const unsigned short* bsp = (const unsigned short*)d_in[3];
  const unsigned short* wout = (const unsigned short*)d_in[4];
  char* ws = (char*)d_ws;
  (void)hipFuncSetAttribute((const void*)k1_gemm_gelu, hipFuncAttributeMaxDynamicSharedMemorySize, K1_LDS);
  (void)hipFuncSetAttribute((const void*)k2_spatial, hipFuncAttributeMaxDynamicSharedMemorySize, K2_LDS);
  (void)hipFuncSetAttribute((const void*)k3_out, hipFuncAttributeMaxDynamicSharedMemorySize, K3_LDS);
  k1_gemm_gelu<<<256, 256, K1_LDS, stream>>>(x, win, ws);
  k_ln<<<64 + 64, 256, 0, stream>>>(ws, wout);
  k2_spatial<<<256, 256, K2_LDS, stream>>>(wsp, bsp, ws);
  k3_out<<<224, 256, K3_LDS, stream>>>(ws, (unsigned short*)d_out);
}
```

```cpp
#include <hip/hip_runtime.h>
#include <hip/hip_bf16.h>
#include <stdint.h>

typedef __attribute__((ext_vector_type(8))) short bf16x8;
typedef __attribute__((ext_vector_type(4))) short s16x4;
typedef __attribute__((ext_vector_type(4))) float f32x4;
typedef __attribute__((ext_vector_type(4))) unsigned v4u;
typedef __attribute__((ext_vector_type(2))) unsigned v2u;
typedef __attribute__((address_space(3))) s16x4* lds_s16x4_p;

#define S_ 2048
#define D_ 448
#define F_ 1792
#define FH_ 896

#define WS_Z1 0
#define WS_Z2 (WS_Z1 + 3670016)
#define WS_H  (WS_Z2 + 3670016)
#define WS_WO (WS_H + 3670016)

#define SCHED() __builtin_amdgcn_sched_barrier(0)
#define STAMP_DECL
#define STAMP()
#define STAMP_OUT(kid)

__device__ __forceinline__ f32x4 mfma16(v4u a, v4u b, f32x4 c) {
  return __builtin_amdgcn_mfma_f32_16x16x32_bf16(__builtin_bit_cast(bf16x8, a), __builtin_bit_cast(bf16x8, b), c, 0, 0, 0);
}
typedef __attribute__((ext_vector_type(2))) float f32x2;
typedef __attribute__((ext_vector_type(2))) __bf16 bf16x2;
__device__ __forceinline__ unsigned pack_bf16(float lo, float hi) {
  const bf16x2 r = __builtin_convertvector((f32x2){lo, hi}, bf16x2);
  return __builtin_bit_cast(unsigned, r);
}
__device__ __forceinline__ float asf(unsigned u) { return __builtin_bit_cast(float, u); }
__device__ __forceinline__ float bf_lo(unsigned u) { return __builtin_bit_cast(float, u << 16); }
__device__ __forceinline__ float bf_hi(unsigned u) { return __builtin_bit_cast(float, u & 0xffff0000u); }

__device__ __forceinline__ void glds16(unsigned voff, const void* sbase, unsigned lds) {
  asm volatile("s_mov_b32 m0, %2\n\ts_nop 0\n\tglobal_load_lds_dwordx4 %0, %1" ::"v"(voff), "s"(sbase), "s"(lds) : "memory");
}
template <int OFF>
__device__ __forceinline__ void glds16o(unsigned voff, const void* sbase, unsigned lds) {
  asm volatile("s_mov_b32 m0, %2\n\ts_nop 0\n\tglobal_load_lds_dwordx4 %0, %1 offset:%3" ::"v"(voff), "s"(sbase), "s"(lds - OFF), "i"(OFF) : "memory");
}
__device__ __forceinline__ unsigned lds_addr(const void* p) { return (unsigned)(size_t)(__attribute__((address_space(3))) const char*)p; }

__device__ __forceinline__ void st_plain8(v2u d, void* p) { asm volatile("global_store_dwordx2 %0, %1, off" ::"v"(p), "v"(d) : "memory"); }

__device__ __forceinline__ float gelu_f(float x) {
  const float c0 = -2.0f * 0.7978845608028654f * 1.4426950408889634f;
  const float c1 = c0 * 0.044715f;
  float t = __builtin_fmaf(x * x, c1, c0);
  float e = __builtin_amdgcn_exp2f(x * t);
  return x * __builtin_amdgcn_rcpf(1.0f + e);
}

#define K1_SLOT 32768
#define K1_LDS (5 * K1_SLOT)
#define WAITBAR(N) asm volatile("s_waitcnt vmcnt(" #N ") lgkmcnt(0)\n\ts_barrier" ::: "memory")
#define WS_EX (WS_WO + 802816)
#define EX_CH 262144
#define EX_GRP (EX_CH + 2048)

__device__ __forceinline__ void ld16_sc1(v4u& d, unsigned voff, const void* sb) { asm volatile("global_load_dwordx4 %0, %1, %2 sc1" : "=v"(d) : "v"(voff), "s"(sb) : "memory"); }
__device__ __forceinline__ void ld8_sc1(v2u& d, unsigned voff, const void* sb) { asm volatile("global_load_dwordx2 %0, %1, %2 sc1" : "=v"(d) : "v"(voff), "s"(sb) : "memory"); }
__device__ __forceinline__ void ld4_sc1(unsigned& d, unsigned voff, const void* sb) { asm volatile("global_load_dword %0, %1, %2 sc1" : "=v"(d) : "v"(voff), "s"(sb) : "memory"); }
__device__ __forceinline__ void st16_sc1(v4u d, unsigned voff, const void* sb) { asm volatile("global_store_dwordx4 %0, %1, %2 sc1\n\ts_nop 1" ::"v"(voff), "v"(d), "s"(sb) : "memory"); }
__device__ __forceinline__ void st8_sc1(v2u d, unsigned voff, const void* sb) { asm volatile("global_store_dwordx2 %0, %1, %2 sc1" ::"v"(voff), "v"(d), "s"(sb) : "memory"); }
__device__ __forceinline__ void st4_sc1(unsigned d, unsigned voff, const void* sb) { asm volatile("global_store_dword %0, %1, %2 sc1" ::"v"(voff), "v"(d), "s"(sb) : "memory"); }
template <int CTRL>
__device__ __forceinline__ float dpp_f(float v) {
  return __builtin_bit_cast(float, __builtin_amdgcn_update_dpp(0, __builtin_bit_cast(int, v), CTRL, 0xf, 0xf, true));
}
__device__ __forceinline__ float row16_sum(float v) {
  v += dpp_f<0x128>(v); v += dpp_f<0x124>(v); v += dpp_f<0x122>(v); v += dpp_f<0x121>(v);
  return v;
}

__global__ __launch_bounds__(512, 2) void k1_gemm_gelu(const unsigned short* __restrict__ x, const unsigned short* __restrict__ win,
                                                      const unsigned short* __restrict__ wsp, const unsigned short* __restrict__ wout,
                                                      char* __restrict__ ws) {
  extern __shared__ __attribute__((aligned(16))) char smem[];
  const int lane = threadIdx.x & 63;
  const int wid = __builtin_amdgcn_readfirstlane(threadIdx.x >> 6);
  const int xcd = blockIdx.x & 7, idx = blockIdx.x >> 3;
  const int g = lane >> 4, j = lane & 15;
  if (idx >= 28) {
    const char* pb = (const char*)wsp + ((size_t)((xcd >> 1) * 512 + (idx - 28) * 128 + wid * 16) * S_) * 2;
    unsigned tdst = 0;
#pragma unroll
    for (int i = 0; i < 16; ++i)
      asm volatile("global_load_dword %0, %1, %2" : "+v"(tdst) : "v"(lane * 64), "s"(pb + i * 4096) : "memory");
    const int hw = ((idx - 28) * 8 + xcd) * 8 + wid;
    unsigned short v[4][8];
#pragma unroll
    for (int i = 0; i < 4; ++i) {
      const int b = min(hw + 256 * i, 783), kf = b / 28, dt = b % 28;
#pragma unroll
      for (int e = 0; e < 8; ++e) v[i][e] = wout[(size_t)(32 * kf + 8 * g + e) * D_ + 16 * dt + j];
    }
#pragma unroll
    for (int i = 0; i < 4; ++i) {
      const int b = min(hw + 256 * i, 783);
      v4u o;
#pragma unroll
      for (int q = 0; q < 4; ++q) o[q] = (unsigned)v[i][2 * q] | ((unsigned)v[i][2 * q + 1] << 16);
      *(v4u*)(ws + WS_WO + ((size_t)b * 64 + lane) * 16) = o;
    }
    asm volatile("s_waitcnt vmcnt(0)" : "+v"(tdst)::"memory");
    return;
  }
  const int rp = (xcd >> 1) * 4 + (idx & 3);
  const int cp = (xcd & 1) * 7 + (idx >> 2);
  const int half = wid >> 2, mp = wid & 3;

  STAMP_DECL STAMP();
  const char* ex = ws + WS_EX + (size_t)(rp * 4 + mp) * EX_GRP;
  const unsigned in_vo = (cp * 16 + (j >> 2)) * 1024 + (4 * g + (j & 3)) * 16;
  const unsigned pin_vo = (cp * 16 + j) * 1024 + 512;
  v4u in[8]; v2u pin;
#define K1_LDIN() { _Pragma("unroll") for (int k = 0; k < 4; ++k) { ld16_sc1(in[2 * k], in_vo, ex + k * 4096); ld16_sc1(in[2 * k + 1], in_vo, ex + k * 4096 + 256); } \
    ld8_sc1(pin, pin_vo, ex); }
#define K1_INREGS "+v"(in[0]), "+v"(in[1]), "+v"(in[2]), "+v"(in[3]), "+v"(in[4]), "+v"(in[5]), "+v"(in[6]), "+v"(in[7]), "+v"(pin)

  const unsigned sm0 = lds_addr(smem);
  const char* xb = (const char*)(x + (size_t)(rp * 128 + wid * 16) * D_);
  const unsigned xvo0 = (lane >> 3) * (D_ * 2) + (((lane & 7) ^ g) * 16);
  const unsigned xvo1 = (lane >> 3) * (D_ * 2) + (((lane & 7) ^ (g + 4)) * 16) + 8 * D_ * 2;
  const char* w1 = (const char*)(win + cp * 64 + (size_t)(wid * 8) * F_);
  const char* w2 = w1 + FH_ * 2;
  const unsigned wvo = (lane >> 3) * (F_ * 2) + (((lane & 7) ^ (2 * ((g & 1) + 2 * (wid & 1)))) * 16);
#define K1_C(c) { const unsigned d_ = sm0 + ((c) % 5) * K1_SLOT; \
    glds16(xvo0, xb + (c) * 128, d_ + wid * 2048); glds16(xvo1, xb + (c) * 128, d_ + wid * 2048 + 1024); \
    glds16(wvo, w2 + (size_t)((c) * 64) * (F_ * 2), d_ + 16384 + wid * 1024); \
    glds16(wvo, w1 + (size_t)((c) * 64) * (F_ * 2), d_ + 24576 + wid * 1024); }
  K1_C(0) K1_C(1) K1_C(2)
  STAMP();
  const int xrd0 = mp * 4096 + j * 128 + ((g ^ (j >> 1)) * 16), xrd1 = xrd0 ^ 64;
  const int fsw = 2 * (((j >> 3) & 1) + 2 * (g & 1));
  const int tra = 16384 + half * 8192 + g * 1024 + (j >> 2) * 128 + (((j >> 1) & 1) * 16) + 8 * (j & 1);
  v4u xf[2][4];
  v4u wf[2][8];
  f32x4 acc[2][4];
#pragma unroll
  for (int n = 0; n < 4; ++n) { acc[0][n] = (f32x4){0.f, 0.f, 0.f, 0.f}; acc[1][n] = (f32x4){0.f, 0.f, 0.f, 0.f}; }
#define K1_STEP(c, RD, MM, ZT) { const char* s_ = smem + ((c) % 5) * K1_SLOT; \
    _Pragma("unroll") for (int i = 0; i < 8; ++i) { \
      const int k2 = i >> 2, n = i & 3; \
      if (RD) { if (i < 4) xf[(c) & 1][i] = *(const v4u*)(s_ + ((i & 1) ? xrd1 : xrd0) + (i >> 1) * 2048); \
        const char* sb = s_ + tra + (((2 * n) ^ fsw) * 16) + k2 * 4096; \
        s16x4 t0 = __builtin_amdgcn_ds_read_tr16_b64_v4i16((lds_s16x4_p)(sb)); \
        s16x4 t1 = __builtin_amdgcn_ds_read_tr16_b64_v4i16((lds_s16x4_p)(sb + 512)); \
        v2u a_ = __builtin_bit_cast(v2u, t0), b_ = __builtin_bit_cast(v2u, t1); \
        wf[(c) & 1][i] = (v4u){a_[0], a_[1], b_[0], b_[1]}; } \
      if (MM) { _Pragma("unroll") for (int mt = 0; mt < 2; ++mt) \
          acc[mt][n] = ZT ? mfma16(xf[((c) - 1) & 1][mt * 2 + k2], wf[((c) - 1) & 1][i], acc[mt][n])        \
                          : mfma16(wf[((c) - 1) & 1][i], xf[((c) - 1) & 1][mt * 2 + k2], acc[mt][n]); }     \
      SCHED(); } }

  if (half) {
    WAITBAR(8);
    K1_C(3) K1_STEP(0, 1, 0, 0)
    WAITBAR(8);
    K1_C(4) K1_STEP(1, 1, 1, 0)
    WAITBAR(8);
    K1_C(5) K1_STEP(2, 1, 1, 0)
    WAITBAR(8);
    K1_C(6) K1_STEP(3, 1, 1, 0)
    WAITBAR(8);
    K1_STEP(4, 1, 1, 0)
    WAITBAR(4);
    K1_STEP(5, 1, 1, 0)
    WAITBAR(0);
    K1_STEP(6, 1, 1, 0)
    K1_STEP(7, 0, 1, 0)
    char* z1 = ws + WS_Z1 + ((size_t)((8 * rp + 2 * mp) * 56 + cp * 4) * 64 + lane) * 8;
#pragma unroll
    for (int mt = 0; mt < 2; ++mt)
#pragma unroll
      for (int n = 0; n < 4; ++n) {
        v2u o;
        o[0] = pack_bf16(gelu_f(acc[mt][n][0]), gelu_f(acc[mt][n][1]));
        o[1] = pack_bf16(gelu_f(acc[mt][n][2]), gelu_f(acc[mt][n][3]));
        *(v2u*)(z1 + mt * 56 * 512 + n * 512) = o;
      }
    return;
  }
  K1_LDIN()
  WAITBAR(17);
  STAMP();
  K1_C(3) K1_STEP(0, 1, 0, 1)
  WAITBAR(17);
  K1_C(4) K1_STEP(1, 1, 1, 1)
  WAITBAR(17);
  K1_C(5) K1_STEP(2, 1, 1, 1)
  asm volatile("s_waitcnt vmcnt(8) lgkmcnt(0)\n\ts_barrier" : K1_INREGS::"memory");
  unsigned nonce = 1;
  for (;;) {
    bool hit = pin[0] == nonce;
#pragma unroll
    for (int k = 0; k < 8; ++k) hit = hit || in[k][0] == nonce || in[k][2] == nonce;
    if (!__any(hit)) break;
    ++nonce;
  }
  if (lane == 0) st4_sc1(nonce, EX_CH + cp * 128, ex);
  K1_C(6) K1_STEP(3, 1, 1, 1)
  WAITBAR(9);
  K1_STEP(4, 1, 1, 1)
  WAITBAR(5);
  K1_STEP(5, 1, 1, 1)
  WAITBAR(0);
  __builtin_amdgcn_s_setprio(2);
  unsigned chal;
  ld4_sc1(chal, EX_CH + j * 128, ex);
  K1_STEP(6, 1, 1, 1)
  K1_STEP(7, 0, 1, 1)
  STAMP();
  float v[2][4][4];
  unsigned my_m[2], my_q[2];
  const int rs = j & 3;
#pragma unroll
  for (int mt = 0; mt < 2; ++mt) {
    float lm[4], lq[4];
#pragma unroll
    for (int n = 0; n < 4; ++n)
#pragma unroll
      for (int r = 0; r < 4; ++r) v[mt][n][r] = gelu_f(acc[mt][n][r]);
#pragma unroll
    for (int r = 0; r < 4; ++r) {
      lm[r] = row16_sum((v[mt][0][r] + v[mt][1][r]) + (v[mt][2][r] + v[mt][3][r])) * (1.0f / 64.0f);
      const float d0 = v[mt][0][r] - lm[r], d1 = v[mt][1][r] - lm[r], d2 = v[mt][2][r] - lm[r], d3 = v[mt][3][r] - lm[r];
      lq[r] = row16_sum((d0 * d0 + d1 * d1) + (d2 * d2 + d3 * d3));
    }
    const float a01 = (rs & 1) ? lm[1] : lm[0], a23 = (rs & 1) ? lm[3] : lm[2];
    const float b01 = (rs & 1) ? lq[1] : lq[0], b23 = (rs & 1) ? lq[3] : lq[2];
    my_m[mt] = __builtin_bit_cast(unsigned, (rs & 2) ? a23 : a01);
    my_q[mt] = __builtin_bit_cast(unsigned, (rs & 2) ? b23 : b01);
  }
  const unsigned out_vo = ((j >> 2) * 16 + cp) * 1024 + (4 * g + rs) * 16;
  const unsigned pout_vo = (j * 16 + cp) * 1024 + 512;
#define K1_SEND(ans) { \
    _Pragma("unroll") for (int k = 0; k < 4; ++k) { const unsigned t_ = __shfl(ans, 4 * k + (j >> 2)); \
      st16_sc1((v4u){t_, my_m[0], t_, my_q[0]}, out_vo, ex + k * 65536); \
      st16_sc1((v4u){t_, my_m[1], t_, my_q[1]}, out_vo, ex + k * 65536 + 256); } \
    if (lane < 16) st8_sc1((v2u){ans, nonce}, pout_vo, ex); }
  asm volatile("s_waitcnt vmcnt(0)" : "+v"(chal)::"memory");
  unsigned answered = chal;
  K1_SEND(answered)
  STAMP();
  for (unsigned spin = 0; spin < (1u << 16); ++spin) {
    K1_LDIN()
    ld4_sc1(chal, EX_CH + j * 128, ex);
    asm volatile("s_waitcnt vmcnt(0)" : K1_INREGS, "+v"(chal)::"memory");
    bool okd = true;
#pragma unroll
    for (int k = 0; k < 6; ++k) okd = okd && in[k][0] == nonce && in[k][2] == nonce;
    okd = okd && (j >= 8 || (in[6][0] == nonce && in[6][2] == nonce && in[7][0] == nonce && in[7][2] == nonce));
    const bool pv = pin[0] == nonce;
    const unsigned cur = pv ? pin[1] : chal;
    const bool live = j < 14;
    if (__any(live && cur != answered)) { answered = cur; K1_SEND(answered) }
    if (__all(okd && (pv || !live))) break;
  }
  STAMP();
#pragma unroll
  for (int mt = 0; mt < 2; ++mt) {
    const float w3 = j < 8 ? 1.f : 0.f;
    const float m0 = asf(in[mt][1]), m1 = asf(in[2 + mt][1]), m2 = asf(in[4 + mt][1]);
    const float m3 = j < 8 ? asf(in[6 + mt][1]) : 0.f;
    float sm = (m0 + m1) + (m2 + m3);
    sm += dpp_f<0x128>(sm); sm += dpp_f<0x124>(sm);
    const float mean = sm * (1.0f / 14.0f);
    const float q3 = j < 8 ? asf(in[6 + mt][3]) : 0.f;
    const float e0 = m0 - mean, e1 = m1 - mean, e2 = m2 - mean, e3 = (m3 - mean) * w3;
    float sq = (asf(in[mt][3]) + asf(in[2 + mt][3])) + (asf(in[4 + mt][3]) + q3) + 64.0f * ((e0 * e0 + e1 * e1) + (e2 * e2 + e3 * e3));
    sq += dpp_f<0x128>(sq); sq += dpp_f<0x124>(sq);
    const float rstd = rsqrtf(sq * (1.0f / FH_) + 1e-5f);
    float mr[4], rr[4];
    mr[0] = dpp_f<0x00>(mean); mr[1] = dpp_f<0x55>(mean); mr[2] = dpp_f<0xAA>(mean); mr[3] = dpp_f<0xFF>(mean);
    rr[0] = dpp_f<0x00>(rstd); rr[1] = dpp_f<0x55>(rstd); rr[2] = dpp_f<0xAA>(rstd); rr[3] = dpp_f<0xFF>(rstd);
    char* z2 = ws + WS_Z2 + ((size_t)((4 * rp + mp) * 56 + cp * 4) * 64 + j + 16 * (2 * mt + (g >> 1))) * 16 + (g & 1) * 8;
#pragma unroll
    for (int n = 0; n < 4; ++n) {
      v2u o;
      o[0] = pack_bf16((v[mt][n][0] - mr[0]) * rr[0], (v[mt][n][1] - mr[1]) * rr[1]);
      o[1] = pack_bf16((v[mt][n][2] - mr[2]) * rr[2], (v[mt][n][3] - mr[3]) * rr[3]);
      *(v2u*)(z2 + n * 1024) = o;
    }
  }
  STAMP(); STAMP_OUT(1);
}

#define K2_LDS (4 * 32768 + 1024)

template <int OFF>
__device__ __forceinline__ void gload_a(v4u& dst, unsigned voff, const void* sbase) {
  asm volatile("global_load_dwordx4 %0, %1, %2 offset:%3" : "=a"(dst) : "v"(voff), "s"(sbase), "i"(OFF) : "memory");
}

__global__ __launch_bounds__(256, 1) void k2_spatial(const unsigned short* __restrict__ wsp, const unsigned short* __restrict__ bsp,
                                                    char* __restrict__ ws) {
  extern __shared__ __attribute__((aligned(16))) char smem[];
  const int lane = threadIdx.x & 63;
  const int wid = __builtin_amdgcn_readfirstlane(threadIdx.x >> 6);
  const int xcd = blockIdx.x & 7, idx = blockIdx.x >> 3;
  const int tp = (xcd >> 1) * 8 + (idx & 7);
  const int fp = (xcd & 1) * 4 + (idx >> 3);
  const int g = lane >> 4;

  STAMP_DECL STAMP();
  v2u z1v[7];
  {
    const v2u* z1 = (const v2u*)(ws + WS_Z1) + ((size_t)((4 * tp + wid) * 56 + fp * 7)) * 64 + lane;
#pragma unroll
    for (int n = 0; n < 7; ++n) z1v[n] = z1[n * 64];
  }
  const float bias = __builtin_bit_cast(float, (unsigned)bsp[tp * 64 + wid * 16 + (lane & 15)] << 16);

  const char* abase = (const char*)(wsp + (size_t)(tp * 64) * S_ + wid * 512);
  const unsigned avo0 = (lane >> 3) * (S_ * 2) + (((lane & 7) ^ g) * 16);
  const unsigned avo1 = (lane >> 3) * (S_ * 2) + (((lane & 7) ^ (g + 4)) * 16);
  const unsigned ring = lds_addr(smem) + 1024 + wid * 32768;
  const char* stg = smem + 1024 + wid * 32768;
  const char* bbase = ws + WS_Z2 + ((size_t)(wid * 16) * 56 + fp * 7 + 3) * 1024;
  const unsigned bvo = lane * 16;
  const int rd0 = (lane & 15) * 128 + ((g ^ ((lane & 15) >> 1)) * 16);
  const int rd1 = rd0 ^ 64;

  v4u Bf[2][14];
  v4u Af[2][8];
  f32x4 acc[4][7];
#pragma unroll
  for (int m = 0; m < 4; ++m)
#pragma unroll
    for (int n = 0; n < 7; ++n) acc[m][n] = (f32x4){0.f, 0.f, 0.f, 0.f};

#define LOAD_A1(j, i) glds16o<(j) * 128>(((i) & 1) ? avo1 : avo0, abase + (size_t)(i) * 8 * (S_ * 2), ring + ((j) & 3) * 8192 + (i) * 1024);
#define LOAD_A(j) LOAD_A1(j, 0) LOAD_A1(j, 1) LOAD_A1(j, 2) LOAD_A1(j, 3) LOAD_A1(j, 4) LOAD_A1(j, 5) LOAD_A1(j, 6) LOAD_A1(j, 7)
#define LOAD_B1(j, ks, n) gload_a<((n) - 3) * 1024>(Bf[(j) & 1][(ks) * 7 + (n)], bvo, bbase + (size_t)(2 * (j) + (ks)) * 56 * 1024);
#define LOAD_Bk(j, ks) LOAD_B1(j, ks, 0) LOAD_B1(j, ks, 1) LOAD_B1(j, ks, 2) LOAD_B1(j, ks, 3) LOAD_B1(j, ks, 4) LOAD_B1(j, ks, 5) LOAD_B1(j, ks, 6)
#define LOAD_B(j) LOAD_Bk(j, 0) LOAD_Bk(j, 1)
#define READ_A(j) _Pragma("unroll") for (int m = 0; m < 4; ++m) _Pragma("unroll") for (int ks = 0; ks < 2; ++ks) \
    Af[(j) & 1][m * 2 + ks] = *(const v4u*)(stg + ((j) & 3) * 8192 + m * 2048 + (ks ? rd1 : rd0));
#define WAIT_B(N, P) asm volatile("s_waitcnt vmcnt(" #N ")" : "+a"(Bf[P][0]), "+a"(Bf[P][1]), "+a"(Bf[P][2]), "+a"(Bf[P][3]), "+a"(Bf[P][4]), \
    "+a"(Bf[P][5]), "+a"(Bf[P][6]), "+a"(Bf[P][7]), "+a"(Bf[P][8]), "+a"(Bf[P][9]), "+a"(Bf[P][10]), "+a"(Bf[P][11]), "+a"(Bf[P][12]), "+a"(Bf[P][13]) :: "memory");
#define MMA(j, ks, n) _Pragma("unroll") for (int m = 0; m < 4; ++m) acc[m][n] = mfma16(Bf[(j) & 1][(ks) * 7 + (n)], Af[(j) & 1][m * 2 + (ks)], acc[m][n]);
#define STEP_BODY(j, LB, LA) \
  MMA(j, 0, 0) SCHED(); if (LB) { LOAD_B1((j) + 2, 0, 0) } if (LA) { LOAD_A1((j) + 3, 0) } \
  MMA(j, 0, 1) SCHED(); if (LB) { LOAD_B1((j) + 2, 0, 1) } if (LA) { LOAD_A1((j) + 3, 1) } \
  MMA(j, 0, 2) SCHED(); if (LB) { LOAD_B1((j) + 2, 0, 2) } if (LA) { LOAD_A1((j) + 3, 2) } \
  MMA(j, 0, 3) SCHED(); if (LB) { LOAD_B1((j) + 2, 0, 3) } if (LA) { LOAD_A1((j) + 3, 3) } \
  MMA(j, 0, 4) SCHED(); if (LB) { LOAD_B1((j) + 2, 0, 4) } if (LA) { LOAD_A1((j) + 3, 4) } \
  MMA(j, 0, 5) SCHED(); if (LB) { LOAD_B1((j) + 2, 0, 5) } if (LA) { LOAD_A1((j) + 3, 5) } \
  MMA(j, 0, 6) SCHED(); if (LB) { LOAD_B1((j) + 2, 0, 6) } if (LA) { LOAD_A1((j) + 3, 6) } \
  MMA(j, 1, 0) SCHED(); if (LB) { LOAD_B1((j) + 2, 1, 0) } if (LA) { LOAD_A1((j) + 3, 7) } \
  MMA(j, 1, 1) SCHED(); if (LB) { LOAD_B1((j) + 2, 1, 1) } \
  MMA(j, 1, 2) SCHED(); if (LB) { LOAD_B1((j) + 2, 1, 2) } \
  MMA(j, 1, 3) SCHED(); if (LB) { LOAD_B1((j) + 2, 1, 3) } \
  MMA(j, 1, 4) SCHED(); if (LB) { LOAD_B1((j) + 2, 1, 4) } \
  MMA(j, 1, 5) SCHED(); if (LB) { LOAD_B1((j) + 2, 1, 5) } \
  MMA(j, 1, 6) SCHED(); if (LB) { LOAD_B1((j) + 2, 1, 6) } \
  SCHED();

  LOAD_A(0) LOAD_B(0) LOAD_A(1)
  LOAD_B(1) LOAD_A(2)
  STAMP();
  asm volatile("s_waitcnt vmcnt(44)" ::: "memory");
  STAMP();
  READ_A(0)
  SCHED();
  WAIT_B(22, 0) STAMP(); READ_A(1) SCHED(); STEP_BODY(0, 1, 1)
  WAIT_B(22, 1) STAMP(); READ_A(2) SCHED(); STEP_BODY(1, 1, 1)
  WAIT_B(22, 0) STAMP(); READ_A(3) SCHED(); STEP_BODY(2, 1, 1)
  WAIT_B(22, 1) STAMP(); READ_A(4) SCHED(); STEP_BODY(3, 1, 1)
  WAIT_B(22, 0) STAMP(); READ_A(5) SCHED(); STEP_BODY(4, 1, 1)
  WAIT_B(22, 1) STAMP(); READ_A(6) SCHED(); STEP_BODY(5, 1, 0)
  WAIT_B(14, 0) STAMP(); READ_A(7) SCHED(); STEP_BODY(6, 0, 0)
  WAIT_B(0, 1) STAMP(); SCHED(); STEP_BODY(7, 0, 0) STAMP();
  {
    char* mine = smem + 1024 + wid * 32768 + lane * 16;
#pragma unroll
    for (int m = 0; m < 4; ++m)
      if (m != wid) {
#pragma unroll
        for (int n = 0; n < 7; ++n) *(f32x4*)(mine + (m * 7 + n) * 1024) = acc[m][n];
      }
  }
  f32x4 o[7];
#pragma unroll
  for (int m = 0; m < 4; ++m)
    if (m == wid) {
#pragma unroll
      for (int n = 0; n < 7; ++n) o[n] = acc[m][n];
    }
  __syncthreads();
  STAMP();
  {
    f32x4 pr[3][7];
#pragma unroll
    for (int d = 0; d < 3; ++d) {
      const char* src = smem + 1024 + ((wid + 1 + d) & 3) * 32768 + wid * 7168 + lane * 16;
#pragma unroll
      for (int n = 0; n < 7; ++n) pr[d][n] = *(const f32x4*)(src + n * 1024);
    }
    SCHED();
#pragma unroll
    for (int n = 0; n < 7; ++n) o[n] += (pr[0][n] + pr[1][n]) + pr[2][n];
  }
  char* hp = ws + WS_H + ((size_t)(4 * tp + wid) * 28 * 64 + (lane & 15) + 16 * (g >> 1)) * 16 + (g & 1) * 8;
#pragma unroll
  for (int n = 0; n < 7; ++n) {
    const int ft = fp * 7 + n;
    v2u h;
    h[0] = pack_bf16(bf_lo(z1v[n][0]) * (o[n][0] + bias), bf_hi(z1v[n][0]) * (o[n][1] + bias));
    h[1] = pack_bf16(bf_lo(z1v[n][1]) * (o[n][2] + bias), bf_hi(z1v[n][1]) * (o[n][3] + bias));
    *(v2u*)(hp + (ft >> 1) * 1024 + (ft & 1) * 512) = h;
  }
  STAMP(); STAMP_OUT(2);
}

#define K3_LDS (4 * 12 * 1024)
__global__ __launch_bounds__(256, 1) void k3_out(const char* __restrict__ ws, unsigned short* __restrict__ out) {
  extern __shared__ __attribute__((aligned(16))) char smem[];
  const int lane = threadIdx.x & 63;
  const int wid = __builtin_amdgcn_readfirstlane(threadIdx.x >> 6);
  const int xcd = blockIdx.x & 7, idx = blockIdx.x >> 3;
  const int tp = xcd * 4 + (idx & 3);
  const int dp = idx >> 2;
  const int g = lane >> 4;
  const v4u* hp = (const v4u*)(ws + WS_H) + ((size_t)(4 * tp) * 28 + wid * 7) * 64 + lane;
  const v4u* wp = (const v4u*)(ws + WS_WO) + ((size_t)(wid * 7) * 28 + dp * 4) * 64 + lane;
  v4u Hf[7][4], Wf[7][4];
#pragma unroll
  for (int k = 0; k < 7; ++k) {
#pragma unroll
    for (int n = 0; n < 4; ++n) Wf[k][n] = wp[(k * 28 + n) * 64];
#pragma unroll
    for (int m = 0; m < 4; ++m) Hf[k][m] = hp[(m * 28 + k) * 64];
  }
  SCHED();
  f32x4 acc[4][4];
#pragma unroll
  for (int m = 0; m < 4; ++m)
#pragma unroll
    for (int n = 0; n < 4; ++n) acc[m][n] = (f32x4){0.f, 0.f, 0.f, 0.f};
#pragma unroll
  for (int k = 0; k < 7; ++k)
#pragma unroll
    for (int m = 0; m < 4; ++m)
#pragma unroll
      for (int n = 0; n < 4; ++n) acc[m][n] = mfma16(Wf[k][n], Hf[k][m], acc[m][n]);
#pragma unroll
  for (int dm = 1; dm < 4; ++dm)
#pragma unroll
    for (int m = 0; m < 4; ++m)
      if (m == ((wid + dm) & 3)) {
#pragma unroll
        for (int n = 0; n < 4; ++n) *(f32x4*)(smem + ((m * 3 + dm - 1) * 4 + n) * 1024 + lane * 16) = acc[m][n];
      }
  __syncthreads();
  f32x4 o[4];
#pragma unroll
  for (int m = 0; m < 4; ++m)
    if (m == wid) {
#pragma unroll
      for (int n = 0; n < 4; ++n) o[n] = acc[m][n];
    }
#pragma unroll
  for (int dm = 0; dm < 3; ++dm)
#pragma unroll
    for (int n = 0; n < 4; ++n) o[n] += *(const f32x4*)(smem + ((wid * 3 + dm) * 4 + n) * 1024 + lane * 16);
  unsigned short* op = out + (size_t)(tp * 64 + wid * 16 + (lane & 15)) * D_ + dp * 64 + 4 * g;
#pragma unroll
  for (int n = 0; n < 4; ++n) {
    v2u h;
    h[0] = pack_bf16(o[n][0], o[n][1]);
    h[1] = pack_bf16(o[n][2], o[n][3]);
    *(v2u*)(op + n * 16) = h;
  }
}

extern "C" void kernel_launch(void* const* d_in, const int* in_sizes, int n_in,
                              void* d_out, int out_size, void* d_ws, size_t ws_size,
                              hipStream_t stream) {
  const unsigned short* x = (const unsigned short*)d_in[0];
  const unsigned short* win = (const unsigned short*)d_in[1];
  const unsigned short* wsp = (const unsigned short*)d_in[2];
  const unsigned short* bsp = (const unsigned short*)d_in[3];
  const unsigned short* wout = (const unsigned short*)d_in[4];
  char* ws = (char*)d_ws;
  (void)hipFuncSetAttribute((const void*)k1_gemm_gelu, hipFuncAttributeMaxDynamicSharedMemorySize, K1_LDS);
  (void)hipFuncSetAttribute((const void*)k2_spatial, hipFuncAttributeMaxDynamicSharedMemorySize, K2_LDS);
  (void)hipFuncSetAttribute((const void*)k3_out, hipFuncAttributeMaxDynamicSharedMemorySize, K3_LDS);
  k1_gemm_gelu<<<256, 512, K1_LDS, stream>>>(x, win, wsp, wout, ws);
  k2_spatial<<<256, 256, K2_LDS, stream>>>(wsp, bsp, ws);
  k3_out<<<224, 256, K3_LDS, stream>>>(ws, (unsigned short*)d_out);
}
```

```cpp
#include <hip/hip_runtime.h>
#include <hip/hip_bf16.h>
#include <stdint.h>

typedef __attribute__((ext_vector_type(8))) short bf16x8;
typedef __attribute__((ext_vector_type(4))) short s16x4;
typedef __attribute__((ext_vector_type(4))) float f32x4;
typedef __attribute__((ext_vector_type(4))) unsigned v4u;
typedef __attribute__((ext_vector_type(2))) unsigned v2u;
typedef __attribute__((address_space(3))) s16x4* lds_s16x4_p;

#define S_ 2048
#define D_ 448
#define F_ 1792
#define FH_ 896

#define WS_Z1 0
#define WS_Z2 (WS_Z1 + 3670016)
#define WS_H  (WS_Z2 + 3670016)
#define WS_WO (WS_H + 3670016)

#define SCHED() __builtin_amdgcn_sched_barrier(0)
#define STAMP_DECL
#define STAMP()
#define STAMP_OUT(kid)

__device__ __forceinline__ f32x4 mfma16(v4u a, v4u b, f32x4 c) {
  return __builtin_amdgcn_mfma_f32_16x16x32_bf16(__builtin_bit_cast(bf16x8, a), __builtin_bit_cast(bf16x8, b), c, 0, 0, 0);
}
typedef __attribute__((ext_vector_type(2))) float f32x2;
typedef __attribute__((ext_vector_type(2))) __bf16 bf16x2;
__device__ __forceinline__ unsigned pack_bf16(float lo, float hi) {
  const bf16x2 r = __builtin_convertvector((f32x2){lo, hi}, bf16x2);
  return __builtin_bit_cast(unsigned, r);
}
__device__ __forceinline__ float asf(unsigned u) { return __builtin_bit_cast(float, u); }
__device__ __forceinline__ float bf_lo(unsigned u) { return __builtin_bit_cast(float, u << 16); }
__device__ __forceinline__ float bf_hi(unsigned u) { return __builtin_bit_cast(float, u & 0xffff0000u); }

__device__ __forceinline__ void glds16(unsigned voff, const void* sbase, unsigned lds) {
  asm volatile("s_mov_b32 m0, %2\n\ts_nop 0\n\tglobal_load_lds_dwordx4 %0, %1" ::"v"(voff), "s"(sbase), "s"(lds) : "memory");
}
template <int OFF>
__device__ __forceinline__ void glds16o(unsigned voff, const void* sbase, unsigned lds) {
  asm volatile("s_mov_b32 m0, %2\n\ts_nop 0\n\tglobal_load_lds_dwordx4 %0, %1 offset:%3" ::"v"(voff), "s"(sbase), "s"(lds - OFF), "i"(OFF) : "memory");
}
__device__ __forceinline__ unsigned lds_addr(const void* p) { return (unsigned)(size_t)(__attribute__((address_space(3))) const char*)p; }

__device__ __forceinline__ void touch(unsigned& dst, const void* p) { asm volatile("global_load_dword %0, %1, off" : "+v"(dst) : "v"(p) : "memory"); }
__device__ __forceinline__ void st_plain8(v2u d, void* p) { asm volatile("global_store_dwordx2 %0, %1, off" ::"v"(p), "v"(d) : "memory"); }

__device__ __forceinline__ float gelu_f(float x) {
  const float c0 = -2.0f * 0.7978845608028654f * 1.4426950408889634f;
  const float c1 = c0 * 0.044715f;
  float t = __builtin_fmaf(x * x, c1, c0);
  float e = __builtin_amdgcn_exp2f(x * t);
  return x * __builtin_amdgcn_rcpf(1.0f + e);
}

#define K1_XS 16384
#define K1_WB (6 * K1_XS)
#define K1_LDS (K1_WB + 8 * 8192)
#define WAITBAR(N) asm volatile("s_waitcnt vmcnt(" #N ") lgkmcnt(0)\n\ts_barrier" ::: "memory")
#define WS_EX (WS_WO + 802816)
#define EX_CH 131072
#define EX_GRP (EX_CH + 2048)

__device__ __forceinline__ void ld8_sc1(v2u& d, unsigned voff, const void* sb) { asm volatile("global_load_dwordx2 %0, %1, %2 sc1" : "=v"(d) : "v"(voff), "s"(sb) : "memory"); }
__device__ __forceinline__ void ld4_sc1(unsigned& d, unsigned voff, const void* sb) { asm volatile("global_load_dword %0, %1, %2 sc1" : "=v"(d) : "v"(voff), "s"(sb) : "memory"); }
__device__ __forceinline__ void st8_sc1(v2u d, unsigned voff, const void* sb) { asm volatile("global_store_dwordx2 %0, %1, %2 sc1" ::"v"(voff), "v"(d), "s"(sb) : "memory"); }
__device__ __forceinline__ void st4_sc1(unsigned d, unsigned voff, const void* sb) { asm volatile("global_store_dword %0, %1, %2 sc1" ::"v"(voff), "v"(d), "s"(sb) : "memory"); }
template <int CTRL>
__device__ __forceinline__ float dpp_f(float v) {
  return __builtin_bit_cast(float, __builtin_amdgcn_update_dpp(0, __builtin_bit_cast(int, v), CTRL, 0xf, 0xf, true));
}
#define DPP_XOR1 0xB1
#define DPP_XOR3 0x1B
#define DPP_XOR7 0x141
#define DPP_XOR15 0x140
#define DPP_XOR8 0x128
#define DPP_BC(n) (0x150 + (n))

__global__ __launch_bounds__(640, 3) void k1_gemm_gelu(const unsigned short* __restrict__ x, const unsigned short* __restrict__ win,
                                                      const unsigned short* __restrict__ wsp, const unsigned short* __restrict__ wout,
                                                      char* __restrict__ ws) {
  extern __shared__ __attribute__((aligned(16))) char smem[];
  const int lane = threadIdx.x & 63;
  const int wid = __builtin_amdgcn_readfirstlane(threadIdx.x >> 6);
  const int xcd = blockIdx.x & 7, idx = blockIdx.x >> 3;
  const int g = lane >> 4, j = lane & 15;
  if (idx >= 28) {
    if (wid >= 8) return;
    STAMP_DECL STAMP();
    const int hw = ((idx - 28) * 8 + xcd) * 8 + wid;
    const char* pb = (const char*)wsp + ((size_t)((xcd >> 1) * 512 + ((xcd & 1) * 4 + idx - 28) * 64 + wid * 8) * S_) * 2;
    unsigned tdst = 0;
#pragma unroll
    for (int i = 0; i < 4; ++i)
      asm volatile("global_load_dword %0, %1, %2" : "+v"(tdst) : "v"(lane * 128), "s"(pb + i * 8192) : "memory");
    STAMP();
    unsigned short v[4][8];
#pragma unroll
    for (int i = 0; i < 4; ++i) {
      const int b = min(hw + 256 * i, 783), kf = b / 28, dt = b % 28;
#pragma unroll
      for (int e = 0; e < 8; ++e) v[i][e] = wout[(size_t)(32 * kf + 8 * g + e) * D_ + 16 * dt + j];
    }
#pragma unroll
    for (int i = 0; i < 4; ++i) {
      const int b = min(hw + 256 * i, 783);
      v4u o;
#pragma unroll
      for (int q = 0; q < 4; ++q) o[q] = (unsigned)v[i][2 * q] | ((unsigned)v[i][2 * q + 1] << 16);
      *(v4u*)(ws + WS_WO + ((size_t)b * 64 + lane) * 16) = o;
    }
    asm volatile("s_waitcnt vmcnt(0)" : "+v"(tdst)::"memory");
    STAMP(); STAMP_OUT(3);
    return;
  }
  const int rp = (xcd >> 1) * 4 + (idx & 3);
  const int cp = (xcd & 1) * 7 + (idx >> 2);

  const unsigned sm0 = lds_addr(smem);
#define BAR_L() asm volatile("s_waitcnt lgkmcnt(0)\n\ts_barrier" ::: "memory")
#define BAR_N() asm volatile("s_barrier" ::: "memory")
#define WAITBAR_V(N) asm volatile("s_waitcnt vmcnt(" #N ")\n\ts_barrier" ::: "memory")
  if (wid >= 8) {
    const int w0 = (wid - 8) * 4;
    const char* xb0 = (const char*)(x + (size_t)(rp * 128 + w0 * 16) * D_);
    const unsigned xvo0 = (lane >> 3) * (D_ * 2) + (((lane & 7) ^ g) * 16);
    const unsigned xvo1 = (lane >> 3) * (D_ * 2) + (((lane & 7) ^ (g + 4)) * 16) + 8 * D_ * 2;
    const char* w1 = (const char*)(win + cp * 64 + (size_t)(w0 * 8) * F_);
    const char* w2 = w1 + FH_ * 2;
    const unsigned wvoa = (lane >> 3) * (F_ * 2) + (((lane & 7) ^ (2 * (g & 1))) * 16);
    const unsigned wvob = wvoa ^ 64;
    const unsigned xl = sm0 + w0 * 2048, wl = sm0 + K1_WB + w0 * 1024;
#define K1_WL(base, c, dst, dstr) { _Pragma("unroll") for (int q = 0; q < 4; ++q) \
      glds16((q & 1) ? wvob : wvoa, (base) + (size_t)((c) * 64 + q * 8) * (F_ * 2), (dst) + q * (dstr)); }
#define K1_C(c) { _Pragma("unroll") for (int q = 0; q < 4; ++q) { \
      glds16(xvo0, xb0 + q * 16 * D_ * 2 + (c) * 128, xl + (c) * K1_XS + q * 2048); \
      glds16(xvo1, xb0 + q * 16 * D_ * 2 + (c) * 128, xl + (c) * K1_XS + q * 2048 + 1024); } \
      K1_WL(w2, c, wl + (c) * 8192, 1024) }
    K1_C(0) K1_C(1) K1_C(2)
    WAITBAR_V(24);
    K1_C(3)
    WAITBAR_V(24);
    K1_C(4)
    WAITBAR_V(24);
    K1_C(5)
    WAITBAR_V(24);
    K1_WL(w2, 6, wl + 6 * 8192, 1024) K1_WL(w1, 0, wl + 7 * 8192, 1024) K1_WL(w1, 1, xl, 2048)
    WAITBAR_V(24);
    K1_WL(w1, 2, xl + 1024, 2048) K1_WL(w1, 3, xl + K1_XS, 2048) K1_WL(w1, 4, xl + K1_XS + 1024, 2048)
    WAITBAR_V(24);
    K1_WL(w1, 5, xl + 2 * K1_XS, 2048) K1_WL(w1, 6, xl + 2 * K1_XS + 1024, 2048)
    WAITBAR_V(28);
    WAITBAR_V(0);
    return;
  }
  STAMP_DECL STAMP();
  const char* ex = ws + WS_EX + (size_t)(rp * 8 + wid) * EX_GRP;
  const unsigned gr_off = (g * 8 + (j >> 1)) * 8;
  const unsigned in_vo = (cp * 16 + (j & 1)) * 512 + gr_off;
  const unsigned out_vo = ((j & 1) * 16 + cp) * 512 + gr_off;
  const unsigned pin_vo = (cp * 16 + j) * 512 + 256;
  const unsigned pout_vo = (j * 16 + cp) * 512 + 256;
  v2u in[7], pin;
#define K1_LDIN() { _Pragma("unroll") for (int k = 0; k < 7; ++k) ld8_sc1(in[k], in_vo, ex + k * 1024); ld8_sc1(pin, pin_vo, ex); }
#define K1_INREGS "+v"(in[0]), "+v"(in[1]), "+v"(in[2]), "+v"(in[3]), "+v"(in[4]), "+v"(in[5]), "+v"(in[6]), "+v"(pin)
  v4u xf[14];
  K1_LDIN()
  asm volatile("global_load_dwordx4 %0, %2, %3 offset:768\n\tglobal_load_dwordx4 %1, %2, %3 offset:832"
               : "=&v"(xf[12]), "=&v"(xf[13]) : "v"(j * (D_ * 2) + g * 16), "s"((const char*)(x + (size_t)(rp * 128 + wid * 16) * D_)) : "memory");
  STAMP();
  const int xrd0 = wid * 2048 + j * 128 + ((g ^ (j >> 1)) * 16), xrd1 = xrd0 ^ 64;
  const int fsw = 2 * (((j >> 3) & 1) + 2 * (g & 1));
  const int tra = g * 1024 + (j >> 2) * 128 + (((j >> 1) & 1) * 16) + 8 * (j & 1);
  const int trb = g * 2048 + (j >> 2) * 128 + (((j >> 1) & 1) * 16) + 8 * (j & 1);
  v4u wf[2][8];
  f32x4 acc[4];
#pragma unroll
  for (int n = 0; n < 4; ++n) acc[n] = (f32x4){0.f, 0.f, 0.f, 0.f};
#define K1_STEP(RDX, c, RDW, b, off, tb, kstr, MM, cm, ZT) { \
    if (RDX) { xf[2 * (c)] = *(const v4u*)(smem + (c) * K1_XS + xrd0); xf[2 * (c) + 1] = *(const v4u*)(smem + (c) * K1_XS + xrd1); } \
    _Pragma("unroll") for (int i = 0; i < 8; ++i) { \
      const int k2 = i >> 2, n = i & 3; \
      if (RDW) { const char* sb = smem + (off) + (tb) + (((2 * n) ^ fsw) * 16) + k2 * (kstr); \
        s16x4 t0 = __builtin_amdgcn_ds_read_tr16_b64_v4i16((lds_s16x4_p)(sb)); \
        s16x4 t1 = __builtin_amdgcn_ds_read_tr16_b64_v4i16((lds_s16x4_p)(sb + 512)); \
        v2u a_ = __builtin_bit_cast(v2u, t0), b_ = __builtin_bit_cast(v2u, t1); \
        wf[b][i] = (v4u){a_[0], a_[1], b_[0], b_[1]}; } \
      if (MM) acc[n] = ZT ? mfma16(xf[2 * (cm) + k2], wf[(b) ^ 1][i], acc[n]) : mfma16(wf[(b) ^ 1][i], xf[2 * (cm) + k2], acc[n]); \
      SCHED(); } }

  BAR_N();
  STAMP();
  K1_STEP(1, 0, 1, 0, K1_WB, tra, 4096, 0, 0, 1)
  BAR_L();
  K1_STEP(1, 1, 1, 1, K1_WB + 8192, tra, 4096, 1, 0, 1)
  asm volatile("s_waitcnt vmcnt(2)" : K1_INREGS::"memory");
  unsigned nonce = 1;
  for (;;) {
    bool hit = pin[0] == nonce;
#pragma unroll
    for (int k = 0; k < 7; ++k) hit = hit || in[k][0] == nonce;
    if (!__any(hit)) break;
    ++nonce;
  }
  if (lane == 0) st4_sc1(nonce, EX_CH + cp * 128, ex);
  BAR_L();
  K1_STEP(1, 2, 1, 0, K1_WB + 2 * 8192, tra, 4096, 1, 1, 1)
  BAR_L();
  K1_STEP(1, 3, 1, 1, K1_WB + 3 * 8192, tra, 4096, 1, 2, 1)
  BAR_N();
  K1_STEP(1, 4, 1, 0, K1_WB + 4 * 8192, tra, 4096, 1, 3, 1)
  BAR_N();
  unsigned chal;
  ld4_sc1(chal, EX_CH + j * 128, ex);
  K1_STEP(1, 5, 1, 1, K1_WB + 5 * 8192, tra, 4096, 1, 4, 1)
  asm volatile("s_waitcnt vmcnt(2)\n\ts_barrier" : "+v"(xf[12]), "+v"(xf[13])::"memory");
  K1_STEP(0, 6, 1, 0, K1_WB + 6 * 8192, tra, 4096, 1, 5, 1)
  K1_STEP(0, 0, 0, 1, 0, trb, 8192, 1, 6, 1)
  STAMP();
  float v[4][4];
  unsigned myval;
  {
    float sh[4], S[4], Q[4];
#pragma unroll
    for (int n = 0; n < 4; ++n)
#pragma unroll
      for (int r = 0; r < 4; ++r) { v[n][r] = gelu_f(acc[n][r]); acc[n][r] = 0.f; }
#pragma unroll
    for (int r = 0; r < 4; ++r) {
      sh[r] = dpp_f<DPP_BC(0)>(v[0][r]);
      const float d0 = v[0][r] - sh[r], d1 = v[1][r] - sh[r], d2 = v[2][r] - sh[r], d3 = v[3][r] - sh[r];
      S[r] = (d0 + d1) + (d2 + d3);
      Q[r] = (d0 * d0 + d1 * d1) + (d2 * d2 + d3 * d3);
    }
    const bool b3 = j & 8, b2 = j & 4, b1 = j & 2;
    float T[4];
#pragma unroll
    for (int r = 0; r < 4; ++r) T[r] = (b3 ? Q[r] : S[r]) + dpp_f<DPP_XOR15>(b3 ? S[r] : Q[r]);
    const float U0 = (b2 ? T[2] : T[0]) + dpp_f<DPP_XOR7>(b2 ? T[0] : T[2]);
    const float U1 = (b2 ? T[3] : T[1]) + dpp_f<DPP_XOR7>(b2 ? T[1] : T[3]);
    float W = (b1 ? U1 : U0) + dpp_f<DPP_XOR3>(b1 ? U0 : U1);
    W += dpp_f<DPP_XOR1>(W);
    const float other = dpp_f<DPP_XOR8>(W);
    const float shr = b2 ? (b1 ? sh[3] : sh[2]) : (b1 ? sh[1] : sh[0]);
    myval = __builtin_bit_cast(unsigned, b3 ? W - other * other * (1.0f / 64.0f) : shr + W * (1.0f / 64.0f));
  }
#define K1_SEND(ans) { \
    _Pragma("unroll") for (int k = 0; k < 7; ++k) { const unsigned t_ = __shfl(ans, 2 * k + (j & 1)); st8_sc1((v2u){t_, myval}, out_vo, ex + k * 16384); } \
    if (lane < 16) st8_sc1((v2u){ans, nonce}, pout_vo, ex); }
  asm volatile("s_waitcnt vmcnt(0)" : "+v"(chal)::"memory");
  unsigned answered = chal;
  K1_SEND(answered)
  STAMP();
  BAR_N();
  STAMP();
  K1_STEP(0, 0, 1, 0, K1_WB + 7 * 8192, tra, 4096, 0, 0, 0)
  K1_STEP(0, 0, 1, 1, 0, trb, 8192, 1, 0, 0)
  K1_STEP(0, 0, 1, 0, 1024, trb, 8192, 1, 1, 0)
  K1_STEP(0, 0, 1, 1, K1_XS, trb, 8192, 1, 2, 0)
  K1_STEP(0, 0, 1, 0, K1_XS + 1024, trb, 8192, 1, 3, 0)
  K1_STEP(0, 0, 1, 1, 2 * K1_XS, trb, 8192, 1, 4, 0)
  K1_STEP(0, 0, 1, 0, 2 * K1_XS + 1024, trb, 8192, 1, 5, 0)
  K1_STEP(0, 0, 0, 1, 0, trb, 8192, 1, 6, 0)
  STAMP();
  K1_LDIN()
  ld4_sc1(chal, EX_CH + j * 128, ex);
  {
    char* z1 = ws + WS_Z1 + ((size_t)((8 * rp + wid) * 56 + cp * 4) * 64 + lane) * 8;
#pragma unroll
    for (int n = 0; n < 4; ++n) {
      v2u o;
      o[0] = pack_bf16(gelu_f(acc[n][0]), gelu_f(acc[n][1]));
      o[1] = pack_bf16(gelu_f(acc[n][2]), gelu_f(acc[n][3]));
      st_plain8(o, z1 + n * 512);
    }
  }
  STAMP();
  asm volatile("s_waitcnt vmcnt(4)" : K1_INREGS, "+v"(chal)::"memory");
  for (unsigned spin = 0; spin < (1u << 16); ++spin) {
    bool okd = true;
#pragma unroll
    for (int k = 0; k < 7; ++k) okd = okd && in[k][0] == nonce;
    const bool pv = pin[0] == nonce;
    const unsigned cur = pv ? pin[1] : chal;
    const bool live = j < 14;
    if (__any(live && cur != answered)) { answered = cur; K1_SEND(answered) }
    if (__all(okd && (pv || !live))) break;
    K1_LDIN()
    ld4_sc1(chal, EX_CH + j * 128, ex);
    asm volatile("s_waitcnt vmcnt(0)" : K1_INREGS, "+v"(chal)::"memory");
  }
  STAMP();
  {
    float sm = ((asf(in[0][1]) + asf(in[1][1])) + (asf(in[2][1]) + asf(in[3][1]))) + ((asf(in[4][1]) + asf(in[5][1])) + asf(in[6][1]));
    sm += dpp_f<DPP_XOR1>(sm);
    const float mean = sm * (1.0f / 14.0f);
    float bt = 0.f;
#pragma unroll
    for (int k = 0; k < 7; ++k) { const float e = asf(in[k][1]) - mean; bt = __builtin_fmaf(e, e, bt); }
    bt += dpp_f<DPP_XOR1>(bt);
    const float between = dpp_f<DPP_XOR8>(bt);
    const float rstd = rsqrtf((sm + 64.0f * between) * (1.0f / FH_) + 1e-5f);
    float mr[4], rr[4];
    mr[0] = dpp_f<DPP_BC(0)>(mean); mr[1] = dpp_f<DPP_BC(2)>(mean); mr[2] = dpp_f<DPP_BC(4)>(mean); mr[3] = dpp_f<DPP_BC(6)>(mean);
    rr[0] = dpp_f<DPP_BC(8)>(rstd); rr[1] = dpp_f<DPP_BC(10)>(rstd); rr[2] = dpp_f<DPP_BC(12)>(rstd); rr[3] = dpp_f<DPP_BC(14)>(rstd);
    char* z2 = ws + WS_Z2 + ((size_t)((4 * rp + (wid >> 1)) * 56 + cp * 4) * 64 + j + 16 * (2 * (wid & 1) + (g >> 1))) * 16 + (g & 1) * 8;
#pragma unroll
    for (int n = 0; n < 4; ++n) {
      v2u o;
      o[0] = pack_bf16((v[n][0] - mr[0]) * rr[0], (v[n][1] - mr[1]) * rr[1]);
      o[1] = pack_bf16((v[n][2] - mr[2]) * rr[2], (v[n][3] - mr[3]) * rr[3]);
      *(v2u*)(z2 + n * 1024) = o;
    }
  }
  STAMP(); STAMP_OUT(1);
}

#define K2_LDS (4 * 32768 + 1024)

template <int OFF>
__device__ __forceinline__ void gload_a(v4u& dst, unsigned voff, const void* sbase) {
  asm volatile("global_load_dwordx4 %0, %1, %2 offset:%3" : "=a"(dst) : "v"(voff), "s"(sbase), "i"(OFF) : "memory");
}

__global__ __launch_bounds__(256, 1) void k2_spatial(const unsigned short* __restrict__ wsp, const unsigned short* __restrict__ bsp,
                                                    char* __restrict__ ws) {
  extern __shared__ __attribute__((aligned(16))) char smem[];
  const int lane = threadIdx.x & 63;
  const int wid = __builtin_amdgcn_readfirstlane(threadIdx.x >> 6);
  const int xcd = blockIdx.x & 7, idx = blockIdx.x >> 3;
  const int tp = (xcd >> 1) * 8 + (idx & 7);
  const int fp = (xcd & 1) * 4 + (idx >> 3);
  const int g = lane >> 4;

  STAMP_DECL STAMP();
  v2u z1v[7];
  {
    const v2u* z1 = (const v2u*)(ws + WS_Z1) + ((size_t)((4 * tp + wid) * 56 + fp * 7)) * 64 + lane;
#pragma unroll
    for (int n = 0; n < 7; ++n) z1v[n] = z1[n * 64];
  }
  const float bias = __builtin_bit_cast(float, (unsigned)bsp[tp * 64 + wid * 16 + (lane & 15)] << 16);

  const char* abase = (const char*)(wsp + (size_t)(tp * 64) * S_ + wid * 512);
  const unsigned avo0 = (lane >> 3) * (S_ * 2) + (((lane & 7) ^ g) * 16);
  const unsigned avo1 = (lane >> 3) * (S_ * 2) + (((lane & 7) ^ (g + 4)) * 16);
  const unsigned ring = lds_addr(smem) + 1024 + wid * 32768;
  const char* stg = smem + 1024 + wid * 32768;
  const char* bbase = ws + WS_Z2 + ((size_t)(wid * 16) * 56 + fp * 7 + 3) * 1024;
  const unsigned bvo = lane * 16;
  const int rd0 = (lane & 15) * 128 + ((g ^ ((lane & 15) >> 1)) * 16);
  const int rd1 = rd0 ^ 64;

  v4u Bf[2][14];
  v4u Af[2][8];
  f32x4 acc[4][7];
#pragma unroll
  for (int m = 0; m < 4; ++m)
#pragma unroll
    for (int n = 0; n < 7; ++n) acc[m][n] = (f32x4){0.f, 0.f, 0.f, 0.f};

#define LOAD_A1(j, i) glds16o<(j) * 128>(((i) & 1) ? avo1 : avo0, abase + (size_t)(i) * 8 * (S_ * 2), ring + ((j) & 3) * 8192 + (i) * 1024);
#define LOAD_A(j) LOAD_A1(j, 0) LOAD_A1(j, 1) LOAD_A1(j, 2) LOAD_A1(j, 3) LOAD_A1(j, 4) LOAD_A1(j, 5) LOAD_A1(j, 6) LOAD_A1(j, 7)
#define LOAD_B1(j, ks, n) gload_a<((n) - 3) * 1024>(Bf[(j) & 1][(ks) * 7 + (n)], bvo, bbase + (size_t)(2 * (j) + (ks)) * 56 * 1024);
#define LOAD_Bk(j, ks) LOAD_B1(j, ks, 0) LOAD_B1(j, ks, 1) LOAD_B1(j, ks, 2) LOAD_B1(j, ks, 3) LOAD_B1(j, ks, 4) LOAD_B1(j, ks, 5) LOAD_B1(j, ks, 6)
#define LOAD_B(j) LOAD_Bk(j, 0) LOAD_Bk(j, 1)
#define READ_A(j) _Pragma("unroll") for (int m = 0; m < 4; ++m) _Pragma("unroll") for (int ks = 0; ks < 2; ++ks) \
    Af[(j) & 1][m * 2 + ks] = *(const v4u*)(stg + ((j) & 3) * 8192 + ((m + wid) & 3) * 2048 + (ks ? rd1 : rd0));
#define WAIT_B(N, P) asm volatile("s_waitcnt vmcnt(" #N ")" : "+a"(Bf[P][0]), "+a"(Bf[P][1]), "+a"(Bf[P][2]), "+a"(Bf[P][3]), "+a"(Bf[P][4]), \
    "+a"(Bf[P][5]), "+a"(Bf[P][6]), "+a"(Bf[P][7]), "+a"(Bf[P][8]), "+a"(Bf[P][9]), "+a"(Bf[P][10]), "+a"(Bf[P][11]), "+a"(Bf[P][12]), "+a"(Bf[P][13]) :: "memory");
#define MMA(j, ks, n) _Pragma("unroll") for (int m = 0; m < 4; ++m) acc[m][n] = mfma16(Bf[(j) & 1][(ks) * 7 + (n)], Af[(j) & 1][m * 2 + (ks)], acc[m][n]);
#define STEP_BODY(j, LB, LA) \
  MMA(j, 0, 0) SCHED(); if (LB) { LOAD_B1((j) + 2, 0, 0) } if (LA) { LOAD_A1((j) + 3, 0) } \
  MMA(j, 0, 1) SCHED(); if (LB) { LOAD_B1((j) + 2, 0, 1) } if (LA) { LOAD_A1((j) + 3, 1) } \
  MMA(j, 0, 2) SCHED(); if (LB) { LOAD_B1((j) + 2, 0, 2) } if (LA) { LOAD_A1((j) + 3, 2) } \
  MMA(j, 0, 3) SCHED(); if (LB) { LOAD_B1((j) + 2, 0, 3) } if (LA) { LOAD_A1((j) + 3, 3) } \
  MMA(j, 0, 4) SCHED(); if (LB) { LOAD_B1((j) + 2, 0, 4) } if (LA) { LOAD_A1((j) + 3, 4) } \
  MMA(j, 0, 5) SCHED(); if (LB) { LOAD_B1((j) + 2, 0, 5) } if (LA) { LOAD_A1((j) + 3, 5) } \
  MMA(j, 0, 6) SCHED(); if (LB) { LOAD_B1((j) + 2, 0, 6) } if (LA) { LOAD_A1((j) + 3, 6) } \
  MMA(j, 1, 0) SCHED(); if (LB) { LOAD_B1((j) + 2, 1, 0) } if (LA) { LOAD_A1((j) + 3, 7) } \
  MMA(j, 1, 1) SCHED(); if (LB) { LOAD_B1((j) + 2, 1, 1) } \
  MMA(j, 1, 2) SCHED(); if (LB) { LOAD_B1((j) + 2, 1, 2) } \
  MMA(j, 1, 3) SCHED(); if (LB) { LOAD_B1((j) + 2, 1, 3) } \
  MMA(j, 1, 4) SCHED(); if (LB) { LOAD_B1((j) + 2, 1, 4) } \
  MMA(j, 1, 5) SCHED(); if (LB) { LOAD_B1((j) + 2, 1, 5) } \
  MMA(j, 1, 6) SCHED(); if (LB) { LOAD_B1((j) + 2, 1, 6) } \
  SCHED();

  LOAD_A(0) LOAD_B(0) LOAD_A(1)
  LOAD_B(1) LOAD_A(2)
  STAMP();
  asm volatile("s_waitcnt vmcnt(44)" ::: "memory");
  STAMP();
  READ_A(0)
  SCHED();
  WAIT_B(22, 0) STAMP(); READ_A(1) SCHED(); STEP_BODY(0, 1, 1)
  WAIT_B(22, 1) STAMP(); READ_A(2) SCHED(); STEP_BODY(1, 1, 1)
  WAIT_B(22, 0) STAMP(); READ_A(3) SCHED(); STEP_BODY(2, 1, 1)
  WAIT_B(22, 1) STAMP(); READ_A(4) SCHED(); STEP_BODY(3, 1, 1)
  WAIT_B(22, 0) STAMP(); READ_A(5) SCHED(); STEP_BODY(4, 1, 1)
  WAIT_B(22, 1) STAMP(); READ_A(6) SCHED(); STEP_BODY(5, 1, 0)
  WAIT_B(14, 0) STAMP(); READ_A(7) SCHED(); STEP_BODY(6, 0, 0)
  WAIT_B(0, 1) STAMP(); SCHED();
  {
    char* mine = smem + 1024 + wid * 32768 + lane * 16;
#pragma unroll
    for (int mi = 1; mi <= 4; ++mi) {
      const int m = mi & 3;
#pragma unroll
      for (int ks = 0; ks < 2; ++ks)
#pragma unroll
        for (int n = 0; n < 7; ++n) acc[m][n] = mfma16(Bf[1][ks * 7 + n], Af[1][m * 2 + ks], acc[m][n]);
      SCHED();
      if (mi >= 2) {
#pragma unroll
        for (int n = 0; n < 7; ++n) *(f32x4*)(mine + ((mi - 1) * 7 + n) * 1024) = acc[mi - 1][n];
      }
      SCHED();
    }
  }
  STAMP();
  __syncthreads();
  STAMP();
  f32x4 o[7];
  {
    f32x4 pr[3][7];
#pragma unroll
    for (int d = 1; d < 4; ++d) {
      const char* src = smem + 1024 + ((wid - d) & 3) * 32768 + d * 7168 + lane * 16;
#pragma unroll
      for (int n = 0; n < 7; ++n) pr[d - 1][n] = *(const f32x4*)(src + n * 1024);
    }
    SCHED();
#pragma unroll
    for (int n = 0; n < 7; ++n) o[n] = (acc[0][n] + pr[0][n]) + (pr[1][n] + pr[2][n]);
  }
  char* hp = ws + WS_H + ((size_t)(4 * tp + wid) * 28 * 64 + (lane & 15) + 16 * (g >> 1)) * 16 + (g & 1) * 8;
#pragma unroll
  for (int n = 0; n < 7; ++n) {
    const int ft = fp * 7 + n;
    v2u h;
    h[0] = pack_bf16(bf_lo(z1v[n][0]) * (o[n][0] + bias), bf_hi(z1v[n][0]) * (o[n][1] + bias));
    h[1] = pack_bf16(bf_lo(z1v[n][1]) * (o[n][2] + bias), bf_hi(z1v[n][1]) * (o[n][3] + bias));
    *(v2u*)(hp + (ft >> 1) * 1024 + (ft & 1) * 512) = h;
  }
  STAMP(); STAMP_OUT(2);
}

#define K3_LDS (4 * 12 * 1024)
__global__ __launch_bounds__(256, 1) void k3_out(char* __restrict__ ws, unsigned short* __restrict__ out) {
  extern __shared__ __attribute__((aligned(16))) char smem[];
  const int lane = threadIdx.x & 63;
  const int wid = __builtin_amdgcn_readfirstlane(threadIdx.x >> 6);
  const int xcd = blockIdx.x & 7, idx = blockIdx.x >> 3;
  const int tp = xcd * 4 + (idx & 3);
  const int dp = idx >> 2;
  const int g = lane >> 4;
  STAMP_DECL STAMP();
  const v4u* hp = (const v4u*)(ws + WS_H) + ((size_t)(4 * tp) * 28 + wid * 7) * 64 + lane;
  const v4u* wp = (const v4u*)(ws + WS_WO) + ((size_t)(wid * 7) * 28 + dp * 4) * 64 + lane;
  v4u Hf[7][4], Wf[7][4];
#define K3_LD(k) { _Pragma("unroll") for (int n = 0; n < 4; ++n) Wf[k][n] = wp[((k) * 28 + n) * 64]; \
    _Pragma("unroll") for (int m = 0; m < 4; ++m) Hf[k][m] = hp[(((m + wid) & 3) * 28 + (k)) * 64]; SCHED(); }
#define K3_MM(k) { _Pragma("unroll") for (int m = 0; m < 4; ++m) _Pragma("unroll") for (int n = 0; n < 4; ++n) \
    acc[m][n] = mfma16(Wf[k][n], Hf[k][m], acc[m][n]); SCHED(); }
  f32x4 acc[4][4];
#pragma unroll
  for (int m = 0; m < 4; ++m)
#pragma unroll
    for (int n = 0; n < 4; ++n) acc[m][n] = (f32x4){0.f, 0.f, 0.f, 0.f};
  K3_LD(0) K3_LD(1) K3_LD(2) K3_LD(3)
  STAMP();
  K3_MM(0) K3_LD(4) K3_MM(1) K3_LD(5) K3_MM(2) K3_LD(6) K3_MM(3) K3_MM(4) K3_MM(5) K3_MM(6)
  STAMP();
#pragma unroll
  for (int m = 1; m < 4; ++m)
#pragma unroll
    for (int n = 0; n < 4; ++n) *(f32x4*)(smem + ((wid * 3 + m - 1) * 4 + n) * 1024 + lane * 16) = acc[m][n];
  __syncthreads();
  STAMP();
  f32x4 pr[3][4];
#pragma unroll
  for (int d = 1; d < 4; ++d)
#pragma unroll
    for (int n = 0; n < 4; ++n) pr[d - 1][n] = *(const f32x4*)(smem + ((((wid - d) & 3) * 3 + d - 1) * 4 + n) * 1024 + lane * 16);
  unsigned short* op = out + (size_t)(tp * 64 + wid * 16 + (lane & 15)) * D_ + dp * 64 + 4 * g;
#pragma unroll
  for (int n = 0; n < 4; ++n) {
    const f32x4 o = (acc[0][n] + pr[0][n]) + (pr[1][n] + pr[2][n]);
    v2u h;
    h[0] = pack_bf16(o[0], o[1]);
    h[1] = pack_bf16(o[2], o[3]);
    *(v2u*)(op + n * 16) = h;
  }
  STAMP(); STAMP_OUT(0);
}

extern "C" void kernel_launch(void* const* d_in, const int* in_sizes, int n_in,
                              void* d_out, int out_size, void* d_ws, size_t ws_size,
                              hipStream_t stream) {
  const unsigned short* x = (const unsigned short*)d_in[0];
  const unsigned short* win = (const unsigned short*)d_in[1];
  const unsigned short* wsp = (const unsigned short*)d_in[2];
  const unsigned short* bsp = (const unsigned short*)d_in[3];
  const unsigned short* wout = (const unsigned short*)d_in[4];
  char* ws = (char*)d_ws;
  (void)hipFuncSetAttribute((const void*)k1_gemm_gelu, hipFuncAttributeMaxDynamicSharedMemorySize, K1_LDS);
  (void)hipFuncSetAttribute((const void*)k2_spatial, hipFuncAttributeMaxDynamicSharedMemorySize, K2_LDS);
  (void)hipFuncSetAttribute((const void*)k3_out, hipFuncAttributeMaxDynamicSharedMemorySize, K3_LDS);
  k1_gemm_gelu<<<256, 640, K1_LDS, stream>>>(x, win, wsp, wout, ws);
  k2_spatial<<<256, 256, K2_LDS, stream>>>(wsp, bsp, ws);
  k3_out<<<224, 256, K3_LDS, stream>>>(ws, (unsigned short*)d_out);
}
```

```cpp
#include <hip/hip_runtime.h>
#include <hip/hip_bf16.h>
#include <stdint.h>

typedef __attribute__((ext_vector_type(8))) short bf16x8;
typedef __attribute__((ext_vector_type(4))) short s16x4;
typedef __attribute__((ext_vector_type(4))) float f32x4;
typedef __attribute__((ext_vector_type(4))) unsigned v4u;
typedef __attribute__((ext_vector_type(2))) unsigned v2u;
typedef __attribute__((address_space(3))) s16x4* lds_s16x4_p;

#define S_ 2048
#define D_ 448
#define F_ 1792
#define FH_ 896

#define WS_Z1 0
#define WS_Z2 (WS_Z1 + 3670016)
#define WS_H  (WS_Z2 + 3670016)
#define WS_WO (WS_H + 3670016)

#define SCHED() __builtin_amdgcn_sched_barrier(0)
#define STAMP_DECL
#define STAMP()
#define STAMP_OUT(kid)

__device__ __forceinline__ f32x4 mfma16(v4u a, v4u b, f32x4 c) {
  return __builtin_amdgcn_mfma_f32_16x16x32_bf16(__builtin_bit_cast(bf16x8, a), __builtin_bit_cast(bf16x8, b), c, 0, 0, 0);
}
typedef __attribute__((ext_vector_type(2))) float f32x2;
typedef __attribute__((ext_vector_type(2))) __bf16 bf16x2;
__device__ __forceinline__ unsigned pack_bf16(float lo, float hi) {
  const bf16x2 r = __builtin_convertvector((f32x2){lo, hi}, bf16x2);
  return __builtin_bit_cast(unsigned, r);
}
__device__ __forceinline__ float asf(unsigned u) { return __builtin_bit_cast(float, u); }
__device__ __forceinline__ float bf_lo(unsigned u) { return __builtin_bit_cast(float, u << 16); }
__device__ __forceinline__ float bf_hi(unsigned u) { return __builtin_bit_cast(float, u & 0xffff0000u); }

__device__ __forceinline__ void glds16(unsigned voff, const void* sbase, unsigned lds) {
  asm volatile("s_mov_b32 m0, %2\n\ts_nop 0\n\tglobal_load_lds_dwordx4 %0, %1" ::"v"(voff), "s"(sbase), "s"(lds) : "memory");
}
template <int OFF>
__device__ __forceinline__ void glds16o(unsigned voff, const void* sbase, unsigned lds) {
  asm volatile("s_mov_b32 m0, %2\n\ts_nop 0\n\tglobal_load_lds_dwordx4 %0, %1 offset:%3" ::"v"(voff), "s"(sbase), "s"(lds - OFF), "i"(OFF) : "memory");
}
__device__ __forceinline__ unsigned lds_addr(const void* p) { return (unsigned)(size_t)(__attribute__((address_space(3))) const char*)p; }

__device__ __forceinline__ void touch(unsigned& dst, const void* p) { asm volatile("global_load_dword %0, %1, off" : "+v"(dst) : "v"(p) : "memory"); }
__device__ __forceinline__ void st_plain8(v2u d, void* p) { asm volatile("global_store_dwordx2 %0, %1, off" ::"v"(p), "v"(d) : "memory"); }

__device__ __forceinline__ float gelu_f(float x) {
  const float c0 = -2.0f * 0.7978845608028654f * 1.4426950408889634f;
  const float c1 = c0 * 0.044715f;
  float t = __builtin_fmaf(x * x, c1, c0);
  float e = __builtin_amdgcn_exp2f(x * t);
  return x * __builtin_amdgcn_rcpf(1.0f + e);
}

#define K1_XS 16384
#define K1_WB (6 * K1_XS)
#define K1_LDS (K1_WB + 8 * 8192)
#define WAITBAR(N) asm volatile("s_waitcnt vmcnt(" #N ") lgkmcnt(0)\n\ts_barrier" ::: "memory")
#define WS_EX (WS_WO + 802816)
#define EX_CH 131072
#define EX_GRP (EX_CH + 2048)

__device__ __forceinline__ void ld8_sc1(v2u& d, unsigned voff, const void* sb) { asm volatile("global_load_dwordx2 %0, %1, %2 sc1" : "=v"(d) : "v"(voff), "s"(sb) : "memory"); }
__device__ __forceinline__ void ld4_sc1(unsigned& d, unsigned voff, const void* sb) { asm volatile("global_load_dword %0, %1, %2 sc1" : "=v"(d) : "v"(voff), "s"(sb) : "memory"); }
__device__ __forceinline__ void st8_sc1(v2u d, unsigned voff, const void* sb) { asm volatile("global_store_dwordx2 %0, %1, %2 sc1" ::"v"(voff), "v"(d), "s"(sb) : "memory"); }
__device__ __forceinline__ void st4_sc1(unsigned d, unsigned voff, const void* sb) { asm volatile("global_store_dword %0, %1, %2 sc1" ::"v"(voff), "v"(d), "s"(sb) : "memory"); }
template <int CTRL>
__device__ __forceinline__ float dpp_f(float v) {
  return __builtin_bit_cast(float, __builtin_amdgcn_update_dpp(0, __builtin_bit_cast(int, v), CTRL, 0xf, 0xf, true));
}
#define DPP_XOR1 0xB1
#define DPP_XOR3 0x1B
#define DPP_XOR7 0x141
#define DPP_XOR15 0x140
#define DPP_XOR8 0x128
#define DPP_BC(n) (0x150 + (n))

__global__ __launch_bounds__(640, 3) void k1_gemm_gelu(const unsigned short* __restrict__ x, const unsigned short* __restrict__ win,
                                                      const unsigned short* __restrict__ wsp, const unsigned short* __restrict__ wout,
                                                      char* __restrict__ ws) {
  extern __shared__ __attribute__((aligned(16))) char smem[];
  const int lane = threadIdx.x & 63;
  const int wid = __builtin_amdgcn_readfirstlane(threadIdx.x >> 6);
  const int xcd = blockIdx.x & 7, idx = blockIdx.x >> 3;
  const int g = lane >> 4, j = lane & 15;
  if (idx >= 28) {
    if (wid >= 8) return;
    STAMP_DECL STAMP();
    const int hw = ((idx - 28) * 8 + xcd) * 8 + wid;
    const char* pb = (const char*)wsp + ((size_t)((xcd >> 1) * 512 + ((xcd & 1) * 4 + idx - 28) * 64 + wid * 8) * S_) * 2;
    unsigned tdst = 0;
    if (idx == 28 && wid < 4) {
      unsigned long long pc; int d0, len;
      asm volatile("s_getpc_b64 %0\n.Lpf_here_%=:\n\ts_mov_b32 %1, _Z12k1_gemm_geluPKtS0_S0_S0_Pc-.Lpf_here_%=\n\t"
                   "s_mov_b32 %2, _Z6k3_outPcPt-_Z12k1_gemm_geluPKtS0_S0_S0_Pc" : "=s"(pc), "=s"(d0), "=s"(len));
      const unsigned off = min((unsigned)(wid * 8192 + lane * 128), (unsigned)(len + 2560));
      asm volatile("global_load_dword %0, %1, %2" : "+v"(tdst) : "v"(off), "s"(pc + (long long)d0) : "memory");
    }
#pragma unroll
    for (int i = 0; i < 4; ++i)
      asm volatile("global_load_dword %0, %1, %2" : "+v"(tdst) : "v"(lane * 128), "s"(pb + i * 8192) : "memory");
    STAMP();
    unsigned short v[4][8];
#pragma unroll
    for (int i = 0; i < 4; ++i) {
      const int b = min(hw + 256 * i, 783), kf = b / 28, dt = b % 28;
#pragma unroll
      for (int e = 0; e < 8; ++e) v[i][e] = wout[(size_t)(32 * kf + 8 * g + e) * D_ + 16 * dt + j];
    }
#pragma unroll
    for (int i = 0; i < 4; ++i) {
      const int b = min(hw + 256 * i, 783);
      v4u o;
#pragma unroll
      for (int q = 0; q < 4; ++q) o[q] = (unsigned)v[i][2 * q] | ((unsigned)v[i][2 * q + 1] << 16);
      *(v4u*)(ws + WS_WO + ((size_t)b * 64 + lane) * 16) = o;
    }
    asm volatile("s_waitcnt vmcnt(0)" : "+v"(tdst)::"memory");
    STAMP(); STAMP_OUT(3);
    return;
  }
  const int rp = (xcd >> 1) * 4 + (idx & 3);
  const int cp = (xcd & 1) * 7 + (idx >> 2);

  const unsigned sm0 = lds_addr(smem);
#define BAR_L() asm volatile("s_waitcnt lgkmcnt(0)\n\ts_barrier" ::: "memory")
#define BAR_N() asm volatile("s_barrier" ::: "memory")
#define WAITBAR_V(N) asm volatile("s_waitcnt vmcnt(" #N ")\n\ts_barrier" ::: "memory")
  if (wid >= 8) {
    const int w0 = (wid - 8) * 4;
    const char* xb0 = (const char*)(x + (size_t)(rp * 128 + w0 * 16) * D_);
    const unsigned xvo0 = (lane >> 3) * (D_ * 2) + (((lane & 7) ^ g) * 16);
    const unsigned xvo1 = (lane >> 3) * (D_ * 2) + (((lane & 7) ^ (g + 4)) * 16) + 8 * D_ * 2;
    const char* w1 = (const char*)(win + cp * 64 + (size_t)(w0 * 8) * F_);
    const char* w2 = w1 + FH_ * 2;
    const unsigned wvoa = (lane >> 3) * (F_ * 2) + (((lane & 7) ^ (2 * (g & 1))) * 16);
    const unsigned wvob = wvoa ^ 64;
    const unsigned xl = sm0 + w0 * 2048, wl = sm0 + K1_WB + w0 * 1024;
#define K1_WL(base, c, dst, dstr) { _Pragma("unroll") for (int q = 0; q < 4; ++q) \
      glds16((q & 1) ? wvob : wvoa, (base) + (size_t)((c) * 64 + q * 8) * (F_ * 2), (dst) + q * (dstr)); }
#define K1_C(c) { _Pragma("unroll") for (int q = 0; q < 4; ++q) { \
      glds16(xvo0, xb0 + q * 16 * D_ * 2 + (c) * 128, xl + (c) * K1_XS + q * 2048); \
      glds16(xvo1, xb0 + q * 16 * D_ * 2 + (c) * 128, xl + (c) * K1_XS + q * 2048 + 1024); } \
      K1_WL(w2, c, wl + (c) * 8192, 1024) }
    K1_C(0) K1_C(1) K1_C(2)
    WAITBAR_V(24);
    K1_C(3)
    WAITBAR_V(24);
    K1_C(4)
    WAITBAR_V(24);
    K1_C(5)
    WAITBAR_V(24);
    K1_WL(w2, 6, wl + 6 * 8192, 1024) K1_WL(w1, 0, wl + 7 * 8192, 1024) K1_WL(w1, 1, xl, 2048)
    WAITBAR_V(24);
    K1_WL(w1, 2, xl + 1024, 2048) K1_WL(w1, 3, xl + K1_XS, 2048) K1_WL(w1, 4, xl + K1_XS + 1024, 2048)
    WAITBAR_V(24);
    K1_WL(w1, 5, xl + 2 * K1_XS, 2048) K1_WL(w1, 6, xl + 2 * K1_XS + 1024, 2048)
    WAITBAR_V(28);
    WAITBAR_V(0);
    return;
  }
  STAMP_DECL STAMP();
  const char* ex = ws + WS_EX + (size_t)(rp * 8 + wid) * EX_GRP;
  const unsigned gr_off = (g * 8 + (j >> 1)) * 8;
  const unsigned in_vo = (cp * 16 + (j & 1)) * 512 + gr_off;
  const unsigned out_vo = ((j & 1) * 16 + cp) * 512 + gr_off;
  const unsigned pin_vo = (cp * 16 + j) * 512 + 256;
  const unsigned pout_vo = (j * 16 + cp) * 512 + 256;
  v2u in[7], pin;
#define K1_LDIN() { _Pragma("unroll") for (int k = 0; k < 7; ++k) ld8_sc1(in[k], in_vo, ex + k * 1024); ld8_sc1(pin, pin_vo, ex); }
#define K1_INREGS "+v"(in[0]), "+v"(in[1]), "+v"(in[2]), "+v"(in[3]), "+v"(in[4]), "+v"(in[5]), "+v"(in[6]), "+v"(pin)
  v4u xf[14];
  K1_LDIN()
  asm volatile("global_load_dwordx4 %0, %2, %3 offset:768\n\tglobal_load_dwordx4 %1, %2, %3 offset:832"
               : "=&v"(xf[12]), "=&v"(xf[13]) : "v"(j * (D_ * 2) + g * 16), "s"((const char*)(x + (size_t)(rp * 128 + wid * 16) * D_)) : "memory");
  STAMP();
  const int xrd0 = wid * 2048 + j * 128 + ((g ^ (j >> 1)) * 16), xrd1 = xrd0 ^ 64;
  const int fsw = 2 * (((j >> 3) & 1) + 2 * (g & 1));
  const int tra = g * 1024 + (j >> 2) * 128 + (((j >> 1) & 1) * 16) + 8 * (j & 1);
  const int trb = g * 2048 + (j >> 2) * 128 + (((j >> 1) & 1) * 16) + 8 * (j & 1);
  v4u wf[2][8];
  f32x4 acc[4];
#pragma unroll
  for (int n = 0; n < 4; ++n) acc[n] = (f32x4){0.f, 0.f, 0.f, 0.f};
#define K1_STEP(RDX, c, RDW, b, off, tb, kstr, MM, cm, ZT) { \
    if (RDX) { xf[2 * (c)] = *(const v4u*)(smem + (c) * K1_XS + xrd0); xf[2 * (c) + 1] = *(const v4u*)(smem + (c) * K1_XS + xrd1); } \
    _Pragma("unroll") for (int i = 0; i < 8; ++i) { \
      const int k2 = i >> 2, n = i & 3; \
      if (RDW) { const char* sb = smem + (off) + (tb) + (((2 * n) ^ fsw) * 16) + k2 * (kstr); \
        s16x4 t0 = __builtin_amdgcn_ds_read_tr16_b64_v4i16((lds_s16x4_p)(sb)); \
        s16x4 t1 = __builtin_amdgcn_ds_read_tr16_b64_v4i16((lds_s16x4_p)(sb + 512)); \
        v2u a_ = __builtin_bit_cast(v2u, t0), b_ = __builtin_bit_cast(v2u, t1); \
        wf[b][i] = (v4u){a_[0], a_[1], b_[0], b_[1]}; } \
      if (MM) acc[n] = ZT ? mfma16(xf[2 * (cm) + k2], wf[(b) ^ 1][i], acc[n]) : mfma16(wf[(b) ^ 1][i], xf[2 * (cm) + k2], acc[n]); \
      SCHED(); } }

  BAR_N();
  STAMP();
  K1_STEP(1, 0, 1, 0, K1_WB, tra, 4096, 0, 0, 1)
  BAR_L();
  K1_STEP(1, 1, 1, 1, K1_WB + 8192, tra, 4096, 1, 0, 1)
  asm volatile("s_waitcnt vmcnt(2)" : K1_INREGS::"memory");
  unsigned nonce = 1;
  for (;;) {
    bool hit = pin[0] == nonce;
#pragma unroll
    for (int k = 0; k < 7; ++k) hit = hit || in[k][0] == nonce;
    if (!__any(hit)) break;
    ++nonce;
  }
  if (lane == 0) st4_sc1(nonce, EX_CH + cp * 128, ex);
  BAR_L();
  K1_STEP(1, 2, 1, 0, K1_WB + 2 * 8192, tra, 4096, 1, 1, 1)
  BAR_L();
  K1_STEP(1, 3, 1, 1, K1_WB + 3 * 8192, tra, 4096, 1, 2, 1)
  BAR_N();
  K1_STEP(1, 4, 1, 0, K1_WB + 4 * 8192, tra, 4096, 1, 3, 1)
  BAR_N();
  unsigned chal;
  ld4_sc1(chal, EX_CH + j * 128, ex);
  K1_STEP(1, 5, 1, 1, K1_WB + 5 * 8192, tra, 4096, 1, 4, 1)
  asm volatile("s_waitcnt vmcnt(2)\n\ts_barrier" : "+v"(xf[12]), "+v"(xf[13])::"memory");
  K1_STEP(0, 6, 1, 0, K1_WB + 6 * 8192, tra, 4096, 1, 5, 1)
  K1_STEP(0, 0, 0, 1, 0, trb, 8192, 1, 6, 1)
  STAMP();
  float v[4][4];
  unsigned myval;
  {
    float sh[4], S[4], Q[4];
#pragma unroll
    for (int n = 0; n < 4; ++n)
#pragma unroll
      for (int r = 0; r < 4; ++r) { v[n][r] = gelu_f(acc[n][r]); acc[n][r] = 0.f; }
#pragma unroll
    for (int r = 0; r < 4; ++r) {
      sh[r] = dpp_f<DPP_BC(0)>(v[0][r]);
      const float d0 = v[0][r] - sh[r], d1 = v[1][r] - sh[r], d2 = v[2][r] - sh[r], d3 = v[3][r] - sh[r];
      S[r] = (d0 + d1) + (d2 + d3);
      Q[r] = (d0 * d0 + d1 * d1) + (d2 * d2 + d3 * d3);
    }
    const bool b3 = j & 8, b2 = j & 4, b1 = j & 2;
    float T[4];
#pragma unroll
    for (int r = 0; r < 4; ++r) T[r] = (b3 ? Q[r] : S[r]) + dpp_f<DPP_XOR15>(b3 ? S[r] : Q[r]);
    const float U0 = (b2 ? T[2] : T[0]) + dpp_f<DPP_XOR7>(b2 ? T[0] : T[2]);
    const float U1 = (b2 ? T[3] : T[1]) + dpp_f<DPP_XOR7>(b2 ? T[1] : T[3]);
    float W = (b1 ? U1 : U0) + dpp_f<DPP_XOR3>(b1 ? U0 : U1);
    W += dpp_f<DPP_XOR1>(W);
    const float other = dpp_f<DPP_XOR8>(W);
    const float shr = b2 ? (b1 ? sh[3] : sh[2]) : (b1 ? sh[1] : sh[0]);
    myval = __builtin_bit_cast(unsigned, b3 ? W - other * other * (1.0f / 64.0f) : shr + W * (1.0f / 64.0f));
  }
#define K1_SEND(ans) { \
    _Pragma("unroll") for (int k = 0; k < 7; ++k) { const unsigned t_ = __shfl(ans, 2 * k + (j & 1)); st8_sc1((v2u){t_, myval}, out_vo, ex + k * 16384); } \
    if (lane < 16) st8_sc1((v2u){ans, nonce}, pout_vo, ex); }
  asm volatile("s_waitcnt vmcnt(0)" : "+v"(chal)::"memory");
  unsigned answered = chal;
  K1_SEND(answered)
  STAMP();
  BAR_N();
  STAMP();
  K1_STEP(0, 0, 1, 0, K1_WB + 7 * 8192, tra, 4096, 0, 0, 0)
  K1_STEP(0, 0, 1, 1, 0, trb, 8192, 1, 0, 0)
  K1_STEP(0, 0, 1, 0, 1024, trb, 8192, 1, 1, 0)
  K1_STEP(0, 0, 1, 1, K1_XS, trb, 8192, 1, 2, 0)
  K1_STEP(0, 0, 1, 0, K1_XS + 1024, trb, 8192, 1, 3, 0)
  K1_STEP(0, 0, 1, 1, 2 * K1_XS, trb, 8192, 1, 4, 0)
  K1_STEP(0, 0, 1, 0, 2 * K1_XS + 1024, trb, 8192, 1, 5, 0)
  K1_STEP(0, 0, 0, 1, 0, trb, 8192, 1, 6, 0)
  STAMP();
  K1_LDIN()
  ld4_sc1(chal, EX_CH + j * 128, ex);
  {
    char* z1 = ws + WS_Z1 + ((size_t)((8 * rp + wid) * 56 + cp * 4) * 64 + lane) * 8;
#pragma unroll
    for (int n = 0; n < 4; ++n) {
      v2u o;
      o[0] = pack_bf16(gelu_f(acc[n][0]), gelu_f(acc[n][1]));
      o[1] = pack_bf16(gelu_f(acc[n][2]), gelu_f(acc[n][3]));
      st_plain8(o, z1 + n * 512);
    }
  }
  STAMP();
  asm volatile("s_waitcnt vmcnt(4)" : K1_INREGS, "+v"(chal)::"memory");
  for (unsigned spin = 0; spin < (1u << 16); ++spin) {
    bool okd = true;
#pragma unroll
    for (int k = 0; k < 7; ++k) okd = okd && in[k][0] == nonce;
    const bool pv = pin[0] == nonce;
    const unsigned cur = pv ? pin[1] : chal;
    const bool live = j < 14;
    if (__any(live && cur != answered)) { answered = cur; K1_SEND(answered) }
    if (__all(okd && (pv || !live))) break;
    K1_LDIN()
    ld4_sc1(chal, EX_CH + j * 128, ex);
    asm volatile("s_waitcnt vmcnt(0)" : K1_INREGS, "+v"(chal)::"memory");
  }
  STAMP();
  {
    float sm = ((asf(in[0][1]) + asf(in[1][1])) + (asf(in[2][1]) + asf(in[3][1]))) + ((asf(in[4][1]) + asf(in[5][1])) + asf(in[6][1]));
    sm += dpp_f<DPP_XOR1>(sm);
    const float mean = sm * (1.0f / 14.0f);
    float bt = 0.f;
#pragma unroll
    for (int k = 0; k < 7; ++k) { const float e = asf(in[k][1]) - mean; bt = __builtin_fmaf(e, e, bt); }
    bt += dpp_f<DPP_XOR1>(bt);
    const float between = dpp_f<DPP_XOR8>(bt);
    const float rstd = rsqrtf((sm + 64.0f * between) * (1.0f / FH_) + 1e-5f);
    float mr[4], rr[4];
    mr[0] = dpp_f<DPP_BC(0)>(mean); mr[1] = dpp_f<DPP_BC(2)>(mean); mr[2] = dpp_f<DPP_BC(4)>(mean); mr[3] = dpp_f<DPP_BC(6)>(mean);
    rr[0] = dpp_f<DPP_BC(8)>(rstd); rr[1] = dpp_f<DPP_BC(10)>(rstd); rr[2] = dpp_f<DPP_BC(12)>(rstd); rr[3] = dpp_f<DPP_BC(14)>(rstd);
    char* z2 = ws + WS_Z2 + ((size_t)((4 * rp + (wid >> 1)) * 56 + cp * 4) * 64 + j + 16 * (2 * (wid & 1) + (g >> 1))) * 16 + (g & 1) * 8;
#pragma unroll
    for (int n = 0; n < 4; ++n) {
      v2u o;
      o[0] = pack_bf16((v[n][0] - mr[0]) * rr[0], (v[n][1] - mr[1]) * rr[1]);
      o[1] = pack_bf16((v[n][2] - mr[2]) * rr[2], (v[n][3] - mr[3]) * rr[3]);
      *(v2u*)(z2 + n * 1024) = o;
    }
  }
  STAMP(); STAMP_OUT(1);
}

#define K2_LDS (4 * 32768 + 1024)

template <int OFF>
__device__ __forceinline__ void gload_a(v4u& dst, unsigned voff, const void* sbase) {
  asm volatile("global_load_dwordx4 %0, %1, %2 offset:%3" : "=a"(dst) : "v"(voff), "s"(sbase), "i"(OFF) : "memory");
}

__global__ __launch_bounds__(256, 1) void k2_spatial(const unsigned short* __restrict__ wsp, const unsigned short* __restrict__ bsp,
                                                    char* __restrict__ ws) {
  extern __shared__ __attribute__((aligned(16))) char smem[];
  const int lane = threadIdx.x & 63;
  const int wid = __builtin_amdgcn_readfirstlane(threadIdx.x >> 6);
  const int xcd = blockIdx.x & 7, idx = blockIdx.x >> 3;
  const int tp = (xcd >> 1) * 8 + (idx & 7);
  const int fp = (xcd & 1) * 4 + (idx >> 3);
  const int g = lane >> 4;

  STAMP_DECL STAMP();
  v2u z1v[7];
  {
    const v2u* z1 = (const v2u*)(ws + WS_Z1) + ((size_t)((4 * tp + wid) * 56 + fp * 7)) * 64 + lane;
#pragma unroll
    for (int n = 0; n < 7; ++n) z1v[n] = z1[n * 64];
  }
  const float bias = __builtin_bit_cast(float, (unsigned)bsp[tp * 64 + wid * 16 + (lane & 15)] << 16);

  const char* abase = (const char*)(wsp + (size_t)(tp * 64) * S_ + wid * 512);
  const unsigned avo0 = (lane >> 3) * (S_ * 2) + (((lane & 7) ^ g) * 16);
  const unsigned avo1 = (lane >> 3) * (S_ * 2) + (((lane & 7) ^ (g + 4)) * 16);
  const unsigned ring = lds_addr(smem) + 1024 + wid * 32768;
  const char* stg = smem + 1024 + wid * 32768;
  const char* bbase = ws + WS_Z2 + ((size_t)(wid * 16) * 56 + fp * 7 + 3) * 1024;
  const unsigned bvo = lane * 16;
  const int rd0 = (lane & 15) * 128 + ((g ^ ((lane & 15) >> 1)) * 16);
  const int rd1 = rd0 ^ 64;

  v4u Bf[2][14];
  v4u Af[2][8];
  f32x4 acc[4][7];
#pragma unroll
  for (int m = 0; m < 4; ++m)
#pragma unroll
    for (int n = 0; n < 7; ++n) acc[m][n] = (f32x4){0.f, 0.f, 0.f, 0.f};

#define LOAD_A1(j, i) glds16o<(j) * 128>(((i) & 1) ? avo1 : avo0, abase + (size_t)(i) * 8 * (S_ * 2), ring + ((j) & 3) * 8192 + (i) * 1024);
#define LOAD_A(j) LOAD_A1(j, 0) LOAD_A1(j, 1) LOAD_A1(j, 2) LOAD_A1(j, 3) LOAD_A1(j, 4) LOAD_A1(j, 5) LOAD_A1(j, 6) LOAD_A1(j, 7)
#define LOAD_B1(j, ks, n) gload_a<((n) - 3) * 1024>(Bf[(j) & 1][(ks) * 7 + (n)], bvo, bbase + (size_t)(2 * (j) + (ks)) * 56 * 1024);
#define LOAD_Bk(j, ks) LOAD_B1(j, ks, 0) LOAD_B1(j, ks, 1) LOAD_B1(j, ks, 2) LOAD_B1(j, ks, 3) LOAD_B1(j, ks, 4) LOAD_B1(j, ks, 5) LOAD_B1(j, ks, 6)
#define LOAD_B(j) LOAD_Bk(j, 0) LOAD_Bk(j, 1)
#define READ_A(j) _Pragma("unroll") for (int m = 0; m < 4; ++m) _Pragma("unroll") for (int ks = 0; ks < 2; ++ks) \
    Af[(j) & 1][m * 2 + ks] = *(const v4u*)(stg + ((j) & 3) * 8192 + ((m + wid) & 3) * 2048 + (ks ? rd1 : rd0));
#define WAIT_B(N, P) asm volatile("s_waitcnt vmcnt(" #N ")" : "+a"(Bf[P][0]), "+a"(Bf[P][1]), "+a"(Bf[P][2]), "+a"(Bf[P][3]), "+a"(Bf[P][4]), \
    "+a"(Bf[P][5]), "+a"(Bf[P][6]), "+a"(Bf[P][7]), "+a"(Bf[P][8]), "+a"(Bf[P][9]), "+a"(Bf[P][10]), "+a"(Bf[P][11]), "+a"(Bf[P][12]), "+a"(Bf[P][13]) :: "memory");
#define MMA(j, ks, n) _Pragma("unroll") for (int m = 0; m < 4; ++m) acc[m][n] = mfma16(Bf[(j) & 1][(ks) * 7 + (n)], Af[(j) & 1][m * 2 + (ks)], acc[m][n]);
#define STEP_BODY(j, LB, LA) \
  MMA(j, 0, 0) SCHED(); if (LB) { LOAD_B1((j) + 2, 0, 0) } if (LA) { LOAD_A1((j) + 3, 0) } \
  MMA(j, 0, 1) SCHED(); if (LB) { LOAD_B1((j) + 2, 0, 1) } if (LA) { LOAD_A1((j) + 3, 1) } \
  MMA(j, 0, 2) SCHED(); if (LB) { LOAD_B1((j) + 2, 0, 2) } if (LA) { LOAD_A1((j) + 3, 2) } \
  MMA(j, 0, 3) SCHED(); if (LB) { LOAD_B1((j) + 2, 0, 3) } if (LA) { LOAD_A1((j) + 3, 3) } \
  MMA(j, 0, 4) SCHED(); if (LB) { LOAD_B1((j) + 2, 0, 4) } if (LA) { LOAD_A1((j) + 3, 4) } \
  MMA(j, 0, 5) SCHED(); if (LB) { LOAD_B1((j) + 2, 0, 5) } if (LA) { LOAD_A1((j) + 3, 5) } \
  MMA(j, 0, 6) SCHED(); if (LB) { LOAD_B1((j) + 2, 0, 6) } if (LA) { LOAD_A1((j) + 3, 6) } \
  MMA(j, 1, 0) SCHED(); if (LB) { LOAD_B1((j) + 2, 1, 0) } if (LA) { LOAD_A1((j) + 3, 7) } \
  MMA(j, 1, 1) SCHED(); if (LB) { LOAD_B1((j) + 2, 1, 1) } \
  MMA(j, 1, 2) SCHED(); if (LB) { LOAD_B1((j) + 2, 1, 2) } \
  MMA(j, 1, 3) SCHED(); if (LB) { LOAD_B1((j) + 2, 1, 3) } \
  MMA(j, 1, 4) SCHED(); if (LB) { LOAD_B1((j) + 2, 1, 4) } \
  MMA(j, 1, 5) SCHED(); if (LB) { LOAD_B1((j) + 2, 1, 5) } \
  MMA(j, 1, 6) SCHED(); if (LB) { LOAD_B1((j) + 2, 1, 6) } \
  SCHED();

  LOAD_A(0) LOAD_B(0) LOAD_A(1)
  LOAD_B(1) LOAD_A(2)
  STAMP();
  asm volatile("s_waitcnt vmcnt(44)" ::: "memory");
  STAMP();
  READ_A(0)
  SCHED();
  WAIT_B(22, 0) STAMP(); READ_A(1) SCHED(); STEP_BODY(0, 1, 1)
  WAIT_B(22, 1) STAMP(); READ_A(2) SCHED(); STEP_BODY(1, 1, 1)
  WAIT_B(22, 0) STAMP(); READ_A(3) SCHED(); STEP_BODY(2, 1, 1)
  WAIT_B(22, 1) STAMP(); READ_A(4) SCHED(); STEP_BODY(3, 1, 1)
  WAIT_B(22, 0) STAMP(); READ_A(5) SCHED(); STEP_BODY(4, 1, 1)
  WAIT_B(22, 1) STAMP(); READ_A(6) SCHED(); STEP_BODY(5, 1, 0)
  WAIT_B(14, 0) STAMP(); READ_A(7) SCHED(); STEP_BODY(6, 0, 0)
  WAIT_B(0, 1) STAMP(); SCHED();
  {
    char* mine = smem + 1024 + wid * 32768 + lane * 16;
#pragma unroll
    for (int mi = 1; mi <= 4; ++mi) {
      const int m = mi & 3;
#pragma unroll
      for (int ks = 0; ks < 2; ++ks)
#pragma unroll
        for (int n = 0; n < 7; ++n) acc[m][n] = mfma16(Bf[1][ks * 7 + n], Af[1][m * 2 + ks], acc[m][n]);
      SCHED();
      if (mi >= 2) {
#pragma unroll
        for (int n = 0; n < 7; ++n) *(f32x4*)(mine + ((mi - 1) * 7 + n) * 1024) = acc[mi - 1][n];
      }
      SCHED();
    }
  }
  STAMP();
  __syncthreads();
  STAMP();
  f32x4 o[7];
  {
    f32x4 pr[3][7];
#pragma unroll
    for (int d = 1; d < 4; ++d) {
      const char* src = smem + 1024 + ((wid - d) & 3) * 32768 + d * 7168 + lane * 16;
#pragma unroll
      for (int n = 0; n < 7; ++n) pr[d - 1][n] = *(const f32x4*)(src + n * 1024);
    }
    SCHED();
#pragma unroll
    for (int n = 0; n < 7; ++n) o[n] = (acc[0][n] + pr[0][n]) + (pr[1][n] + pr[2][n]);
  }
  char* hp = ws + WS_H + ((size_t)(4 * tp + wid) * 28 * 64 + (lane & 15) + 16 * (g >> 1)) * 16 + (g & 1) * 8;
#pragma unroll
  for (int n = 0; n < 7; ++n) {
    const int ft = fp * 7 + n;
    v2u h;
    h[0] = pack_bf16(bf_lo(z1v[n][0]) * (o[n][0] + bias), bf_hi(z1v[n][0]) * (o[n][1] + bias));
    h[1] = pack_bf16(bf_lo(z1v[n][1]) * (o[n][2] + bias), bf_hi(z1v[n][1]) * (o[n][3] + bias));
    *(v2u*)(hp + (ft >> 1) * 1024 + (ft & 1) * 512) = h;
  }
  STAMP(); STAMP_OUT(2);
}

#define K3_LDS (4 * 12 * 1024)
__global__ __launch_bounds__(256, 1) void k3_out(char* __restrict__ ws, unsigned short* __restrict__ out) {
  extern __shared__ __attribute__((aligned(16))) char smem[];
  const int lane = threadIdx.x & 63;
  const int wid = __builtin_amdgcn_readfirstlane(threadIdx.x >> 6);
  const int xcd = blockIdx.x & 7, idx = blockIdx.x >> 3;
  const int tp = xcd * 4 + (idx & 3);
  const int dp = idx >> 2;
  const int g = lane >> 4;
  STAMP_DECL STAMP();
  const v4u* hp = (const v4u*)(ws + WS_H) + ((size_t)(4 * tp) * 28 + wid * 7) * 64 + lane;
  const v4u* wp = (const v4u*)(ws + WS_WO) + ((size_t)(wid * 7) * 28 + dp * 4) * 64 + lane;
  v4u Hf[7][4], Wf[7][4];
#define K3_LD(k) { _Pragma("unroll") for (int n = 0; n < 4; ++n) Wf[k][n] = wp[((k) * 28 + n) * 64]; \
    _Pragma("unroll") for (int m = 0; m < 4; ++m) Hf[k][m] = hp[(((m + wid) & 3) * 28 + (k)) * 64]; SCHED(); }
#define K3_MM(k) { _Pragma("unroll") for (int m = 0; m < 4; ++m) _Pragma("unroll") for (int n = 0; n < 4; ++n) \
    acc[m][n] = mfma16(Wf[k][n], Hf[k][m], acc[m][n]); SCHED(); }
  f32x4 acc[4][4];
#pragma unroll
  for (int m = 0; m < 4; ++m)
#pragma unroll
    for (int n = 0; n < 4; ++n) acc[m][n] = (f32x4){0.f, 0.f, 0.f, 0.f};
  K3_LD(0) K3_LD(1) K3_LD(2) K3_LD(3)
  STAMP();
  K3_MM(0) K3_LD(4) K3_MM(1) K3_LD(5) K3_MM(2) K3_LD(6) K3_MM(3) K3_MM(4) K3_MM(5) K3_MM(6)
  STAMP();
#pragma unroll
  for (int m = 1; m < 4; ++m)
#pragma unroll
    for (int n = 0; n < 4; ++n) *(f32x4*)(smem + ((wid * 3 + m - 1) * 4 + n) * 1024 + lane * 16) = acc[m][n];
  __syncthreads();
  STAMP();
  f32x4 pr[3][4];
#pragma unroll
  for (int d = 1; d < 4; ++d)
#pragma unroll
    for (int n = 0; n < 4; ++n) pr[d - 1][n] = *(const f32x4*)(smem + ((((wid - d) & 3) * 3 + d - 1) * 4 + n) * 1024 + lane * 16);
  unsigned short* op = out + (size_t)(tp * 64 + wid * 16 + (lane & 15)) * D_ + dp * 64 + 4 * g;
#pragma unroll
  for (int n = 0; n < 4; ++n) {
    const f32x4 o = (acc[0][n] + pr[0][n]) + (pr[1][n] + pr[2][n]);
    v2u h;
    h[0] = pack_bf16(o[0], o[1]);
    h[1] = pack_bf16(o[2], o[3]);
    *(v2u*)(op + n * 16) = h;
  }
  STAMP(); STAMP_OUT(0);
}

extern "C" void kernel_launch(void* const* d_in, const int* in_sizes, int n_in,
                              void* d_out, int out_size, void* d_ws, size_t ws_size,
                              hipStream_t stream) {
  const unsigned short* x = (const unsigned short*)d_in[0];
  const unsigned short* win = (const unsigned short*)d_in[1];
  const unsigned short* wsp = (const unsigned short*)d_in[2];
  const unsigned short* bsp = (const unsigned short*)d_in[3];
  const unsigned short* wout = (const unsigned short*)d_in[4];
  char* ws = (char*)d_ws;
  (void)hipFuncSetAttribute((const void*)k1_gemm_gelu, hipFuncAttributeMaxDynamicSharedMemorySize, K1_LDS);
  (void)hipFuncSetAttribute((const void*)k2_spatial, hipFuncAttributeMaxDynamicSharedMemorySize, K2_LDS);
  (void)hipFuncSetAttribute((const void*)k3_out, hipFuncAttributeMaxDynamicSharedMemorySize, K3_LDS);
  k1_gemm_gelu<<<256, 640, K1_LDS, stream>>>(x, win, wsp, wout, ws);
  k2_spatial<<<256, 256, K2_LDS, stream>>>(wsp, bsp, ws);
  k3_out<<<224, 256, K3_LDS, stream>>>(ws, (unsigned short*)d_out);
}
```

```cpp
#include <hip/hip_runtime.h>
#include <hip/hip_bf16.h>
#include <stdint.h>

typedef __attribute__((ext_vector_type(8))) short bf16x8;
typedef __attribute__((ext_vector_type(4))) short s16x4;
typedef __attribute__((ext_vector_type(4))) float f32x4;
typedef __attribute__((ext_vector_type(4))) unsigned v4u;
typedef __attribute__((ext_vector_type(2))) unsigned v2u;
typedef __attribute__((address_space(3))) s16x4* lds_s16x4_p;

#define S_ 2048
#define D_ 448
#define F_ 1792
#define FH_ 896

#define WS_Z1 0
#define WS_Z2 (WS_Z1 + 3670016)
#define WS_H  (WS_Z2 + 3670016)
#define WS_WO (WS_H + 3670016)

#define SCHED() __builtin_amdgcn_sched_barrier(0)
#define STAMP_DECL
#define STAMP()
#define STAMP_OUT(kid)

__device__ __forceinline__ f32x4 mfma16(v4u a, v4u b, f32x4 c) {
  return __builtin_amdgcn_mfma_f32_16x16x32_bf16(__builtin_bit_cast(bf16x8, a), __builtin_bit_cast(bf16x8, b), c, 0, 0, 0);
}
typedef __attribute__((ext_vector_type(2))) float f32x2;
typedef __attribute__((ext_vector_type(2))) __bf16 bf16x2;
__device__ __forceinline__ unsigned pack_bf16(float lo, float hi) {
  const bf16x2 r = __builtin_convertvector((f32x2){lo, hi}, bf16x2);
  return __builtin_bit_cast(unsigned, r);
}
__device__ __forceinline__ float asf(unsigned u) { return __builtin_bit_cast(float, u); }
__device__ __forceinline__ float bf_lo(unsigned u) { return __builtin_bit_cast(float, u << 16); }
__device__ __forceinline__ float bf_hi(unsigned u) { return __builtin_bit_cast(float, u & 0xffff0000u); }

__device__ __forceinline__ void glds16(unsigned voff, const void* sbase, unsigned lds) {
  asm volatile("s_mov_b32 m0, %2\n\ts_nop 0\n\tglobal_load_lds_dwordx4 %0, %1" ::"v"(voff), "s"(sbase), "s"(lds) : "memory");
}
template <int OFF>
__device__ __forceinline__ void glds16o(unsigned voff, const void* sbase, unsigned lds) {
  asm volatile("s_mov_b32 m0, %2\n\ts_nop 0\n\tglobal_load_lds_dwordx4 %0, %1 offset:%3" ::"v"(voff), "s"(sbase), "s"(lds - OFF), "i"(OFF) : "memory");
}
__device__ __forceinline__ unsigned lds_addr(const void* p) { return (unsigned)(size_t)(__attribute__((address_space(3))) const char*)p; }

__device__ __forceinline__ void touch(unsigned& dst, const void* p) { asm volatile("global_load_dword %0, %1, off" : "+v"(dst) : "v"(p) : "memory"); }
__device__ __forceinline__ void st_plain8(v2u d, void* p) { asm volatile("global_store_dwordx2 %0, %1, off" ::"v"(p), "v"(d) : "memory"); }

__device__ __forceinline__ float gelu_f(float x) {
  const float c0 = -2.0f * 0.7978845608028654f * 1.4426950408889634f;
  const float c1 = c0 * 0.044715f;
  float t = __builtin_fmaf(x * x, c1, c0);
  float e = __builtin_amdgcn_exp2f(x * t);
  return x * __builtin_amdgcn_rcpf(1.0f + e);
}

#define K1_XS 16384
#define K1_WB (6 * K1_XS)
#define K1_LDS (K1_WB + 8 * 8192)
#define WAITBAR(N) asm volatile("s_waitcnt vmcnt(" #N ") lgkmcnt(0)\n\ts_barrier" ::: "memory")
#define WS_EX (WS_WO + 802816)
#define EX_CH 131072
#define EX_GRP (EX_CH + 2048)

__device__ __forceinline__ void ld8_sc1(v2u& d, unsigned voff, const void* sb) { asm volatile("global_load_dwordx2 %0, %1, %2 sc1" : "=v"(d) : "v"(voff), "s"(sb) : "memory"); }
__device__ __forceinline__ void ld4_sc1(unsigned& d, unsigned voff, const void* sb) { asm volatile("global_load_dword %0, %1, %2 sc1" : "=v"(d) : "v"(voff), "s"(sb) : "memory"); }
__device__ __forceinline__ void st8_sc1(v2u d, unsigned voff, const void* sb) { asm volatile("global_store_dwordx2 %0, %1, %2 sc1" ::"v"(voff), "v"(d), "s"(sb) : "memory"); }
__device__ __forceinline__ void st4_sc1(unsigned d, unsigned voff, const void* sb) { asm volatile("global_store_dword %0, %1, %2 sc1" ::"v"(voff), "v"(d), "s"(sb) : "memory"); }
template <int CTRL>
__device__ __forceinline__ float dpp_f(float v) {
  return __builtin_bit_cast(float, __builtin_amdgcn_update_dpp(0, __builtin_bit_cast(int, v), CTRL, 0xf, 0xf, true));
}
#define DPP_XOR1 0xB1
#define DPP_XOR3 0x1B
#define DPP_XOR7 0x141
#define DPP_XOR15 0x140
#define DPP_XOR8 0x128
#define DPP_BC(n) (0x150 + (n))

__global__ __launch_bounds__(640, 3) void k1_gemm_gelu(const unsigned short* __restrict__ x, const unsigned short* __restrict__ win,
                                                      const unsigned short* __restrict__ wsp, const unsigned short* __restrict__ wout,
                                                      char* __restrict__ ws, const char* __restrict__ outp) {
  extern __shared__ __attribute__((aligned(16))) char smem[];
  const int lane = threadIdx.x & 63;
  const int wid = __builtin_amdgcn_readfirstlane(threadIdx.x >> 6);
  const int xcd = blockIdx.x & 7, idx = blockIdx.x >> 3;
  const int g = lane >> 4, j = lane & 15;
  if (idx >= 28) {
    if (wid >= 8) return;
    STAMP_DECL STAMP();
    const int hw = ((idx - 28) * 8 + xcd) * 8 + wid;
    const char* pb = (const char*)wsp + ((size_t)((xcd >> 1) * 512 + ((xcd & 1) * 4 + idx - 28) * 64 + wid * 8) * S_) * 2;
    unsigned tdst = 0;
    if (idx == 29) {
      const char* b = wid < 3 ? ws + wid * (64u << 16) : wid < 5 ? (const char*)wsp + (wid - 3) * (64u << 16) : outp;
      const unsigned off = wid < 5 ? lane << 16 : min(lane, 27) << 16;
      asm volatile("global_load_dword %0, %1, %2" : "+v"(tdst) : "v"(off), "s"(b) : "memory");
    }
    if (idx == 28 && wid < 4) {
      unsigned long long pc; int d0, len;
      asm volatile("s_getpc_b64 %0\n.Lpf_here_%=:\n\ts_mov_b32 %1, _Z12k1_gemm_geluPKtS0_S0_S0_PcPKc-.Lpf_here_%=\n\t"
                   "s_mov_b32 %2, _Z6k3_outPcPt-_Z12k1_gemm_geluPKtS0_S0_S0_PcPKc" : "=s"(pc), "=s"(d0), "=s"(len));
      const unsigned off = min((unsigned)(wid * 8192 + lane * 128), (unsigned)(len + 2560));
      asm volatile("global_load_dword %0, %1, %2" : "+v"(tdst) : "v"(off), "s"(pc + (long long)d0) : "memory");
    }
#pragma unroll
    for (int i = 0; i < 4; ++i)
      asm volatile("global_load_dword %0, %1, %2" : "+v"(tdst) : "v"(lane * 128), "s"(pb + i * 8192) : "memory");
    STAMP();
    unsigned short v[4][8];
#pragma unroll
    for (int i = 0; i < 4; ++i) {
      const int b = min(hw + 256 * i, 783), kf = b / 28, dt = b % 28;
#pragma unroll
      for (int e = 0; e < 8; ++e) v[i][e] = wout[(size_t)(32 * kf + 8 * g + e) * D_ + 16 * dt + j];
    }
#pragma unroll
    for (int i = 0; i < 4; ++i) {
      const int b = min(hw + 256 * i, 783);
      v4u o;
#pragma unroll
      for (int q = 0; q < 4; ++q) o[q] = (unsigned)v[i][2 * q] | ((unsigned)v[i][2 * q + 1] << 16);
      *(v4u*)(ws + WS_WO + ((size_t)b * 64 + lane) * 16) = o;
    }
    asm volatile("s_waitcnt vmcnt(0)" : "+v"(tdst)::"memory");
    STAMP(); STAMP_OUT(3);
    return;
  }
  const int rp = (xcd >> 1) * 4 + (idx & 3);
  const int cp = (xcd & 1) * 7 + (idx >> 2);

  const unsigned sm0 = lds_addr(smem);
#define BAR_L() asm volatile("s_waitcnt lgkmcnt(0)\n\ts_barrier" ::: "memory")
#define BAR_N() asm volatile("s_barrier" ::: "memory")
#define WAITBAR_V(N) asm volatile("s_waitcnt vmcnt(" #N ")\n\ts_barrier" ::: "memory")
  if (wid >= 8) {
    const int w0 = (wid - 8) * 4;
    const char* xb0 = (const char*)(x + (size_t)(rp * 128 + w0 * 16) * D_);
    const unsigned xvo0 = (lane >> 3) * (D_ * 2) + (((lane & 7) ^ g) * 16);
    const unsigned xvo1 = (lane >> 3) * (D_ * 2) + (((lane & 7) ^ (g + 4)) * 16) + 8 * D_ * 2;
    const char* w1 = (const char*)(win + cp * 64 + (size_t)(w0 * 8) * F_);
    const char* w2 = w1 + FH_ * 2;
    const unsigned wvoa = (lane >> 3) * (F_ * 2) + (((lane & 7) ^ (2 * (g & 1))) * 16);
    const unsigned wvob = wvoa ^ 64;
    const unsigned xl = sm0 + w0 * 2048, wl = sm0 + K1_WB + w0 * 1024;
#define K1_WL(base, c, dst, dstr) { _Pragma("unroll") for (int q = 0; q < 4; ++q) \
      glds16((q & 1) ? wvob : wvoa, (base) + (size_t)((c) * 64 + q * 8) * (F_ * 2), (dst) + q * (dstr)); }
#define K1_C(c) { _Pragma("unroll") for (int q = 0; q < 4; ++q) { \
      glds16(xvo0, xb0 + q * 16 * D_ * 2 + (c) * 128, xl + (c) * K1_XS + q * 2048); \
      glds16(xvo1, xb0 + q * 16 * D_ * 2 + (c) * 128, xl + (c) * K1_XS + q * 2048 + 1024); } \
      K1_WL(w2, c, wl + (c) * 8192, 1024) }
    K1_C(0) K1_C(1) K1_C(2)
    WAITBAR_V(24);
    K1_C(3)
    WAITBAR_V(24);
    K1_C(4)
    WAITBAR_V(24);
    K1_C(5)
    WAITBAR_V(24);
    K1_WL(w2, 6, wl + 6 * 8192, 1024) K1_WL(w1, 0, wl + 7 * 8192, 1024) K1_WL(w1, 1, xl, 2048)
    WAITBAR_V(24);
    K1_WL(w1, 2, xl + 1024, 2048) K1_WL(w1, 3, xl + K1_XS, 2048) K1_WL(w1, 4, xl + K1_XS + 1024, 2048)
    WAITBAR_V(24);
    K1_WL(w1, 5, xl + 2 * K1_XS, 2048) K1_WL(w1, 6, xl + 2 * K1_XS + 1024, 2048)
    WAITBAR_V(28);
    WAITBAR_V(0);
    return;
  }
  STAMP_DECL STAMP();
  const char* ex = ws + WS_EX + (size_t)(rp * 8 + wid) * EX_GRP;
  const unsigned gr_off = (g * 8 + (j >> 1)) * 8;
  const unsigned in_vo = (cp * 16 + (j & 1)) * 512 + gr_off;
  const unsigned out_vo = ((j & 1) * 16 + cp) * 512 + gr_off;
  const unsigned pin_vo = (cp * 16 + j) * 512 + 256;
  const unsigned pout_vo = (j * 16 + cp) * 512 + 256;
  v2u in[7], pin;
#define K1_LDIN() { _Pragma("unroll") for (int k = 0; k < 7; ++k) ld8_sc1(in[k], in_vo, ex + k * 1024); ld8_sc1(pin, pin_vo, ex); }
#define K1_INREGS "+v"(in[0]), "+v"(in[1]), "+v"(in[2]), "+v"(in[3]), "+v"(in[4]), "+v"(in[5]), "+v"(in[6]), "+v"(pin)
  v4u xf[14];
  K1_LDIN()
  asm volatile("global_load_dwordx4 %0, %2, %3 offset:768\n\tglobal_load_dwordx4 %1, %2, %3 offset:832"
               : "=&v"(xf[12]), "=&v"(xf[13]) : "v"(j * (D_ * 2) + g * 16), "s"((const char*)(x + (size_t)(rp * 128 + wid * 16) * D_)) : "memory");
  STAMP();
  const int xrd0 = wid * 2048 + j * 128 + ((g ^ (j >> 1)) * 16), xrd1 = xrd0 ^ 64;
  const int fsw = 2 * (((j >> 3) & 1) + 2 * (g & 1));
  const int tra = g * 1024 + (j >> 2) * 128 + (((j >> 1) & 1) * 16) + 8 * (j & 1);
  const int trb = g * 2048 + (j >> 2) * 128 + (((j >> 1) & 1) * 16) + 8 * (j & 1);
  v4u wf[2][8];
  f32x4 acc[4];
#pragma unroll
  for (int n = 0; n < 4; ++n) acc[n] = (f32x4){0.f, 0.f, 0.f, 0.f};
#define K1_STEP(RDX, c, RDW, b, off, tb, kstr, MM, cm, ZT) { \
    if (RDX) { xf[2 * (c)] = *(const v4u*)(smem + (c) * K1_XS + xrd0); xf[2 * (c) + 1] = *(const v4u*)(smem + (c) * K1_XS + xrd1); } \
    _Pragma("unroll") for (int i = 0; i < 8; ++i) { \
      const int k2 = i >> 2, n = i & 3; \
      if (RDW) { const char* sb = smem + (off) + (tb) + (((2 * n) ^ fsw) * 16) + k2 * (kstr); \
        s16x4 t0 = __builtin_amdgcn_ds_read_tr16_b64_v4i16((lds_s16x4_p)(sb)); \
        s16x4 t1 = __builtin_amdgcn_ds_read_tr16_b64_v4i16((lds_s16x4_p)(sb + 512)); \
        v2u a_ = __builtin_bit_cast(v2u, t0), b_ = __builtin_bit_cast(v2u, t1); \
        wf[b][i] = (v4u){a_[0], a_[1], b_[0], b_[1]}; } \
      if (MM) acc[n] = ZT ? mfma16(xf[2 * (cm) + k2], wf[(b) ^ 1][i], acc[n]) : mfma16(wf[(b) ^ 1][i], xf[2 * (cm) + k2], acc[n]); \
      SCHED(); } }

  BAR_N();
  STAMP();
  K1_STEP(1, 0, 1, 0, K1_WB, tra, 4096, 0, 0, 1)
  BAR_L();
  K1_STEP(1, 1, 1, 1, K1_WB + 8192, tra, 4096, 1, 0, 1)
  asm volatile("s_waitcnt vmcnt(2)" : K1_INREGS::"memory");
  unsigned nonce = 1;
  for (;;) {
    bool hit = pin[0] == nonce;
#pragma unroll
    for (int k = 0; k < 7; ++k) hit = hit || in[k][0] == nonce;
    if (!__any(hit)) break;
    ++nonce;
  }
  if (lane == 0) st4_sc1(nonce, EX_CH + cp * 128, ex);
  BAR_L();
  K1_STEP(1, 2, 1, 0, K1_WB + 2 * 8192, tra, 4096, 1, 1, 1)
  BAR_L();
  K1_STEP(1, 3, 1, 1, K1_WB + 3 * 8192, tra, 4096, 1, 2, 1)
  BAR_N();
  K1_STEP(1, 4, 1, 0, K1_WB + 4 * 8192, tra, 4096, 1, 3, 1)
  BAR_N();
  unsigned chal;
  ld4_sc1(chal, EX_CH + j * 128, ex);
  K1_STEP(1, 5, 1, 1, K1_WB + 5 * 8192, tra, 4096, 1, 4, 1)
  asm volatile("s_waitcnt vmcnt(2)\n\ts_barrier" : "+v"(xf[12]), "+v"(xf[13])::"memory");
  K1_STEP(0, 6, 1, 0, K1_WB + 6 * 8192, tra, 4096, 1, 5, 1)
  K1_STEP(0, 0, 0, 1, 0, trb, 8192, 1, 6, 1)
  STAMP();
  float v[4][4];
  unsigned myval;
  {
    float sh[4], S[4], Q[4];
#pragma unroll
    for (int n = 0; n < 4; ++n)
#pragma unroll
      for (int r = 0; r < 4; ++r) { v[n][r] = gelu_f(acc[n][r]); acc[n][r] = 0.f; }
#pragma unroll
    for (int r = 0; r < 4; ++r) {
      sh[r] = dpp_f<DPP_BC(0)>(v[0][r]);
      const float d0 = v[0][r] - sh[r], d1 = v[1][r] - sh[r], d2 = v[2][r] - sh[r], d3 = v[3][r] - sh[r];
      S[r] = (d0 + d1) + (d2 + d3);
      Q[r] = (d0 * d0 + d1 * d1) + (d2 * d2 + d3 * d3);
    }
    const bool b3 = j & 8, b2 = j & 4, b1 = j & 2;
    float T[4];
#pragma unroll
    for (int r = 0; r < 4; ++r) T[r] = (b3 ? Q[r] : S[r]) + dpp_f<DPP_XOR15>(b3 ? S[r] : Q[r]);
    const float U0 = (b2 ? T[2] : T[0]) + dpp_f<DPP_XOR7>(b2 ? T[0] : T[2]);
    const float U1 = (b2 ? T[3] : T[1]) + dpp_f<DPP_XOR7>(b2 ? T[1] : T[3]);
    float W = (b1 ? U1 : U0) + dpp_f<DPP_XOR3>(b1 ? U0 : U1);
    W += dpp_f<DPP_XOR1>(W);
    const float other = dpp_f<DPP_XOR8>(W);
    const float shr = b2 ? (b1 ? sh[3] : sh[2]) : (b1 ? sh[1] : sh[0]);
    myval = __builtin_bit_cast(unsigned, b3 ? W - other * other * (1.0f / 64.0f) : shr + W * (1.0f / 64.0f));
  }
#define K1_SEND(ans) { \
    _Pragma("unroll") for (int k = 0; k < 7; ++k) { const unsigned t_ = __shfl(ans, 2 * k + (j & 1)); st8_sc1((v2u){t_, myval}, out_vo, ex + k * 16384); } \
    if (lane < 16) st8_sc1((v2u){ans, nonce}, pout_vo, ex); }
  asm volatile("s_waitcnt vmcnt(0)" : "+v"(chal)::"memory");
  unsigned answered = chal;
  K1_SEND(answered)
  STAMP();
  BAR_N();
  STAMP();
  K1_STEP(0, 0, 1, 0, K1_WB + 7 * 8192, tra, 4096, 0, 0, 0)
  K1_STEP(0, 0, 1, 1, 0, trb, 8192, 1, 0, 0)
  K1_STEP(0, 0, 1, 0, 1024, trb, 8192, 1, 1, 0)
  K1_STEP(0, 0, 1, 1, K1_XS, trb, 8192, 1, 2, 0)
  K1_STEP(0, 0, 1, 0, K1_XS + 1024, trb, 8192, 1, 3, 0)
  K1_STEP(0, 0, 1, 1, 2 * K1_XS, trb, 8192, 1, 4, 0)
  K1_STEP(0, 0, 1, 0, 2 * K1_XS + 1024, trb, 8192, 1, 5, 0)
  K1_STEP(0, 0, 0, 1, 0, trb, 8192, 1, 6, 0)
  STAMP();
  K1_LDIN()
  ld4_sc1(chal, EX_CH + j * 128, ex);
  {
    char* z1 = ws + WS_Z1 + ((size_t)((8 * rp + wid) * 56 + cp * 4) * 64 + lane) * 8;
#pragma unroll
    for (int n = 0; n < 4; ++n) {
      v2u o;
      o[0] = pack_bf16(gelu_f(acc[n][0]), gelu_f(acc[n][1]));
      o[1] = pack_bf16(gelu_f(acc[n][2]), gelu_f(acc[n][3]));
      st_plain8(o, z1 + n * 512);
    }
  }
  STAMP();
  asm volatile("s_waitcnt vmcnt(4)" : K1_INREGS, "+v"(chal)::"memory");
  for (unsigned spin = 0; spin < (1u << 16); ++spin) {
    bool okd = true;
#pragma unroll
    for (int k = 0; k < 7; ++k) okd = okd && in[k][0] == nonce;
    const bool pv = pin[0] == nonce;
    const unsigned cur = pv ? pin[1] : chal;
    const bool live = j < 14;
    if (__any(live && cur != answered)) { answered = cur; K1_SEND(answered) }
    if (__all(okd && (pv || !live))) break;
    K1_LDIN()
    ld4_sc1(chal, EX_CH + j * 128, ex);
    asm volatile("s_waitcnt vmcnt(0)" : K1_INREGS, "+v"(chal)::"memory");
  }
  STAMP();
  {
    float sm = ((asf(in[0][1]) + asf(in[1][1])) + (asf(in[2][1]) + asf(in[3][1]))) + ((asf(in[4][1]) + asf(in[5][1])) + asf(in[6][1]));
    sm += dpp_f<DPP_XOR1>(sm);
    const float mean = sm * (1.0f / 14.0f);
    float bt = 0.f;
#pragma unroll
    for (int k = 0; k < 7; ++k) { const float e = asf(in[k][1]) - mean; bt = __builtin_fmaf(e, e, bt); }
    bt += dpp_f<DPP_XOR1>(bt);
    const float between = dpp_f<DPP_XOR8>(bt);
    const float rstd = rsqrtf((sm + 64.0f * between) * (1.0f / FH_) + 1e-5f);
    float mr[4], rr[4];
    mr[0] = dpp_f<DPP_BC(0)>(mean); mr[1] = dpp_f<DPP_BC(2)>(mean); mr[2] = dpp_f<DPP_BC(4)>(mean); mr[3] = dpp_f<DPP_BC(6)>(mean);
    rr[0] = dpp_f<DPP_BC(8)>(rstd); rr[1] = dpp_f<DPP_BC(10)>(rstd); rr[2] = dpp_f<DPP_BC(12)>(rstd); rr[3] = dpp_f<DPP_BC(14)>(rstd);
    char* z2 = ws + WS_Z2 + ((size_t)((4 * rp + (wid >> 1)) * 56 + cp * 4) * 64 + j + 16 * (2 * (wid & 1) + (g >> 1))) * 16 + (g & 1) * 8;
#pragma unroll
    for (int n = 0; n < 4; ++n) {
      v2u o;
      o[0] = pack_bf16((v[n][0] - mr[0]) * rr[0], (v[n][1] - mr[1]) * rr[1]);
      o[1] = pack_bf16((v[n][2] - mr[2]) * rr[2], (v[n][3] - mr[3]) * rr[3]);
      *(v2u*)(z2 + n * 1024) = o;
    }
  }
  STAMP(); STAMP_OUT(1);
}

#define K2_LDS (4 * 32768 + 1024)

template <int OFF>
__device__ __forceinline__ void gload_a(v4u& dst, unsigned voff, const void* sbase) {
  asm volatile("global_load_dwordx4 %0, %1, %2 offset:%3" : "=a"(dst) : "v"(voff), "s"(sbase), "i"(OFF) : "memory");
}

__global__ __launch_bounds__(256, 1) void k2_spatial(const unsigned short* __restrict__ wsp, const unsigned short* __restrict__ bsp,
                                                    char* __restrict__ ws) {
  extern __shared__ __attribute__((aligned(16))) char smem[];
  const int lane = threadIdx.x & 63;
  const int wid = __builtin_amdgcn_readfirstlane(threadIdx.x >> 6);
  const int xcd = blockIdx.x & 7, idx = blockIdx.x >> 3;
  const int tp = (xcd >> 1) * 8 + (idx & 7);
  const int fp = (xcd & 1) * 4 + (idx >> 3);
  const int g = lane >> 4;

  STAMP_DECL STAMP();
  v2u z1v[7];
  {
    const v2u* z1 = (const v2u*)(ws + WS_Z1) + ((size_t)((4 * tp + wid) * 56 + fp * 7)) * 64 + lane;
#pragma unroll
    for (int n = 0; n < 7; ++n) z1v[n] = z1[n * 64];
  }
  const float bias = __builtin_bit_cast(float, (unsigned)bsp[tp * 64 + wid * 16 + (lane & 15)] << 16);

  const char* abase = (const char*)(wsp + (size_t)(tp * 64) * S_ + wid * 512);
  const unsigned avo0 = (lane >> 3) * (S_ * 2) + (((lane & 7) ^ g) * 16);
  const unsigned avo1 = (lane >> 3) * (S_ * 2) + (((lane & 7) ^ (g + 4)) * 16);
  const unsigned ring = lds_addr(smem) + 1024 + wid * 32768;
  const char* stg = smem + 1024 + wid * 32768;
  const char* bbase = ws + WS_Z2 + ((size_t)(wid * 16) * 56 + fp * 7 + 3) * 1024;
  const unsigned bvo = lane * 16;
  const int rd0 = (lane & 15) * 128 + ((g ^ ((lane & 15) >> 1)) * 16);
  const int rd1 = rd0 ^ 64;

  v4u Bf[2][14];
  v4u Af[2][8];
  f32x4 acc[4][7];
#pragma unroll
  for (int m = 0; m < 4; ++m)
#pragma unroll
    for (int n = 0; n < 7; ++n) acc[m][n] = (f32x4){0.f, 0.f, 0.f, 0.f};

#define LOAD_A1(j, i) glds16o<(j) * 128>(((i) & 1) ? avo1 : avo0, abase + (size_t)(i) * 8 * (S_ * 2), ring + ((j) & 3) * 8192 + (i) * 1024);
#define LOAD_A(j) LOAD_A1(j, 0) LOAD_A1(j, 1) LOAD_A1(j, 2) LOAD_A1(j, 3) LOAD_A1(j, 4) LOAD_A1(j, 5) LOAD_A1(j, 6) LOAD_A1(j, 7)
#define LOAD_B1(j, ks, n) gload_a<((n) - 3) * 1024>(Bf[(j) & 1][(ks) * 7 + (n)], bvo, bbase + (size_t)(2 * (j) + (ks)) * 56 * 1024);
#define LOAD_Bk(j, ks) LOAD_B1(j, ks, 0) LOAD_B1(j, ks, 1) LOAD_B1(j, ks, 2) LOAD_B1(j, ks, 3) LOAD_B1(j, ks, 4) LOAD_B1(j, ks, 5) LOAD_B1(j, ks, 6)
#define LOAD_B(j) LOAD_Bk(j, 0) LOAD_Bk(j, 1)
#define READ_A(j) _Pragma("unroll") for (int m = 0; m < 4; ++m) _Pragma("unroll") for (int ks = 0; ks < 2; ++ks) \
    Af[(j) & 1][m * 2 + ks] = *(const v4u*)(stg + ((j) & 3) * 8192 + ((m + wid) & 3) * 2048 + (ks ? rd1 : rd0));
#define WAIT_B(N, P) asm volatile("s_waitcnt vmcnt(" #N ")" : "+a"(Bf[P][0]), "+a"(Bf[P][1]), "+a"(Bf[P][2]), "+a"(Bf[P][3]), "+a"(Bf[P][4]), \
    "+a"(Bf[P][5]), "+a"(Bf[P][6]), "+a"(Bf[P][7]), "+a"(Bf[P][8]), "+a"(Bf[P][9]), "+a"(Bf[P][10]), "+a"(Bf[P][11]), "+a"(Bf[P][12]), "+a"(Bf[P][13]) :: "memory");
#define MMA(j, ks, n) _Pragma("unroll") for (int m = 0; m < 4; ++m) acc[m][n] = mfma16(Bf[(j) & 1][(ks) * 7 + (n)], Af[(j) & 1][m * 2 + (ks)], acc[m][n]);
#define STEP_BODY(j, LB, LA) \
  MMA(j, 0, 0) SCHED(); if (LB) { LOAD_B1((j) + 2, 0, 0) } if (LA) { LOAD_A1((j) + 3, 0) } \
  MMA(j, 0, 1) SCHED(); if (LB) { LOAD_B1((j) + 2, 0, 1) } if (LA) { LOAD_A1((j) + 3, 1) } \
  MMA(j, 0, 2) SCHED(); if (LB) { LOAD_B1((j) + 2, 0, 2) } if (LA) { LOAD_A1((j) + 3, 2) } \
  MMA(j, 0, 3) SCHED(); if (LB) { LOAD_B1((j) + 2, 0, 3) } if (LA) { LOAD_A1((j) + 3, 3) } \
  MMA(j, 0, 4) SCHED(); if (LB) { LOAD_B1((j) + 2, 0, 4) } if (LA) { LOAD_A1((j) + 3, 4) } \
  MMA(j, 0, 5) SCHED(); if (LB) { LOAD_B1((j) + 2, 0, 5) } if (LA) { LOAD_A1((j) + 3, 5) } \
  MMA(j, 0, 6) SCHED(); if (LB) { LOAD_B1((j) + 2, 0, 6) } if (LA) { LOAD_A1((j) + 3, 6) } \
  MMA(j, 1, 0) SCHED(); if (LB) { LOAD_B1((j) + 2, 1, 0) } if (LA) { LOAD_A1((j) + 3, 7) } \
  MMA(j, 1, 1) SCHED(); if (LB) { LOAD_B1((j) + 2, 1, 1) } \
  MMA(j, 1, 2) SCHED(); if (LB) { LOAD_B1((j) + 2, 1, 2) } \
  MMA(j, 1, 3) SCHED(); if (LB) { LOAD_B1((j) + 2, 1, 3) } \
  MMA(j, 1, 4) SCHED(); if (LB) { LOAD_B1((j) + 2, 1, 4) } \
  MMA(j, 1, 5) SCHED(); if (LB) { LOAD_B1((j) + 2, 1, 5) } \
  MMA(j, 1, 6) SCHED(); if (LB) { LOAD_B1((j) + 2, 1, 6) } \
  SCHED();

  LOAD_A(0) LOAD_B(0) LOAD_A(1)
  LOAD_B(1) LOAD_A(2)
  STAMP();
  asm volatile("s_waitcnt vmcnt(44)" ::: "memory");
  STAMP();
  READ_A(0)
  SCHED();
  WAIT_B(22, 0) STAMP(); READ_A(1) SCHED(); STEP_BODY(0, 1, 1)
  WAIT_B(22, 1) STAMP(); READ_A(2) SCHED(); STEP_BODY(1, 1, 1)
  WAIT_B(22, 0) STAMP(); READ_A(3) SCHED(); STEP_BODY(2, 1, 1)
  WAIT_B(22, 1) STAMP(); READ_A(4) SCHED(); STEP_BODY(3, 1, 1)
  WAIT_B(22, 0) STAMP(); READ_A(5) SCHED(); STEP_BODY(4, 1, 1)
  WAIT_B(22, 1) STAMP(); READ_A(6) SCHED(); STEP_BODY(5, 1, 0)
  WAIT_B(14, 0) STAMP(); READ_A(7) SCHED(); STEP_BODY(6, 0, 0)
  WAIT_B(0, 1) STAMP(); SCHED();
  {
    char* mine = smem + 1024 + wid * 32768 + lane * 16;
#pragma unroll
    for (int mi = 1; mi <= 4; ++mi) {
      const int m = mi & 3;
#pragma unroll
      for (int ks = 0; ks < 2; ++ks)
#pragma unroll
        for (int n = 0; n < 7; ++n) acc[m][n] = mfma16(Bf[1][ks * 7 + n], Af[1][m * 2 + ks], acc[m][n]);
      SCHED();
      if (mi >= 2) {
#pragma unroll
        for (int n = 0; n < 7; ++n) *(f32x4*)(mine + ((mi - 1) * 7 + n) * 1024) = acc[mi - 1][n];
      }
      SCHED();
    }
  }
  STAMP();
  __syncthreads();
  STAMP();
  f32x4 o[7];
  {
    f32x4 pr[3][7];
#pragma unroll
    for (int d = 1; d < 4; ++d) {
      const char* src = smem + 1024 + ((wid - d) & 3) * 32768 + d * 7168 + lane * 16;
#pragma unroll
      for (int n = 0; n < 7; ++n) pr[d - 1][n] = *(const f32x4*)(src + n * 1024);
    }
    SCHED();
#pragma unroll
    for (int n = 0; n < 7; ++n) o[n] = (acc[0][n] + pr[0][n]) + (pr[1][n] + pr[2][n]);
  }
  char* hp = ws + WS_H + ((size_t)(4 * tp + wid) * 28 * 64 + (lane & 15) + 16 * (g >> 1)) * 16 + (g & 1) * 8;
#pragma unroll
  for (int n = 0; n < 7; ++n) {
    const int ft = fp * 7 + n;
    v2u h;
    h[0] = pack_bf16(bf_lo(z1v[n][0]) * (o[n][0] + bias), bf_hi(z1v[n][0]) * (o[n][1] + bias));
    h[1] = pack_bf16(bf_lo(z1v[n][1]) * (o[n][2] + bias), bf_hi(z1v[n][1]) * (o[n][3] + bias));
    *(v2u*)(hp + (ft >> 1) * 1024 + (ft & 1) * 512) = h;
  }
  STAMP(); STAMP_OUT(2);
}

#define K3_LDS (4 * 12 * 1024)
__global__ __launch_bounds__(256, 1) void k3_out(char* __restrict__ ws, unsigned short* __restrict__ out) {
  extern __shared__ __attribute__((aligned(16))) char smem[];
  const int lane = threadIdx.x & 63;
  const int wid = __builtin_amdgcn_readfirstlane(threadIdx.x >> 6);
  const int xcd = blockIdx.x & 7, idx = blockIdx.x >> 3;
  const int tp = xcd * 4 + (idx & 3);
  const int dp = idx >> 2;
  const int g = lane >> 4;
  STAMP_DECL STAMP();
  const v4u* hp = (const v4u*)(ws + WS_H) + ((size_t)(4 * tp) * 28 + wid * 7) * 64 + lane;
  const v4u* wp = (const v4u*)(ws + WS_WO) + ((size_t)(wid * 7) * 28 + dp * 4) * 64 + lane;
  v4u Hf[7][4], Wf[7][4];
#define K3_LD(k) { _Pragma("unroll") for (int n = 0; n < 4; ++n) Wf[k][n] = wp[((k) * 28 + n) * 64]; \
    _Pragma("unroll") for (int m = 0; m < 4; ++m) Hf[k][m] = hp[(((m + wid) & 3) * 28 + (k)) * 64]; SCHED(); }
#define K3_MM(k) { _Pragma("unroll") for (int m = 0; m < 4; ++m) _Pragma("unroll") for (int n = 0; n < 4; ++n) \
    acc[m][n] = mfma16(Wf[k][n], Hf[k][m], acc[m][n]); SCHED(); }
  f32x4 acc[4][4];
#pragma unroll
  for (int m = 0; m < 4; ++m)
#pragma unroll
    for (int n = 0; n < 4; ++n) acc[m][n] = (f32x4){0.f, 0.f, 0.f, 0.f};
  K3_LD(0) K3_LD(1) K3_LD(2) K3_LD(3)
  STAMP();
  K3_MM(0) K3_LD(4) K3_MM(1) K3_LD(5) K3_MM(2) K3_LD(6) K3_MM(3) K3_MM(4) K3_MM(5) K3_MM(6)
  STAMP();
#pragma unroll
  for (int m = 1; m < 4; ++m)
#pragma unroll
    for (int n = 0; n < 4; ++n) *(f32x4*)(smem + ((wid * 3 + m - 1) * 4 + n) * 1024 + lane * 16) = acc[m][n];
  __syncthreads();
  STAMP();
  f32x4 pr[3][4];
#pragma unroll
  for (int d = 1; d < 4; ++d)
#pragma unroll
    for (int n = 0; n < 4; ++n) pr[d - 1][n] = *(const f32x4*)(smem + ((((wid - d) & 3) * 3 + d - 1) * 4 + n) * 1024 + lane * 16);
  unsigned short* op = out + (size_t)(tp * 64 + wid * 16 + (lane & 15)) * D_ + dp * 64 + 4 * g;
#pragma unroll
  for (int n = 0; n < 4; ++n) {
    const f32x4 o = (acc[0][n] + pr[0][n]) + (pr[1][n] + pr[2][n]);
    v2u h;
    h[0] = pack_bf16(o[0], o[1]);
    h[1] = pack_bf16(o[2], o[3]);
    *(v2u*)(op + n * 16) = h;
  }
  STAMP(); STAMP_OUT(0);
}

extern "C" void kernel_launch(void* const* d_in, const int* in_sizes, int n_in,
                              void* d_out, int out_size, void* d_ws, size_t ws_size,
                              hipStream_t stream) {
  const unsigned short* x = (const unsigned short*)d_in[0];
  const unsigned short* win = (const unsigned short*)d_in[1];
  const unsigned short* wsp = (const unsigned short*)d_in[2];
  const unsigned short* bsp = (const unsigned short*)d_in[3];
  const unsigned short* wout = (const unsigned short*)d_in[4];
  char* ws = (char*)d_ws;
  (void)hipFuncSetAttribute((const void*)k1_gemm_gelu, hipFuncAttributeMaxDynamicSharedMemorySize, K1_LDS);
  (void)hipFuncSetAttribute((const void*)k2_spatial, hipFuncAttributeMaxDynamicSharedMemorySize, K2_LDS);
  (void)hipFuncSetAttribute((const void*)k3_out, hipFuncAttributeMaxDynamicSharedMemorySize, K3_LDS);
  k1_gemm_gelu<<<256, 640, K1_LDS, stream>>>(x, win, wsp, wout, ws, (const char*)d_out);
  k2_spatial<<<256, 256, K2_LDS, stream>>>(wsp, bsp, ws);
  k3_out<<<224, 256, K3_LDS, stream>>>(ws, (unsigned short*)d_out);
}
```

```cpp
#include <hip/hip_runtime.h>
#include <hip/hip_bf16.h>
#include <stdint.h>

typedef __attribute__((ext_vector_type(8))) short bf16x8;
typedef __attribute__((ext_vector_type(4))) short s16x4;
typedef __attribute__((ext_vector_type(4))) float f32x4;
typedef __attribute__((ext_vector_type(4))) unsigned v4u;
typedef __attribute__((ext_vector_type(2))) unsigned v2u;
typedef __attribute__((address_space(3))) s16x4* lds_s16x4_p;

#define S_ 2048
#define D_ 448
#define F_ 1792
#define FH_ 896

#define WS_Z1 0
#define WS_Z2 (WS_Z1 + 3670016)
#define WS_H  (WS_Z2 + 3670016)
#define WS_WO (WS_H + 3670016)

#define SCHED() __builtin_amdgcn_sched_barrier(0)
#define STAMP_DECL
#define STAMP()
#define STAMP_OUT(kid)

__device__ __forceinline__ f32x4 mfma16(v4u a, v4u b, f32x4 c) {
  return __builtin_amdgcn_mfma_f32_16x16x32_bf16(__builtin_bit_cast(bf16x8, a), __builtin_bit_cast(bf16x8, b), c, 0, 0, 0);
}
typedef __attribute__((ext_vector_type(2))) float f32x2;
typedef __attribute__((ext_vector_type(2))) __bf16 bf16x2;
__device__ __forceinline__ unsigned pack_bf16(float lo, float hi) {
  const bf16x2 r = __builtin_convertvector((f32x2){lo, hi}, bf16x2);
  return __builtin_bit_cast(unsigned, r);
}
__device__ __forceinline__ float asf(unsigned u) { return __builtin_bit_cast(float, u); }
__device__ __forceinline__ float bf_lo(unsigned u) { return __builtin_bit_cast(float, u << 16); }
__device__ __forceinline__ float bf_hi(unsigned u) { return __builtin_bit_cast(float, u & 0xffff0000u); }

__device__ __forceinline__ void glds16(unsigned voff, const void* sbase, unsigned lds) {
  asm volatile("s_mov_b32 m0, %2\n\ts_nop 0\n\tglobal_load_lds_dwordx4 %0, %1" ::"v"(voff), "s"(sbase), "s"(lds) : "memory");
}
template <int OFF>
__device__ __forceinline__ void glds16o(unsigned voff, const void* sbase, unsigned lds) {
  asm volatile("s_mov_b32 m0, %2\n\ts_nop 0\n\tglobal_load_lds_dwordx4 %0, %1 offset:%3" ::"v"(voff), "s"(sbase), "s"(lds - OFF), "i"(OFF) : "memory");
}
__device__ __forceinline__ unsigned lds_addr(const void* p) { return (unsigned)(size_t)(__attribute__((address_space(3))) const char*)p; }

__device__ __forceinline__ void touch(unsigned& dst, const void* p) { asm volatile("global_load_dword %0, %1, off" : "+v"(dst) : "v"(p) : "memory"); }
__device__ __forceinline__ void st_plain8(v2u d, void* p) { asm volatile("global_store_dwordx2 %0, %1, off" ::"v"(p), "v"(d) : "memory"); }

__device__ __forceinline__ float gelu_f(float x) {
  const float c0 = -2.0f * 0.7978845608028654f * 1.4426950408889634f;
  const float c1 = c0 * 0.044715f;
  float t = __builtin_fmaf(x * x, c1, c0);
  float e = __builtin_amdgcn_exp2f(x * t);
  return x * __builtin_amdgcn_rcpf(1.0f + e);
}

#define K1_XS 16384
#define K1_WB (6 * K1_XS)
#define K1_LDS (K1_WB + 8 * 8192)
#define WAITBAR(N) asm volatile("s_waitcnt vmcnt(" #N ") lgkmcnt(0)\n\ts_barrier" ::: "memory")
#define WS_CNT (WS_WO + 802816)
#define WS_EX (WS_CNT + 4096)
#define EX_CH 131072
#define EX_GRP (EX_CH + 2048)

__device__ __forceinline__ void ld8_sc1(v2u& d, unsigned voff, const void* sb) { asm volatile("global_load_dwordx2 %0, %1, %2 sc1" : "=v"(d) : "v"(voff), "s"(sb) : "memory"); }
__device__ __forceinline__ void ld4_sc1(unsigned& d, unsigned voff, const void* sb) { asm volatile("global_load_dword %0, %1, %2 sc1" : "=v"(d) : "v"(voff), "s"(sb) : "memory"); }
__device__ __forceinline__ void st8_sc1(v2u d, unsigned voff, const void* sb) { asm volatile("global_store_dwordx2 %0, %1, %2 sc1" ::"v"(voff), "v"(d), "s"(sb) : "memory"); }
__device__ __forceinline__ void st4_sc1(unsigned d, unsigned voff, const void* sb) { asm volatile("global_store_dword %0, %1, %2 sc1" ::"v"(voff), "v"(d), "s"(sb) : "memory"); }
template <int CTRL>
__device__ __forceinline__ float dpp_f(float v) {
  return __builtin_bit_cast(float, __builtin_amdgcn_update_dpp(0, __builtin_bit_cast(int, v), CTRL, 0xf, 0xf, true));
}
#define DPP_XOR1 0xB1
#define DPP_XOR3 0x1B
#define DPP_XOR7 0x141
#define DPP_XOR15 0x140
#define DPP_XOR8 0x128
#define DPP_BC(n) (0x150 + (n))

__global__ __launch_bounds__(640, 3) void k1_gemm_gelu(const unsigned short* __restrict__ x, const unsigned short* __restrict__ win,
                                                      const unsigned short* __restrict__ wsp, const unsigned short* __restrict__ wout,
                                                      char* __restrict__ ws, const char* __restrict__ outp) {
  extern __shared__ __attribute__((aligned(16))) char smem[];
  const int lane = threadIdx.x & 63;
  const int wid = __builtin_amdgcn_readfirstlane(threadIdx.x >> 6);
  const int xcd = blockIdx.x & 7, idx = blockIdx.x >> 3;
  const int g = lane >> 4, j = lane & 15;
  if (idx >= 28) {
    if (wid >= 8) return;
    STAMP_DECL STAMP();
    const int hw = ((idx - 28) * 8 + xcd) * 8 + wid;
    const char* pb = (const char*)wsp + ((size_t)((xcd >> 1) * 512 + ((xcd & 1) * 4 + idx - 28) * 64 + wid * 8) * S_) * 2;
    unsigned tdst = 0;
    if (idx == 30 && xcd == 0 && wid == 0 && lane < 32) *(unsigned*)(ws + WS_CNT + lane * 128) = 0u;
    if (idx == 29) {
      const char* b = wid < 3 ? ws + wid * (64u << 16) : wid < 5 ? (const char*)wsp + (wid - 3) * (64u << 16) : outp;
      const unsigned off = wid < 5 ? lane << 16 : min(lane, 27) << 16;
      asm volatile("global_load_dword %0, %1, %2" : "+v"(tdst) : "v"(off), "s"(b) : "memory");
    }
    if (idx == 28 && wid < 4) {
      unsigned long long pc; int d0, len;
      asm volatile("s_getpc_b64 %0\n.Lpf_here_%=:\n\ts_mov_b32 %1, _Z12k1_gemm_geluPKtS0_S0_S0_PcPKc-.Lpf_here_%=\n\t"
                   "s_mov_b32 %2, _Z10k2_spatialPKtS0_PcPt-_Z12k1_gemm_geluPKtS0_S0_S0_PcPKc" : "=s"(pc), "=s"(d0), "=s"(len));
      const unsigned off = min((unsigned)(wid * 8192 + lane * 128), (unsigned)(len + 11264));
      asm volatile("global_load_dword %0, %1, %2" : "+v"(tdst) : "v"(off), "s"(pc + (long long)d0) : "memory");
    }
#pragma unroll
    for (int i = 0; i < 4; ++i)
      asm volatile("global_load_dword %0, %1, %2" : "+v"(tdst) : "v"(lane * 128), "s"(pb + i * 8192) : "memory");
    STAMP();
    unsigned short v[4][8];
#pragma unroll
    for (int i = 0; i < 4; ++i) {
      const int b = min(hw + 256 * i, 783), kf = b / 28, dt = b % 28;
#pragma unroll
      for (int e = 0; e < 8; ++e) v[i][e] = wout[(size_t)(32 * kf + 8 * g + e) * D_ + 16 * dt + j];
    }
#pragma unroll
    for (int i = 0; i < 4; ++i) {
      const int b = min(hw + 256 * i, 783);
      v4u o;
#pragma unroll
      for (int q = 0; q < 4; ++q) o[q] = (unsigned)v[i][2 * q] | ((unsigned)v[i][2 * q + 1] << 16);
      *(v4u*)(ws + WS_WO + ((size_t)b * 64 + lane) * 16) = o;
    }
    asm volatile("s_waitcnt vmcnt(0)" : "+v"(tdst)::"memory");
    STAMP(); STAMP_OUT(3);
    return;
  }
  const int rp = (xcd >> 1) * 4 + (idx & 3);
  const int cp = (xcd & 1) * 7 + (idx >> 2);

  const unsigned sm0 = lds_addr(smem);
#define BAR_L() asm volatile("s_waitcnt lgkmcnt(0)\n\ts_barrier" ::: "memory")
#define BAR_N() asm volatile("s_barrier" ::: "memory")
#define WAITBAR_V(N) asm volatile("s_waitcnt vmcnt(" #N ")\n\ts_barrier" ::: "memory")
  if (wid >= 8) {
    const int w0 = (wid - 8) * 4;
    const char* xb0 = (const char*)(x + (size_t)(rp * 128 + w0 * 16) * D_);
    const unsigned xvo0 = (lane >> 3) * (D_ * 2) + (((lane & 7) ^ g) * 16);
    const unsigned xvo1 = (lane >> 3) * (D_ * 2) + (((lane & 7) ^ (g + 4)) * 16) + 8 * D_ * 2;
    const char* w1 = (const char*)(win + cp * 64 + (size_t)(w0 * 8) * F_);
    const char* w2 = w1 + FH_ * 2;
    const unsigned wvoa = (lane >> 3) * (F_ * 2) + (((lane & 7) ^ (2 * (g & 1))) * 16);
    const unsigned wvob = wvoa ^ 64;
    const unsigned xl = sm0 + w0 * 2048, wl = sm0 + K1_WB + w0 * 1024;
#define K1_WL(base, c, dst, dstr) { _Pragma("unroll") for (int q = 0; q < 4; ++q) \
      glds16((q & 1) ? wvob : wvoa, (base) + (size_t)((c) * 64 + q * 8) * (F_ * 2), (dst) + q * (dstr)); }
#define K1_C(c) { _Pragma("unroll") for (int q = 0; q < 4; ++q) { \
      glds16(xvo0, xb0 + q * 16 * D_ * 2 + (c) * 128, xl + (c) * K1_XS + q * 2048); \
      glds16(xvo1, xb0 + q * 16 * D_ * 2 + (c) * 128, xl + (c) * K1_XS + q * 2048 + 1024); } \
      K1_WL(w2, c, wl + (c) * 8192, 1024) }
    K1_C(0) K1_C(1) K1_C(2)
    WAITBAR_V(24);
    K1_C(3)
    WAITBAR_V(24);
    K1_C(4)
    WAITBAR_V(24);
    K1_C(5)
    WAITBAR_V(24);
    K1_WL(w2, 6, wl + 6 * 8192, 1024) K1_WL(w1, 0, wl + 7 * 8192, 1024) K1_WL(w1, 1, xl, 2048)
    WAITBAR_V(24);
    K1_WL(w1, 2, xl + 1024, 2048) K1_WL(w1, 3, xl + K1_XS, 2048) K1_WL(w1, 4, xl + K1_XS + 1024, 2048)
    WAITBAR_V(24);
    K1_WL(w1, 5, xl + 2 * K1_XS, 2048) K1_WL(w1, 6, xl + 2 * K1_XS + 1024, 2048)
    WAITBAR_V(28);
    WAITBAR_V(0);
    return;
  }
  STAMP_DECL STAMP();
  const char* ex = ws + WS_EX + (size_t)(rp * 8 + wid) * EX_GRP;
  const unsigned gr_off = (g * 8 + (j >> 1)) * 8;
  const unsigned in_vo = (cp * 16 + (j & 1)) * 512 + gr_off;
  const unsigned out_vo = ((j & 1) * 16 + cp) * 512 + gr_off;
  const unsigned pin_vo = (cp * 16 + j) * 512 + 256;
  const unsigned pout_vo = (j * 16 + cp) * 512 + 256;
  v2u in[7], pin;
#define K1_LDIN() { _Pragma("unroll") for (int k = 0; k < 7; ++k) ld8_sc1(in[k], in_vo, ex + k * 1024); ld8_sc1(pin, pin_vo, ex); }
#define K1_INREGS "+v"(in[0]), "+v"(in[1]), "+v"(in[2]), "+v"(in[3]), "+v"(in[4]), "+v"(in[5]), "+v"(in[6]), "+v"(pin)
  v4u xf[14];
  K1_LDIN()
  asm volatile("global_load_dwordx4 %0, %2, %3 offset:768\n\tglobal_load_dwordx4 %1, %2, %3 offset:832"
               : "=&v"(xf[12]), "=&v"(xf[13]) : "v"(j * (D_ * 2) + g * 16), "s"((const char*)(x + (size_t)(rp * 128 + wid * 16) * D_)) : "memory");
  STAMP();
  const int xrd0 = wid * 2048 + j * 128 + ((g ^ (j >> 1)) * 16), xrd1 = xrd0 ^ 64;
  const int fsw = 2 * (((j >> 3) & 1) + 2 * (g & 1));
  const int tra = g * 1024 + (j >> 2) * 128 + (((j >> 1) & 1) * 16) + 8 * (j & 1);
  const int trb = g * 2048 + (j >> 2) * 128 + (((j >> 1) & 1) * 16) + 8 * (j & 1);
  v4u wf[2][8];
  f32x4 acc[4];
#pragma unroll
  for (int n = 0; n < 4; ++n) acc[n] = (f32x4){0.f, 0.f, 0.f, 0.f};
#define K1_STEP(RDX, c, RDW, b, off, tb, kstr, MM, cm, ZT) { \
    if (RDX) { xf[2 * (c)] = *(const v4u*)(smem + (c) * K1_XS + xrd0); xf[2 * (c) + 1] = *(const v4u*)(smem + (c) * K1_XS + xrd1); } \
    _Pragma("unroll") for (int i = 0; i < 8; ++i) { \
      const int k2 = i >> 2, n = i & 3; \
      if (RDW) { const char* sb = smem + (off) + (tb) + (((2 * n) ^ fsw) * 16) + k2 * (kstr); \
        s16x4 t0 = __builtin_amdgcn_ds_read_tr16_b64_v4i16((lds_s16x4_p)(sb)); \
        s16x4 t1 = __builtin_amdgcn_ds_read_tr16_b64_v4i16((lds_s16x4_p)(sb + 512)); \
        v2u a_ = __builtin_bit_cast(v2u, t0), b_ = __builtin_bit_cast(v2u, t1); \
        wf[b][i] = (v4u){a_[0], a_[1], b_[0], b_[1]}; } \
      if (MM) acc[n] = ZT ? mfma16(xf[2 * (cm) + k2], wf[(b) ^ 1][i], acc[n]) : mfma16(wf[(b) ^ 1][i], xf[2 * (cm) + k2], acc[n]); \
      SCHED(); } }

  BAR_N();
  STAMP();
  K1_STEP(1, 0, 1, 0, K1_WB, tra, 4096, 0, 0, 1)
  BAR_L();
  K1_STEP(1, 1, 1, 1, K1_WB + 8192, tra, 4096, 1, 0, 1)
  asm volatile("s_waitcnt vmcnt(2)" : K1_INREGS::"memory");
  unsigned nonce = 1;
  for (;;) {
    bool hit = pin[0] == nonce;
#pragma unroll
    for (int k = 0; k < 7; ++k) hit = hit || in[k][0] == nonce;
    if (!__any(hit)) break;
    ++nonce;
  }
  if (lane == 0) st4_sc1(nonce, EX_CH + cp * 128, ex);
  BAR_L();
  K1_STEP(1, 2, 1, 0, K1_WB + 2 * 8192, tra, 4096, 1, 1, 1)
  BAR_L();
  K1_STEP(1, 3, 1, 1, K1_WB + 3 * 8192, tra, 4096, 1, 2, 1)
  BAR_N();
  K1_STEP(1, 4, 1, 0, K1_WB + 4 * 8192, tra, 4096, 1, 3, 1)
  BAR_N();
  unsigned chal;
  ld4_sc1(chal, EX_CH + j * 128, ex);
  K1_STEP(1, 5, 1, 1, K1_WB + 5 * 8192, tra, 4096, 1, 4, 1)
  asm volatile("s_waitcnt vmcnt(2)\n\ts_barrier" : "+v"(xf[12]), "+v"(xf[13])::"memory");
  K1_STEP(0, 6, 1, 0, K1_WB + 6 * 8192, tra, 4096, 1, 5, 1)
  K1_STEP(0, 0, 0, 1, 0, trb, 8192, 1, 6, 1)
  STAMP();
  float v[4][4];
  unsigned myval;
  {
    float sh[4], S[4], Q[4];
#pragma unroll
    for (int n = 0; n < 4; ++n)
#pragma unroll
      for (int r = 0; r < 4; ++r) { v[n][r] = gelu_f(acc[n][r]); acc[n][r] = 0.f; }
#pragma unroll
    for (int r = 0; r < 4; ++r) {
      sh[r] = dpp_f<DPP_BC(0)>(v[0][r]);
      const float d0 = v[0][r] - sh[r], d1 = v[1][r] - sh[r], d2 = v[2][r] - sh[r], d3 = v[3][r] - sh[r];
      S[r] = (d0 + d1) + (d2 + d3);
      Q[r] = (d0 * d0 + d1 * d1) + (d2 * d2 + d3 * d3);
    }
    const bool b3 = j & 8, b2 = j & 4, b1 = j & 2;
    float T[4];
#pragma unroll
    for (int r = 0; r < 4; ++r) T[r] = (b3 ? Q[r] : S[r]) + dpp_f<DPP_XOR15>(b3 ? S[r] : Q[r]);
    const float U0 = (b2 ? T[2] : T[0]) + dpp_f<DPP_XOR7>(b2 ? T[0] : T[2]);
    const float U1 = (b2 ? T[3] : T[1]) + dpp_f<DPP_XOR7>(b2 ? T[1] : T[3]);
    float W = (b1 ? U1 : U0) + dpp_f<DPP_XOR3>(b1 ? U0 : U1);
    W += dpp_f<DPP_XOR1>(W);
    const float other = dpp_f<DPP_XOR8>(W);
    const float shr = b2 ? (b1 ? sh[3] : sh[2]) : (b1 ? sh[1] : sh[0]);
    myval = __builtin_bit_cast(unsigned, b3 ? W - other * other * (1.0f / 64.0f) : shr + W * (1.0f / 64.0f));
  }
#define K1_SEND(ans) { \
    _Pragma("unroll") for (int k = 0; k < 7; ++k) { const unsigned t_ = __shfl(ans, 2 * k + (j & 1)); st8_sc1((v2u){t_, myval}, out_vo, ex + k * 16384); } \
    if (lane < 16) st8_sc1((v2u){ans, nonce}, pout_vo, ex); }
  asm volatile("s_waitcnt vmcnt(0)" : "+v"(chal)::"memory");
  unsigned answered = chal;
  K1_SEND(answered)
  STAMP();
  BAR_N();
  STAMP();
  K1_STEP(0, 0, 1, 0, K1_WB + 7 * 8192, tra, 4096, 0, 0, 0)
  K1_STEP(0, 0, 1, 1, 0, trb, 8192, 1, 0, 0)
  K1_STEP(0, 0, 1, 0, 1024, trb, 8192, 1, 1, 0)
  K1_STEP(0, 0, 1, 1, K1_XS, trb, 8192, 1, 2, 0)
  K1_STEP(0, 0, 1, 0, K1_XS + 1024, trb, 8192, 1, 3, 0)
  K1_STEP(0, 0, 1, 1, 2 * K1_XS, trb, 8192, 1, 4, 0)
  K1_STEP(0, 0, 1, 0, 2 * K1_XS + 1024, trb, 8192, 1, 5, 0)
  K1_STEP(0, 0, 0, 1, 0, trb, 8192, 1, 6, 0)
  STAMP();
  K1_LDIN()
  ld4_sc1(chal, EX_CH + j * 128, ex);
  {
    char* z1 = ws + WS_Z1 + ((size_t)((8 * rp + wid) * 56 + cp * 4) * 64 + lane) * 8;
#pragma unroll
    for (int n = 0; n < 4; ++n) {
      v2u o;
      o[0] = pack_bf16(gelu_f(acc[n][0]), gelu_f(acc[n][1]));
      o[1] = pack_bf16(gelu_f(acc[n][2]), gelu_f(acc[n][3]));
      st_plain8(o, z1 + n * 512);
    }
  }
  STAMP();
  asm volatile("s_waitcnt vmcnt(4)" : K1_INREGS, "+v"(chal)::"memory");
  for (unsigned spin = 0; spin < (1u << 16); ++spin) {
    bool okd = true;
#pragma unroll
    for (int k = 0; k < 7; ++k) okd = okd && in[k][0] == nonce;
    const bool pv = pin[0] == nonce;
    const unsigned cur = pv ? pin[1] : chal;
    const bool live = j < 14;
    if (__any(live && cur != answered)) { answered = cur; K1_SEND(answered) }
    if (__all(okd && (pv || !live))) break;
    K1_LDIN()
    ld4_sc1(chal, EX_CH + j * 128, ex);
    asm volatile("s_waitcnt vmcnt(0)" : K1_INREGS, "+v"(chal)::"memory");
  }
  STAMP();
  {
    float sm = ((asf(in[0][1]) + asf(in[1][1])) + (asf(in[2][1]) + asf(in[3][1]))) + ((asf(in[4][1]) + asf(in[5][1])) + asf(in[6][1]));
    sm += dpp_f<DPP_XOR1>(sm);
    const float mean = sm * (1.0f / 14.0f);
    float bt = 0.f;
#pragma unroll
    for (int k = 0; k < 7; ++k) { const float e = asf(in[k][1]) - mean; bt = __builtin_fmaf(e, e, bt); }
    bt += dpp_f<DPP_XOR1>(bt);
    const float between = dpp_f<DPP_XOR8>(bt);
    const float rstd = rsqrtf((sm + 64.0f * between) * (1.0f / FH_) + 1e-5f);
    float mr[4], rr[4];
    mr[0] = dpp_f<DPP_BC(0)>(mean); mr[1] = dpp_f<DPP_BC(2)>(mean); mr[2] = dpp_f<DPP_BC(4)>(mean); mr[3] = dpp_f<DPP_BC(6)>(mean);
    rr[0] = dpp_f<DPP_BC(8)>(rstd); rr[1] = dpp_f<DPP_BC(10)>(rstd); rr[2] = dpp_f<DPP_BC(12)>(rstd); rr[3] = dpp_f<DPP_BC(14)>(rstd);
    char* z2 = ws + WS_Z2 + ((size_t)((4 * rp + (wid >> 1)) * 56 + cp * 4) * 64 + j + 16 * (2 * (wid & 1) + (g >> 1))) * 16 + (g & 1) * 8;
#pragma unroll
    for (int n = 0; n < 4; ++n) {
      v2u o;
      o[0] = pack_bf16((v[n][0] - mr[0]) * rr[0], (v[n][1] - mr[1]) * rr[1]);
      o[1] = pack_bf16((v[n][2] - mr[2]) * rr[2], (v[n][3] - mr[3]) * rr[3]);
      *(v2u*)(z2 + n * 1024) = o;
    }
  }
  STAMP(); STAMP_OUT(1);
}

#define K2_LDS (4 * 32768 + 1024)

template <int OFF>
__device__ __forceinline__ void gload_a(v4u& dst, unsigned voff, const void* sbase) {
  asm volatile("global_load_dwordx4 %0, %1, %2 offset:%3" : "=a"(dst) : "v"(voff), "s"(sbase), "i"(OFF) : "memory");
}

template <int OFF>
__device__ __forceinline__ void gload_v(v4u& dst, unsigned voff, const void* sbase) {
  asm volatile("global_load_dwordx4 %0, %1, %2 offset:%3" : "=v"(dst) : "v"(voff), "s"(sbase), "i"(OFF) : "memory");
}
template <int OFF>
__device__ __forceinline__ void gload_s(v4u& dst, unsigned voff, const void* sbase) {
  asm volatile("global_load_dwordx4 %0, %1, %2 offset:%3 sc1" : "=v"(dst) : "v"(voff), "s"(sbase), "i"(OFF) : "memory");
}

__global__ __launch_bounds__(256, 1) void k2_spatial(const unsigned short* __restrict__ wsp, const unsigned short* __restrict__ bsp,
                                                    char* __restrict__ ws, unsigned short* __restrict__ out) {
  extern __shared__ __attribute__((aligned(16))) char smem[];
  const int lane = threadIdx.x & 63;
  const int wid = __builtin_amdgcn_readfirstlane(threadIdx.x >> 6);
  const int xcd = blockIdx.x & 7, idx = blockIdx.x >> 3;
  const int tp = (xcd >> 1) * 8 + (idx & 7);
  const int fp = (xcd & 1) * 4 + (idx >> 3);
  const int g = lane >> 4;

  STAMP_DECL STAMP();
  v2u z1v[7];
  {
    const v2u* z1 = (const v2u*)(ws + WS_Z1) + ((size_t)((4 * tp + wid) * 56 + fp * 7)) * 64 + lane;
#pragma unroll
    for (int n = 0; n < 7; ++n) z1v[n] = z1[n * 64];
  }
  const float bias = __builtin_bit_cast(float, (unsigned)bsp[tp * 64 + wid * 16 + (lane & 15)] << 16);

  const char* abase = (const char*)(wsp + (size_t)(tp * 64) * S_ + wid * 512);
  const unsigned avo0 = (lane >> 3) * (S_ * 2) + (((lane & 7) ^ g) * 16);
  const unsigned avo1 = (lane >> 3) * (S_ * 2) + (((lane & 7) ^ (g + 4)) * 16);
  const unsigned ring = lds_addr(smem) + 1024 + wid * 32768;
  const char* stg = smem + 1024 + wid * 32768;
  const char* bbase = ws + WS_Z2 + ((size_t)(wid * 16) * 56 + fp * 7 + 3) * 1024;
  const unsigned bvo = lane * 16;
  const int rd0 = (lane & 15) * 128 + ((g ^ ((lane & 15) >> 1)) * 16);
  const int rd1 = rd0 ^ 64;

  v4u Bf[2][14];
  v4u Af[2][8];
  f32x4 acc[4][7];
#pragma unroll
  for (int m = 0; m < 4; ++m)
#pragma unroll
    for (int n = 0; n < 7; ++n) acc[m][n] = (f32x4){0.f, 0.f, 0.f, 0.f};

#define LOAD_A1(j, i) glds16o<(j) * 128>(((i) & 1) ? avo1 : avo0, abase + (size_t)(i) * 8 * (S_ * 2), ring + ((j) & 3) * 8192 + (i) * 1024);
#define LOAD_A(j) LOAD_A1(j, 0) LOAD_A1(j, 1) LOAD_A1(j, 2) LOAD_A1(j, 3) LOAD_A1(j, 4) LOAD_A1(j, 5) LOAD_A1(j, 6) LOAD_A1(j, 7)
#define LOAD_B1(j, ks, n) gload_a<((n) - 3) * 1024>(Bf[(j) & 1][(ks) * 7 + (n)], bvo, bbase + (size_t)(2 * (j) + (ks)) * 56 * 1024);
#define LOAD_Bk(j, ks) LOAD_B1(j, ks, 0) LOAD_B1(j, ks, 1) LOAD_B1(j, ks, 2) LOAD_B1(j, ks, 3) LOAD_B1(j, ks, 4) LOAD_B1(j, ks, 5) LOAD_B1(j, ks, 6)
#define LOAD_B(j) LOAD_Bk(j, 0) LOAD_Bk(j, 1)
#define READ_A(j) _Pragma("unroll") for (int m = 0; m < 4; ++m) _Pragma("unroll") for (int ks = 0; ks < 2; ++ks) \
    Af[(j) & 1][m * 2 + ks] = *(const v4u*)(stg + ((j) & 3) * 8192 + ((m + wid) & 3) * 2048 + (ks ? rd1 : rd0));
#define WAIT_B(N, P) asm volatile("s_waitcnt vmcnt(" #N ")" : "+a"(Bf[P][0]), "+a"(Bf[P][1]), "+a"(Bf[P][2]), "+a"(Bf[P][3]), "+a"(Bf[P][4]), \
    "+a"(Bf[P][5]), "+a"(Bf[P][6]), "+a"(Bf[P][7]), "+a"(Bf[P][8]), "+a"(Bf[P][9]), "+a"(Bf[P][10]), "+a"(Bf[P][11]), "+a"(Bf[P][12]), "+a"(Bf[P][13]) :: "memory");
#define MMA(j, ks, n) _Pragma("unroll") for (int m = 0; m < 4; ++m) acc[m][n] = mfma16(Bf[(j) & 1][(ks) * 7 + (n)], Af[(j) & 1][m * 2 + (ks)], acc[m][n]);
#define STEP_BODY(j, LB, LA) \
  MMA(j, 0, 0) SCHED(); if (LB) { LOAD_B1((j) + 2, 0, 0) } if (LA) { LOAD_A1((j) + 3, 0) } \
  MMA(j, 0, 1) SCHED(); if (LB) { LOAD_B1((j) + 2, 0, 1) } if (LA) { LOAD_A1((j) + 3, 1) } \
  MMA(j, 0, 2) SCHED(); if (LB) { LOAD_B1((j) + 2, 0, 2) } if (LA) { LOAD_A1((j) + 3, 2) } \
  MMA(j, 0, 3) SCHED(); if (LB) { LOAD_B1((j) + 2, 0, 3) } if (LA) { LOAD_A1((j) + 3, 3) } \
  MMA(j, 0, 4) SCHED(); if (LB) { LOAD_B1((j) + 2, 0, 4) } if (LA) { LOAD_A1((j) + 3, 4) } \
  MMA(j, 0, 5) SCHED(); if (LB) { LOAD_B1((j) + 2, 0, 5) } if (LA) { LOAD_A1((j) + 3, 5) } \
  MMA(j, 0, 6) SCHED(); if (LB) { LOAD_B1((j) + 2, 0, 6) } if (LA) { LOAD_A1((j) + 3, 6) } \
  MMA(j, 1, 0) SCHED(); if (LB) { LOAD_B1((j) + 2, 1, 0) } if (LA) { LOAD_A1((j) + 3, 7) } \
  MMA(j, 1, 1) SCHED(); if (LB) { LOAD_B1((j) + 2, 1, 1) } \
  MMA(j, 1, 2) SCHED(); if (LB) { LOAD_B1((j) + 2, 1, 2) } \
  MMA(j, 1, 3) SCHED(); if (LB) { LOAD_B1((j) + 2, 1, 3) } \
  MMA(j, 1, 4) SCHED(); if (LB) { LOAD_B1((j) + 2, 1, 4) } \
  MMA(j, 1, 5) SCHED(); if (LB) { LOAD_B1((j) + 2, 1, 5) } \
  MMA(j, 1, 6) SCHED(); if (LB) { LOAD_B1((j) + 2, 1, 6) } \
  SCHED();

  const int th = fp & 1, dq = fp >> 1;
  v4u Wf[7][7], Hf[7][2];
  const char* wb = ws + WS_WO + ((size_t)(wid * 7) * 28 + dq * 7 + 3) * 1024;
#define LW(k, n) { if ((k) < 4) gload_v<((n) - 3) * 1024>(Wf[k][n], bvo, wb + (k) * 28 * 1024); else gload_a<((n) - 3) * 1024>(Wf[k][n], bvo, wb + (k) * 28 * 1024); }
  LOAD_A(0) LOAD_B(0) LOAD_A(1)
  LOAD_B(1) LOAD_A(2)
  STAMP();
  asm volatile("s_waitcnt vmcnt(44)" ::: "memory");
  STAMP();
  READ_A(0)
  SCHED();
  WAIT_B(22, 0) STAMP(); READ_A(1) SCHED(); STEP_BODY(0, 1, 1)
  WAIT_B(22, 1) STAMP(); READ_A(2) SCHED(); STEP_BODY(1, 1, 1)
  WAIT_B(22, 0) STAMP(); READ_A(3) SCHED(); STEP_BODY(2, 1, 1)
  WAIT_B(22, 1) STAMP(); READ_A(4) SCHED(); STEP_BODY(3, 1, 1)
  WAIT_B(22, 0) STAMP(); READ_A(5) SCHED(); STEP_BODY(4, 1, 1)
  WAIT_B(22, 1) STAMP(); READ_A(6) SCHED(); STEP_BODY(5, 1, 0)
  WAIT_B(14, 0) STAMP(); READ_A(7) SCHED();
  MMA(6, 0, 0) SCHED(); LW(0, 0) LW(0, 1) LW(0, 2)
  MMA(6, 0, 1) SCHED(); LW(0, 3) LW(0, 4) LW(0, 5)
  MMA(6, 0, 2) SCHED(); LW(0, 6) LW(1, 0) LW(1, 1)
  MMA(6, 0, 3) SCHED(); LW(1, 2) LW(1, 3) LW(1, 4)
  MMA(6, 0, 4) SCHED(); LW(1, 5) LW(1, 6) LW(2, 0)
  MMA(6, 0, 5) SCHED(); LW(2, 1) LW(2, 2) LW(2, 3)
  MMA(6, 0, 6) SCHED(); LW(2, 4) LW(2, 5) LW(2, 6)
  MMA(6, 1, 0) SCHED(); LW(3, 0) LW(3, 1)
  MMA(6, 1, 1) SCHED(); LW(3, 2) LW(3, 3)
  MMA(6, 1, 2) SCHED(); LW(3, 4) LW(3, 5)
  MMA(6, 1, 3) SCHED(); LW(3, 6) LW(4, 0)
  MMA(6, 1, 4) SCHED(); LW(4, 1) LW(4, 2)
  MMA(6, 1, 5) SCHED(); LW(4, 3) LW(4, 4)
  MMA(6, 1, 6) SCHED(); LW(4, 5) LW(4, 6)
  SCHED();
  WAIT_B(35, 1) STAMP(); SCHED();
  LW(5, 0) LW(5, 1) LW(5, 2) LW(5, 3) LW(5, 4) LW(5, 5) LW(5, 6) LW(6, 0) LW(6, 1) LW(6, 2) LW(6, 3) LW(6, 4) LW(6, 5) LW(6, 6)
  SCHED();
  {
    char* mine = smem + 1024 + wid * 32768 + lane * 16;
#pragma unroll
    for (int mi = 1; mi <= 4; ++mi) {
      const int m = mi & 3;
#pragma unroll
      for (int ks = 0; ks < 2; ++ks)
#pragma unroll
        for (int n = 0; n < 7; ++n) acc[m][n] = mfma16(Bf[1][ks * 7 + n], Af[1][m * 2 + ks], acc[m][n]);
      SCHED();
      if (mi >= 2) {
#pragma unroll
        for (int n = 0; n < 7; ++n) *(f32x4*)(mine + ((mi - 1) * 7 + n) * 1024) = acc[mi - 1][n];
      }
      SCHED();
    }
  }
  STAMP();
  __syncthreads();
  STAMP();
  f32x4 o[7];
  {
    f32x4 pr[3][7];
#pragma unroll
    for (int d = 1; d < 4; ++d) {
      const char* src = smem + 1024 + ((wid - d) & 3) * 32768 + d * 7168 + lane * 16;
#pragma unroll
      for (int n = 0; n < 7; ++n) pr[d - 1][n] = *(const f32x4*)(src + n * 1024);
    }
    SCHED();
#pragma unroll
    for (int n = 0; n < 7; ++n) o[n] = (acc[0][n] + pr[0][n]) + (pr[1][n] + pr[2][n]);
  }
  {
    const char* hb = ws + WS_H + (size_t)(4 * tp + wid) * 28 * 1024 + (size_t)(fp * 7 >> 1) * 1024;
    const unsigned hvo = ((lane & 15) + 16 * (g >> 1)) * 16 + (g & 1) * 8;
#pragma unroll
    for (int n = 0; n < 7; ++n) {
      v2u h;
      h[0] = pack_bf16(bf_lo(z1v[n][0]) * (o[n][0] + bias), bf_hi(z1v[n][0]) * (o[n][1] + bias));
      h[1] = pack_bf16(bf_lo(z1v[n][1]) * (o[n][2] + bias), bf_hi(z1v[n][1]) * (o[n][3] + bias));
      st8_sc1(h, hvo + ((fp & 1) + n) * 512, hb);
    }
  }
  asm volatile("s_waitcnt vmcnt(0) lgkmcnt(0)\n\ts_barrier" ::: "memory");
  STAMP();
  unsigned* cnt = (unsigned*)(ws + WS_CNT + tp * 128);
  if (threadIdx.x == 0) __hip_atomic_fetch_add(cnt, 1u, __ATOMIC_RELAXED, __HIP_MEMORY_SCOPE_AGENT);
  for (unsigned spin = 0; spin < (1u << 16); ++spin) {
    unsigned c;
    asm volatile("global_load_dword %0, %1, %2 sc1\n\ts_waitcnt vmcnt(0)" : "=&v"(c) : "v"(0u), "s"(cnt) : "memory");
    if (__builtin_amdgcn_readfirstlane(c) >= 8u) break;
    __builtin_amdgcn_s_sleep(1);
  }
  STAMP();
  {
    const char* hb = ws + WS_H + ((size_t)(4 * tp + 2 * th) * 28 + wid * 7 + 3) * 1024;
#define K3_LH(m) { gload_s<-3072>(Hf[0][m], bvo, hb + (m) * 28 * 1024); gload_s<-2048>(Hf[1][m], bvo, hb + (m) * 28 * 1024); \
      gload_s<-1024>(Hf[2][m], bvo, hb + (m) * 28 * 1024); gload_s<0>(Hf[3][m], bvo, hb + (m) * 28 * 1024); \
      gload_s<1024>(Hf[4][m], bvo, hb + (m) * 28 * 1024); gload_s<2048>(Hf[5][m], bvo, hb + (m) * 28 * 1024); gload_s<3072>(Hf[6][m], bvo, hb + (m) * 28 * 1024); }
    K3_LH(0) K3_LH(1)
  }
#define K3_WV(k) "+v"(Wf[k][0]), "+v"(Wf[k][1]), "+v"(Wf[k][2]), "+v"(Wf[k][3]), "+v"(Wf[k][4]), "+v"(Wf[k][5]), "+v"(Wf[k][6])
#define K3_WA(k) "+a"(Wf[k][0]), "+a"(Wf[k][1]), "+a"(Wf[k][2]), "+a"(Wf[k][3]), "+a"(Wf[k][4]), "+a"(Wf[k][5]), "+a"(Wf[k][6])
  asm volatile("" : K3_WV(0), K3_WV(1), K3_WV(2), K3_WV(3));
  asm volatile("" : K3_WA(4), K3_WA(5), K3_WA(6));
  asm volatile("s_waitcnt vmcnt(7)" : "+v"(Hf[0][0]), "+v"(Hf[1][0]), "+v"(Hf[2][0]), "+v"(Hf[3][0]), "+v"(Hf[4][0]), "+v"(Hf[5][0]), "+v"(Hf[6][0])::"memory");
  f32x4 a3[2][7];
#pragma unroll
  for (int n = 0; n < 7; ++n) { a3[0][n] = (f32x4){0.f, 0.f, 0.f, 0.f}; a3[1][n] = (f32x4){0.f, 0.f, 0.f, 0.f}; }
#pragma unroll
  for (int k = 0; k < 7; ++k)
#pragma unroll
    for (int n = 0; n < 7; ++n) a3[0][n] = mfma16(Wf[k][n], Hf[k][0], a3[0][n]);
  SCHED();
  asm volatile("s_waitcnt vmcnt(0)" : "+v"(Hf[0][1]), "+v"(Hf[1][1]), "+v"(Hf[2][1]), "+v"(Hf[3][1]), "+v"(Hf[4][1]), "+v"(Hf[5][1]), "+v"(Hf[6][1])::"memory");
  char* mine3 = smem + wid * 14336 + lane * 16;
#pragma unroll
  for (int n = 0; n < 7; ++n) *(f32x4*)(mine3 + n * 1024) = a3[0][n];
#pragma unroll
  for (int k = 0; k < 7; ++k)
#pragma unroll
    for (int n = 0; n < 7; ++n) a3[1][n] = mfma16(Wf[k][n], Hf[k][1], a3[1][n]);
#pragma unroll
  for (int n = 0; n < 7; ++n) *(f32x4*)(mine3 + (7 + n) * 1024) = a3[1][n];
  STAMP();
  __syncthreads();
#pragma unroll
  for (int i = 0; i < 4; ++i) {
    const int tile = wid + 4 * i;
    if (tile < 14) {
      const char* src = smem + tile * 1024 + lane * 16;
      const f32x4 r = (*(const f32x4*)(src) + *(const f32x4*)(src + 14336)) + (*(const f32x4*)(src + 28672) + *(const f32x4*)(src + 43008));
      const int m = tile >= 7, n = tile - 7 * m;
      v2u h;
      h[0] = pack_bf16(r[0], r[1]);
      h[1] = pack_bf16(r[2], r[3]);
      *(v2u*)(out + (size_t)(tp * 64 + th * 32 + m * 16 + (lane & 15)) * D_ + dq * 112 + n * 16 + 4 * g) = h;
    }
  }
  STAMP(); STAMP_OUT(2);
}

extern "C" void kernel_launch(void* const* d_in, const int* in_sizes, int n_in,
                              void* d_out, int out_size, void* d_ws, size_t ws_size,
                              hipStream_t stream) {
  const unsigned short* x = (const unsigned short*)d_in[0];
  const unsigned short* win = (const unsigned short*)d_in[1];
  const unsigned short* wsp = (const unsigned short*)d_in[2];
  const unsigned short* bsp = (const unsigned short*)d_in[3];
  const unsigned short* wout = (const unsigned short*)d_in[4];
  char* ws = (char*)d_ws;
  (void)hipFuncSetAttribute((const void*)k1_gemm_gelu, hipFuncAttributeMaxDynamicSharedMemorySize, K1_LDS);
  (void)hipFuncSetAttribute((const void*)k2_spatial, hipFuncAttributeMaxDynamicSharedMemorySize, K2_LDS);
  k1_gemm_gelu<<<256, 640, K1_LDS, stream>>>(x, win, wsp, wout, ws, (const char*)d_out);
  k2_spatial<<<256, 256, K2_LDS, stream>>>(wsp, bsp, ws, (unsigned short*)d_out);
}
```

```cpp
#include <hip/hip_runtime.h>
#include <hip/hip_bf16.h>
#include <stdint.h>

typedef __attribute__((ext_vector_type(8))) short bf16x8;
typedef __attribute__((ext_vector_type(4))) short s16x4;
typedef __attribute__((ext_vector_type(4))) float f32x4;
typedef __attribute__((ext_vector_type(4))) unsigned v4u;
typedef __attribute__((ext_vector_type(2))) unsigned v2u;
typedef __attribute__((address_space(3))) s16x4* lds_s16x4_p;

#define S_ 2048
#define D_ 448
#define F_ 1792
#define FH_ 896

#define WS_Z1 0
#define WS_Z2 (WS_Z1 + 3670016)
#define WS_H  (WS_Z2 + 3670016)
#define WS_WO (WS_H + 3670016)

#define SCHED() __builtin_amdgcn_sched_barrier(0)
#define STAMP_DECL
#define STAMP()
#define STAMP_OUT(kid)

__device__ __forceinline__ f32x4 mfma16(v4u a, v4u b, f32x4 c) {
  return __builtin_amdgcn_mfma_f32_16x16x32_bf16(__builtin_bit_cast(bf16x8, a), __builtin_bit_cast(bf16x8, b), c, 0, 0, 0);
}
typedef __attribute__((ext_vector_type(2))) float f32x2;
typedef __attribute__((ext_vector_type(2))) __bf16 bf16x2;
__device__ __forceinline__ unsigned pack_bf16(float lo, float hi) {
  const bf16x2 r = __builtin_convertvector((f32x2){lo, hi}, bf16x2);
  return __builtin_bit_cast(unsigned, r);
}
__device__ __forceinline__ float asf(unsigned u) { return __builtin_bit_cast(float, u); }
__device__ __forceinline__ float bf_lo(unsigned u) { return __builtin_bit_cast(float, u << 16); }
__device__ __forceinline__ float bf_hi(unsigned u) { return __builtin_bit_cast(float, u & 0xffff0000u); }

__device__ __forceinline__ void glds16(unsigned voff, const void* sbase, unsigned lds) {
  asm volatile("s_mov_b32 m0, %2\n\ts_nop 0\n\tglobal_load_lds_dwordx4 %0, %1" ::"v"(voff), "s"(sbase), "s"(lds) : "memory");
}
template <int OFF>
__device__ __forceinline__ void glds16o(unsigned voff, const void* sbase, unsigned lds) {
  asm volatile("s_mov_b32 m0, %2\n\ts_nop 0\n\tglobal_load_lds_dwordx4 %0, %1 offset:%3" ::"v"(voff), "s"(sbase), "s"(lds - OFF), "i"(OFF) : "memory");
}
__device__ __forceinline__ unsigned lds_addr(const void* p) { return (unsigned)(size_t)(__attribute__((address_space(3))) const char*)p; }

__device__ __forceinline__ void touch(unsigned& dst, const void* p) { asm volatile("global_load_dword %0, %1, off" : "+v"(dst) : "v"(p) : "memory"); }
__device__ __forceinline__ void st_plain8(v2u d, void* p) { asm volatile("global_store_dwordx2 %0, %1, off" ::"v"(p), "v"(d) : "memory"); }

__device__ __forceinline__ float gelu_f(float x) {
  const float c0 = -2.0f * 0.7978845608028654f * 1.4426950408889634f;
  const float c1 = c0 * 0.044715f;
  float t = __builtin_fmaf(x * x, c1, c0);
  float e = __builtin_amdgcn_exp2f(x * t);
  return x * __builtin_amdgcn_rcpf(1.0f + e);
}

#define K1_XS 16384
#define K1_WB (6 * K1_XS)
#define K1_LDS (K1_WB + 8 * 8192)
#define WAITBAR(N) asm volatile("s_waitcnt vmcnt(" #N ") lgkmcnt(0)\n\ts_barrier" ::: "memory")
#define WS_EX (WS_WO + 802816)
#define EX_CH 65536
#define EX_GRP (EX_CH + 2048)

__device__ __forceinline__ void ld8_sc1(v2u& d, unsigned voff, const void* sb) { asm volatile("global_load_dwordx2 %0, %1, %2 sc1" : "=v"(d) : "v"(voff), "s"(sb) : "memory"); }
__device__ __forceinline__ void ld4_sc1(unsigned& d, unsigned voff, const void* sb) { asm volatile("global_load_dword %0, %1, %2 sc1" : "=v"(d) : "v"(voff), "s"(sb) : "memory"); }
__device__ __forceinline__ void st8_sc1(v2u d, unsigned voff, const void* sb) { asm volatile("global_store_dwordx2 %0, %1, %2 sc1" ::"v"(voff), "v"(d), "s"(sb) : "memory"); }
__device__ __forceinline__ void st4_sc1(unsigned d, unsigned voff, const void* sb) { asm volatile("global_store_dword %0, %1, %2 sc1" ::"v"(voff), "v"(d), "s"(sb) : "memory"); }
template <int CTRL>
__device__ __forceinline__ float dpp_f(float v) {
  return __builtin_bit_cast(float, __builtin_amdgcn_update_dpp(0, __builtin_bit_cast(int, v), CTRL, 0xf, 0xf, true));
}
#define DPP_XOR1 0xB1
#define DPP_XOR3 0x1B
#define DPP_XOR7 0x141
#define DPP_XOR15 0x140
#define DPP_XOR8 0x128
#define DPP_BC(n) (0x150 + (n))

__global__ __launch_bounds__(640, 3) void k1_gemm_gelu(const unsigned short* __restrict__ x, const unsigned short* __restrict__ win,
                                                      const unsigned short* __restrict__ wsp, const unsigned short* __restrict__ wout,
                                                      char* __restrict__ ws, const char* __restrict__ outp) {
  extern __shared__ __attribute__((aligned(16))) char smem[];
  const int lane = threadIdx.x & 63;
  const int wid = __builtin_amdgcn_readfirstlane(threadIdx.x >> 6);
  const int xcd = blockIdx.x & 7, idx = blockIdx.x >> 3;
  const int g = lane >> 4, j = lane & 15;
  if (idx >= 28) {
    if (wid >= 8) return;
    STAMP_DECL STAMP();
    const int hw = ((idx - 28) * 8 + xcd) * 8 + wid;
    const char* pb = (const char*)wsp + ((size_t)((xcd >> 1) * 512 + ((xcd & 1) * 4 + idx - 28) * 64 + wid * 8) * S_) * 2;
    unsigned tdst = 0;
    if (idx == 29) {
      const char* b = wid < 3 ? ws + wid * (64u << 16) : wid < 5 ? (const char*)wsp + (wid - 3) * (64u << 16) : outp;
      const unsigned off = wid < 5 ? lane << 16 : min(lane, 27) << 16;
      asm volatile("global_load_dword %0, %1, %2" : "+v"(tdst) : "v"(off), "s"(b) : "memory");
    }
    if (idx == 28 && wid < 4) {
      unsigned long long pc; int d0, len;
      asm volatile("s_getpc_b64 %0\n.Lpf_here_%=:\n\ts_mov_b32 %1, _Z12k1_gemm_geluPKtS0_S0_S0_PcPKc-.Lpf_here_%=\n\t"
                   "s_mov_b32 %2, _Z6k3_outPcPt-_Z12k1_gemm_geluPKtS0_S0_S0_PcPKc" : "=s"(pc), "=s"(d0), "=s"(len));
      const unsigned off = min((unsigned)(wid * 8192 + lane * 128), (unsigned)(len + 2560));
      asm volatile("global_load_dword %0, %1, %2" : "+v"(tdst) : "v"(off), "s"(pc + (long long)d0) : "memory");
    }
#pragma unroll
    for (int i = 0; i < 4; ++i)
      asm volatile("global_load_dword %0, %1, %2" : "+v"(tdst) : "v"(lane * 128), "s"(pb + i * 8192) : "memory");
    STAMP();
    unsigned short v[4][8];
#pragma unroll
    for (int i = 0; i < 4; ++i) {
      const int b = min(hw + 256 * i, 783), kf = b / 28, dt = b % 28;
#pragma unroll
      for (int e = 0; e < 8; ++e) v[i][e] = wout[(size_t)(32 * kf + 8 * g + e) * D_ + 16 * dt + j];
    }
#pragma unroll
    for (int i = 0; i < 4; ++i) {
      const int b = min(hw + 256 * i, 783);
      v4u o;
#pragma unroll
      for (int q = 0; q < 4; ++q) o[q] = (unsigned)v[i][2 * q] | ((unsigned)v[i][2 * q + 1] << 16);
      *(v4u*)(ws + WS_WO + ((size_t)b * 64 + lane) * 16) = o;
    }
    asm volatile("s_waitcnt vmcnt(0)" : "+v"(tdst)::"memory");
    STAMP(); STAMP_OUT(3);
    return;
  }
  const int rp = (xcd >> 1) * 4 + (idx & 3);
  const int cp = (xcd & 1) * 7 + (idx >> 2);

  const unsigned sm0 = lds_addr(smem);
#define BAR_L() asm volatile("s_waitcnt lgkmcnt(0)\n\ts_barrier" ::: "memory")
#define BAR_N() asm volatile("s_barrier" ::: "memory")
#define WAITBAR_V(N) asm volatile("s_waitcnt vmcnt(" #N ")\n\ts_barrier" ::: "memory")
  if (wid >= 8) {
    const int w0 = (wid - 8) * 4;
    const char* xb0 = (const char*)(x + (size_t)(rp * 128 + w0 * 16) * D_);
    const unsigned xvo0 = (lane >> 3) * (D_ * 2) + (((lane & 7) ^ g) * 16);
    const unsigned xvo1 = (lane >> 3) * (D_ * 2) + (((lane & 7) ^ (g + 4)) * 16) + 8 * D_ * 2;
    const char* w1 = (const char*)(win + cp * 64 + (size_t)(w0 * 8) * F_);
    const char* w2 = w1 + FH_ * 2;
    const unsigned wvoa = (lane >> 3) * (F_ * 2) + (((lane & 7) ^ (2 * (g & 1))) * 16);
    const unsigned wvob = wvoa ^ 64;
    const unsigned xl = sm0 + w0 * 2048, wl = sm0 + K1_WB + w0 * 1024;
#define K1_WL(base, c, dst, dstr) { _Pragma("unroll") for (int q = 0; q < 4; ++q) \
      glds16((q & 1) ? wvob : wvoa, (base) + (size_t)((c) * 64 + q * 8) * (F_ * 2), (dst) + q * (dstr)); }
#define K1_C(c) { _Pragma("unroll") for (int q = 0; q < 4; ++q) { \
      glds16(xvo0, xb0 + q * 16 * D_ * 2 + (c) * 128, xl + (c) * K1_XS + q * 2048); \
      glds16(xvo1, xb0 + q * 16 * D_ * 2 + (c) * 128, xl + (c) * K1_XS + q * 2048 + 1024); } \
      K1_WL(w2, c, wl + (c) * 8192, 1024) }
    K1_C(0) K1_C(1) K1_C(2)
    WAITBAR_V(24);
    K1_C(3)
    WAITBAR_V(24);
    K1_C(4)
    WAITBAR_V(24);
    K1_C(5)
    WAITBAR_V(24);
    K1_WL(w2, 6, wl + 6 * 8192, 1024) K1_WL(w1, 0, wl + 7 * 8192, 1024) K1_WL(w1, 1, xl, 2048)
    WAITBAR_V(24);
    K1_WL(w1, 2, xl + 1024, 2048) K1_WL(w1, 3, xl + K1_XS, 2048) K1_WL(w1, 4, xl + K1_XS + 1024, 2048)
    WAITBAR_V(24);
    K1_WL(w1, 5, xl + 2 * K1_XS, 2048) K1_WL(w1, 6, xl + 2 * K1_XS + 1024, 2048)
    WAITBAR_V(28);
    WAITBAR_V(0);
    return;
  }
  STAMP_DECL STAMP();
  const char* ex = ws + WS_EX + (size_t)(rp * 8 + wid) * EX_GRP;
  const unsigned gr_off = (g * 8 + (j >> 1)) * 8;
  const unsigned in_vo = cp * 4096 + (j & 1) * 256 + gr_off;
  const unsigned out_vo = (j & 1) * 4096 + cp * 256 + gr_off;
  const unsigned pin_vo = cp * 4096 + 3584 + j * 8;
  const unsigned pout_vo = j * 4096 + 3584 + cp * 8;
  v2u in[7], pin;
#define K1_LDIN() { _Pragma("unroll") for (int k = 0; k < 7; ++k) ld8_sc1(in[k], in_vo, ex + k * 512); ld8_sc1(pin, pin_vo, ex); }
#define K1_INREGS "+v"(in[0]), "+v"(in[1]), "+v"(in[2]), "+v"(in[3]), "+v"(in[4]), "+v"(in[5]), "+v"(in[6]), "+v"(pin)
  v4u xf[14];
  v4u sc0, sc1_, sc2, sc3;
  asm volatile("global_load_dwordx4 %0, %4, %5 sc1\n\tglobal_load_dwordx4 %1, %4, %5 offset:1024 sc1\n\t"
               "global_load_dwordx4 %2, %4, %5 offset:2048 sc1\n\tglobal_load_dwordx4 %3, %4, %5 offset:3072 sc1"
               : "=&v"(sc0), "=&v"(sc1_), "=&v"(sc2), "=&v"(sc3) : "v"(cp * 4096 + lane * 16), "s"(ex) : "memory");
  asm volatile("global_load_dwordx4 %0, %2, %3 offset:768\n\tglobal_load_dwordx4 %1, %2, %3 offset:832"
               : "=&v"(xf[12]), "=&v"(xf[13]) : "v"(j * (D_ * 2) + g * 16), "s"((const char*)(x + (size_t)(rp * 128 + wid * 16) * D_)) : "memory");
  STAMP();
  const int xrd0 = wid * 2048 + j * 128 + ((g ^ (j >> 1)) * 16), xrd1 = xrd0 ^ 64;
  const int fsw = 2 * (((j >> 3) & 1) + 2 * (g & 1));
  const int tra = g * 1024 + (j >> 2) * 128 + (((j >> 1) & 1) * 16) + 8 * (j & 1);
  const int trb = g * 2048 + (j >> 2) * 128 + (((j >> 1) & 1) * 16) + 8 * (j & 1);
  v4u wf[2][8];
  f32x4 acc[4];
#pragma unroll
  for (int n = 0; n < 4; ++n) acc[n] = (f32x4){0.f, 0.f, 0.f, 0.f};
#define K1_STEP(RDX, c, RDW, b, off, tb, kstr, MM, cm, ZT) { \
    if (RDX) { xf[2 * (c)] = *(const v4u*)(smem + (c) * K1_XS + xrd0); xf[2 * (c) + 1] = *(const v4u*)(smem + (c) * K1_XS + xrd1); } \
    _Pragma("unroll") for (int i = 0; i < 8; ++i) { \
      const int k2 = i >> 2, n = i & 3; \
      if (RDW) { const char* sb = smem + (off) + (tb) + (((2 * n) ^ fsw) * 16) + k2 * (kstr); \
        s16x4 t0 = __builtin_amdgcn_ds_read_tr16_b64_v4i16((lds_s16x4_p)(sb)); \
        s16x4 t1 = __builtin_amdgcn_ds_read_tr16_b64_v4i16((lds_s16x4_p)(sb + 512)); \
        v2u a_ = __builtin_bit_cast(v2u, t0), b_ = __builtin_bit_cast(v2u, t1); \
        wf[b][i] = (v4u){a_[0], a_[1], b_[0], b_[1]}; } \
      if (MM) acc[n] = ZT ? mfma16(xf[2 * (cm) + k2], wf[(b) ^ 1][i], acc[n]) : mfma16(wf[(b) ^ 1][i], xf[2 * (cm) + k2], acc[n]); \
      SCHED(); } }

  BAR_N();
  STAMP();
  K1_STEP(1, 0, 1, 0, K1_WB, tra, 4096, 0, 0, 1)
  BAR_L();
  K1_STEP(1, 1, 1, 1, K1_WB + 8192, tra, 4096, 1, 0, 1)
  asm volatile("s_waitcnt vmcnt(2)" : "+v"(sc0), "+v"(sc1_), "+v"(sc2), "+v"(sc3)::"memory");
  unsigned nonce = 1;
  while (__any(sc0[0] == nonce || sc0[2] == nonce || sc1_[0] == nonce || sc1_[2] == nonce || sc2[0] == nonce || sc2[2] == nonce ||
               sc3[0] == nonce || sc3[2] == nonce)) ++nonce;
  if (lane == 0) st4_sc1(nonce, EX_CH + cp * 128, ex);
  BAR_L();
  K1_STEP(1, 2, 1, 0, K1_WB + 2 * 8192, tra, 4096, 1, 1, 1)
  BAR_L();
  K1_STEP(1, 3, 1, 1, K1_WB + 3 * 8192, tra, 4096, 1, 2, 1)
  BAR_N();
  K1_STEP(1, 4, 1, 0, K1_WB + 4 * 8192, tra, 4096, 1, 3, 1)
  BAR_N();
  unsigned chal;
  ld4_sc1(chal, EX_CH + j * 128, ex);
  K1_STEP(1, 5, 1, 1, K1_WB + 5 * 8192, tra, 4096, 1, 4, 1)
  asm volatile("s_waitcnt vmcnt(2)\n\ts_barrier" : "+v"(xf[12]), "+v"(xf[13])::"memory");
  K1_STEP(0, 6, 1, 0, K1_WB + 6 * 8192, tra, 4096, 1, 5, 1)
  K1_STEP(0, 0, 0, 1, 0, trb, 8192, 1, 6, 1)
  STAMP();
  asm volatile("s_waitcnt vmcnt(0)" : "+v"(chal)::"memory");
  unsigned answered = chal;
  unsigned tg[7];
#pragma unroll
  for (int k = 0; k < 7; ++k) tg[k] = __shfl(answered, 2 * k + (j & 1));
  float v[4][4];
  unsigned myval;
  {
    float sh[4], S[4], Q[4];
#pragma unroll
    for (int n = 0; n < 4; ++n)
#pragma unroll
      for (int r = 0; r < 4; ++r) { v[n][r] = gelu_f(acc[n][r]); acc[n][r] = 0.f; }
#pragma unroll
    for (int r = 0; r < 4; ++r) {
      sh[r] = dpp_f<DPP_BC(0)>(v[0][r]);
      const float d0 = v[0][r] - sh[r], d1 = v[1][r] - sh[r], d2 = v[2][r] - sh[r], d3 = v[3][r] - sh[r];
      S[r] = (d0 + d1) + (d2 + d3);
      Q[r] = (d0 * d0 + d1 * d1) + (d2 * d2 + d3 * d3);
    }
    const bool b3 = j & 8, b2 = j & 4, b1 = j & 2;
    float T[4];
#pragma unroll
    for (int r = 0; r < 4; ++r) T[r] = (b3 ? Q[r] : S[r]) + dpp_f<DPP_XOR15>(b3 ? S[r] : Q[r]);
    const float U0 = (b2 ? T[2] : T[0]) + dpp_f<DPP_XOR7>(b2 ? T[0] : T[2]);
    const float U1 = (b2 ? T[3] : T[1]) + dpp_f<DPP_XOR7>(b2 ? T[1] : T[3]);
    float W = (b1 ? U1 : U0) + dpp_f<DPP_XOR3>(b1 ? U0 : U1);
    W += dpp_f<DPP_XOR1>(W);
    const float other = dpp_f<DPP_XOR8>(W);
    const float shr = b2 ? (b1 ? sh[3] : sh[2]) : (b1 ? sh[1] : sh[0]);
    myval = __builtin_bit_cast(unsigned, b3 ? W - other * other * (1.0f / 64.0f) : shr + W * (1.0f / 64.0f));
  }
#define K1_SEND(ans) { \
    _Pragma("unroll") for (int k = 0; k < 7; ++k) { const unsigned t_ = __shfl(ans, 2 * k + (j & 1)); st8_sc1((v2u){t_, myval}, out_vo, ex + k * 8192); } \
    if (lane < 16) st8_sc1((v2u){ans, nonce}, pout_vo, ex); }
#pragma unroll
  for (int k = 0; k < 7; ++k) st8_sc1((v2u){tg[k], myval}, out_vo, ex + k * 8192);
  if (lane < 16) st8_sc1((v2u){answered, nonce}, pout_vo, ex);
  STAMP();
  BAR_N();
  STAMP();
  K1_STEP(0, 0, 1, 0, K1_WB + 7 * 8192, tra, 4096, 0, 0, 0)
  K1_STEP(0, 0, 1, 1, 0, trb, 8192, 1, 0, 0)
  K1_STEP(0, 0, 1, 0, 1024, trb, 8192, 1, 1, 0)
  K1_STEP(0, 0, 1, 1, K1_XS, trb, 8192, 1, 2, 0)
  K1_STEP(0, 0, 1, 0, K1_XS + 1024, trb, 8192, 1, 3, 0)
  K1_STEP(0, 0, 1, 1, 2 * K1_XS, trb, 8192, 1, 4, 0)
  K1_STEP(0, 0, 1, 0, 2 * K1_XS + 1024, trb, 8192, 1, 5, 0)
  K1_STEP(0, 0, 0, 1, 0, trb, 8192, 1, 6, 0)
  STAMP();
  K1_LDIN()
  ld4_sc1(chal, EX_CH + j * 128, ex);
  {
    char* z1 = ws + WS_Z1 + ((size_t)((8 * rp + wid) * 56 + cp * 4) * 64 + lane) * 8;
#pragma unroll
    for (int n = 0; n < 4; ++n) {
      v2u o;
      o[0] = pack_bf16(gelu_f(acc[n][0]), gelu_f(acc[n][1]));
      o[1] = pack_bf16(gelu_f(acc[n][2]), gelu_f(acc[n][3]));
      st_plain8(o, z1 + n * 512);
    }
  }
  STAMP();
  asm volatile("s_waitcnt vmcnt(4)" : K1_INREGS, "+v"(chal)::"memory");
  for (unsigned spin = 0; spin < (1u << 16); ++spin) {
    bool okd = true;
#pragma unroll
    for (int k = 0; k < 7; ++k) okd = okd && in[k][0] == nonce;
    const bool pv = pin[0] == nonce;
    const unsigned cur = pv ? pin[1] : chal;
    const bool live = j < 14;
    if (__any(live && cur != answered)) { answered = cur; K1_SEND(answered) }
    if (__all(okd && (pv || !live))) break;
    K1_LDIN()
    ld4_sc1(chal, EX_CH + j * 128, ex);
    asm volatile("s_waitcnt vmcnt(0)" : K1_INREGS, "+v"(chal)::"memory");
  }
  STAMP();
  {
    float sm = ((asf(in[0][1]) + asf(in[1][1])) + (asf(in[2][1]) + asf(in[3][1]))) + ((asf(in[4][1]) + asf(in[5][1])) + asf(in[6][1]));
    sm += dpp_f<DPP_XOR1>(sm);
    const float mean = sm * (1.0f / 14.0f);
    float bt = 0.f;
#pragma unroll
    for (int k = 0; k < 7; ++k) { const float e = asf(in[k][1]) - mean; bt = __builtin_fmaf(e, e, bt); }
    bt += dpp_f<DPP_XOR1>(bt);
    const float between = dpp_f<DPP_XOR8>(bt);
    const float rstd = rsqrtf((sm + 64.0f * between) * (1.0f / FH_) + 1e-5f);
    float mr[4], rr[4];
    mr[0] = dpp_f<DPP_BC(0)>(mean); mr[1] = dpp_f<DPP_BC(2)>(mean); mr[2] = dpp_f<DPP_BC(4)>(mean); mr[3] = dpp_f<DPP_BC(6)>(mean);
    rr[0] = dpp_f<DPP_BC(8)>(rstd); rr[1] = dpp_f<DPP_BC(10)>(rstd); rr[2] = dpp_f<DPP_BC(12)>(rstd); rr[3] = dpp_f<DPP_BC(14)>(rstd);
    char* z2 = ws + WS_Z2 + ((size_t)((4 * rp + (wid >> 1)) * 56 + cp * 4) * 64 + j + 16 * (2 * (wid & 1) + (g >> 1))) * 16 + (g & 1) * 8;
#pragma unroll
    for (int n = 0; n < 4; ++n) {
      v2u o;
      o[0] = pack_bf16((v[n][0] - mr[0]) * rr[0], (v[n][1] - mr[1]) * rr[1]);
      o[1] = pack_bf16((v[n][2] - mr[2]) * rr[2], (v[n][3] - mr[3]) * rr[3]);
      *(v2u*)(z2 + n * 1024) = o;
    }
  }
  STAMP(); STAMP_OUT(1);
}

#define K2_LDS (4 * 32768 + 1024)

template <int OFF>
__device__ __forceinline__ void gload_a(v4u& dst, unsigned voff, const void* sbase) {
  asm volatile("global_load_dwordx4 %0, %1, %2 offset:%3" : "=a"(dst) : "v"(voff), "s"(sbase), "i"(OFF) : "memory");
}

__global__ __launch_bounds__(256, 1) void k2_spatial(const unsigned short* __restrict__ wsp, const unsigned short* __restrict__ bsp,
                                                    char* __restrict__ ws) {
  extern __shared__ __attribute__((aligned(16))) char smem[];
  const int lane = threadIdx.x & 63;
  const int wid = __builtin_amdgcn_readfirstlane(threadIdx.x >> 6);
  const int xcd = blockIdx.x & 7, idx = blockIdx.x >> 3;
  const int tp = (xcd >> 1) * 8 + (idx & 7);
  const int fp = (xcd & 1) * 4 + (idx >> 3);
  const int g = lane >> 4;

  STAMP_DECL STAMP();
  const char* abase = (const char*)(wsp + (size_t)(tp * 64) * S_ + wid * 64);
  const unsigned avo0 = (lane >> 3) * (S_ * 2) + (((lane & 7) ^ g) * 16);
  const unsigned avo1 = (lane >> 3) * (S_ * 2) + (((lane & 7) ^ (g + 4)) * 16);
  const unsigned ring = lds_addr(smem) + 1024 + wid * 32768;
  const char* stg = smem + 1024 + wid * 32768;
  const char* bbase = ws + WS_Z2 + ((size_t)(wid * 2) * 56 + fp * 7 + 3) * 1024;
  const unsigned bvo = lane * 16;
  const int rd0 = (lane & 15) * 128 + ((g ^ ((lane & 15) >> 1)) * 16);
  const int rd1 = rd0 ^ 64;

  v4u Bf[2][14];
  v4u Af[2][8];
  f32x4 acc[4][7];
#pragma unroll
  for (int m = 0; m < 4; ++m)
#pragma unroll
    for (int n = 0; n < 7; ++n) acc[m][n] = (f32x4){0.f, 0.f, 0.f, 0.f};

#define K2_ROT 0
  const int rot = K2_ROT;
#define RJ(j) (4 * (j))
#define LOAD_A1(j, i) glds16(((i) & 1) ? avo1 : avo0, abase + (size_t)(i) * 8 * (S_ * 2) + RJ(j) * 128, ring + ((j) & 3) * 8192 + (i) * 1024);
#define LOAD_A(j) LOAD_A1(j, 0) LOAD_A1(j, 1) LOAD_A1(j, 2) LOAD_A1(j, 3) LOAD_A1(j, 4) LOAD_A1(j, 5) LOAD_A1(j, 6) LOAD_A1(j, 7)
#define LOAD_B1(j, ks, n) gload_a<((n) - 3) * 1024>(Bf[(j) & 1][(ks) * 7 + (n)], bvo, bbase + (size_t)(2 * RJ(j) + (ks)) * 56 * 1024);
#define LOAD_Bk(j, ks) LOAD_B1(j, ks, 0) LOAD_B1(j, ks, 1) LOAD_B1(j, ks, 2) LOAD_B1(j, ks, 3) LOAD_B1(j, ks, 4) LOAD_B1(j, ks, 5) LOAD_B1(j, ks, 6)
#define LOAD_B(j) LOAD_Bk(j, 0) LOAD_Bk(j, 1)
#define READ_A(j) _Pragma("unroll") for (int m = 0; m < 4; ++m) _Pragma("unroll") for (int ks = 0; ks < 2; ++ks) \
    Af[(j) & 1][m * 2 + ks] = *(const v4u*)(stg + ((j) & 3) * 8192 + ((m + wid) & 3) * 2048 + (ks ? rd1 : rd0));
#define WAIT_B(N, P) asm volatile("s_waitcnt vmcnt(" #N ")" : "+a"(Bf[P][0]), "+a"(Bf[P][1]), "+a"(Bf[P][2]), "+a"(Bf[P][3]), "+a"(Bf[P][4]), \
    "+a"(Bf[P][5]), "+a"(Bf[P][6]), "+a"(Bf[P][7]), "+a"(Bf[P][8]), "+a"(Bf[P][9]), "+a"(Bf[P][10]), "+a"(Bf[P][11]), "+a"(Bf[P][12]), "+a"(Bf[P][13]) :: "memory");
#define MMA(j, ks, n) _Pragma("unroll") for (int m = 0; m < 4; ++m) acc[m][n] = mfma16(Bf[(j) & 1][(ks) * 7 + (n)], Af[(j) & 1][m * 2 + (ks)], acc[m][n]);
#define STEP_BODY(j, LB, LA) \
  MMA(j, 0, 0) SCHED(); if (LB) { LOAD_B1((j) + 2, 0, 0) } if (LA) { LOAD_A1((j) + 3, 0) } \
  MMA(j, 0, 1) SCHED(); if (LB) { LOAD_B1((j) + 2, 0, 1) } if (LA) { LOAD_A1((j) + 3, 1) } \
  MMA(j, 0, 2) SCHED(); if (LB) { LOAD_B1((j) + 2, 0, 2) } if (LA) { LOAD_A1((j) + 3, 2) } \
  MMA(j, 0, 3) SCHED(); if (LB) { LOAD_B1((j) + 2, 0, 3) } if (LA) { LOAD_A1((j) + 3, 3) } \
  MMA(j, 0, 4) SCHED(); if (LB) { LOAD_B1((j) + 2, 0, 4) } if (LA) { LOAD_A1((j) + 3, 4) } \
  MMA(j, 0, 5) SCHED(); if (LB) { LOAD_B1((j) + 2, 0, 5) } if (LA) { LOAD_A1((j) + 3, 5) } \
  MMA(j, 0, 6) SCHED(); if (LB) { LOAD_B1((j) + 2, 0, 6) } if (LA) { LOAD_A1((j) + 3, 6) } \
  MMA(j, 1, 0) SCHED(); if (LB) { LOAD_B1((j) + 2, 1, 0) } if (LA) { LOAD_A1((j) + 3, 7) } \
  MMA(j, 1, 1) SCHED(); if (LB) { LOAD_B1((j) + 2, 1, 1) } \
  MMA(j, 1, 2) SCHED(); if (LB) { LOAD_B1((j) + 2, 1, 2) } \
  MMA(j, 1, 3) SCHED(); if (LB) { LOAD_B1((j) + 2, 1, 3) } \
  MMA(j, 1, 4) SCHED(); if (LB) { LOAD_B1((j) + 2, 1, 4) } \
  MMA(j, 1, 5) SCHED(); if (LB) { LOAD_B1((j) + 2, 1, 5) } \
  MMA(j, 1, 6) SCHED(); if (LB) { LOAD_B1((j) + 2, 1, 6) } \
  SCHED();

  LOAD_A(0) LOAD_B(0) LOAD_A(1)
  LOAD_B(1) LOAD_A(2)
  STAMP();
  asm volatile("s_waitcnt vmcnt(44)" ::: "memory");
  STAMP();
  READ_A(0)
  SCHED();
  WAIT_B(22, 0) STAMP(); READ_A(1) SCHED(); STEP_BODY(0, 1, 1)
  WAIT_B(22, 1) STAMP(); READ_A(2) SCHED(); STEP_BODY(1, 1, 1)
  WAIT_B(22, 0) STAMP(); READ_A(3) SCHED(); STEP_BODY(2, 1, 1)
  WAIT_B(22, 1) STAMP(); READ_A(4) SCHED(); STEP_BODY(3, 1, 1)
  WAIT_B(22, 0) STAMP(); READ_A(5) SCHED(); STEP_BODY(4, 1, 1)
  WAIT_B(22, 1) STAMP(); READ_A(6) SCHED(); STEP_BODY(5, 1, 0)
  WAIT_B(14, 0) STAMP(); READ_A(7) SCHED();
  v2u z1v[7];
  {
    const v2u* z1 = (const v2u*)(ws + WS_Z1) + ((size_t)((4 * tp + wid) * 56 + fp * 7)) * 64 + lane;
#pragma unroll
    for (int n = 0; n < 7; ++n) z1v[n] = z1[n * 64];
  }
  const float bias = __builtin_bit_cast(float, (unsigned)bsp[tp * 64 + wid * 16 + (lane & 15)] << 16);

 STEP_BODY(6, 0, 0)
  WAIT_B(8, 1) STAMP(); SCHED();
  {
    char* mine = smem + 1024 + wid * 32768 + lane * 16;
#pragma unroll
    for (int mi = 1; mi <= 4; ++mi) {
      const int m = mi & 3;
#pragma unroll
      for (int ks = 0; ks < 2; ++ks)
#pragma unroll
        for (int n = 0; n < 7; ++n) acc[m][n] = mfma16(Bf[1][ks * 7 + n], Af[1][m * 2 + ks], acc[m][n]);
      SCHED();
      if (mi >= 2) {
#pragma unroll
        for (int n = 0; n < 7; ++n) *(f32x4*)(mine + ((mi - 1) * 7 + n) * 1024) = acc[mi - 1][n];
      }
      SCHED();
    }
  }
  STAMP();
  __syncthreads();
  STAMP();
  f32x4 o[7];
  {
    f32x4 pr[3][7];
#pragma unroll
    for (int d = 1; d < 4; ++d) {
      const char* src = smem + 1024 + ((wid - d) & 3) * 32768 + d * 7168 + lane * 16;
#pragma unroll
      for (int n = 0; n < 7; ++n) pr[d - 1][n] = *(const f32x4*)(src + n * 1024);
    }
    SCHED();
#pragma unroll
    for (int n = 0; n < 7; ++n) o[n] = (acc[0][n] + pr[0][n]) + (pr[1][n] + pr[2][n]);
  }
  char* hp = ws + WS_H + ((size_t)(4 * tp + wid) * 28 * 64 + (lane & 15) + 16 * (g >> 1)) * 16 + (g & 1) * 8;
#pragma unroll
  for (int n = 0; n < 7; ++n) {
    const int ft = fp * 7 + n;
    v2u h;
    h[0] = pack_bf16(bf_lo(z1v[n][0]) * (o[n][0] + bias), bf_hi(z1v[n][0]) * (o[n][1] + bias));
    h[1] = pack_bf16(bf_lo(z1v[n][1]) * (o[n][2] + bias), bf_hi(z1v[n][1]) * (o[n][3] + bias));
    *(v2u*)(hp + (ft >> 1) * 1024 + (ft & 1) * 512) = h;
  }
  STAMP(); STAMP_OUT(2);
}

#define K3_LDS (4 * 12 * 1024)
__global__ __launch_bounds__(256, 1) void k3_out(char* __restrict__ ws, unsigned short* __restrict__ out) {
  extern __shared__ __attribute__((aligned(16))) char smem[];
  const int lane = threadIdx.x & 63;
  const int wid = __builtin_amdgcn_readfirstlane(threadIdx.x >> 6);
  const int xcd = blockIdx.x & 7, idx = blockIdx.x >> 3;
  const int tp = xcd * 4 + (idx & 3);
  const int dp = idx >> 2;
  const int g = lane >> 4;
  STAMP_DECL STAMP();
  const v4u* hp = (const v4u*)(ws + WS_H) + ((size_t)(4 * tp) * 28 + wid * 7) * 64 + lane;
  const v4u* wp = (const v4u*)(ws + WS_WO) + ((size_t)(wid * 7) * 28 + dp * 4) * 64 + lane;
  v4u Hf[7][4], Wf[7][4];
#define K3_LD(k) { _Pragma("unroll") for (int n = 0; n < 4; ++n) Wf[k][n] = wp[((k) * 28 + n) * 64]; \
    _Pragma("unroll") for (int m = 0; m < 4; ++m) Hf[k][m] = hp[(((m + wid) & 3) * 28 + (k)) * 64]; SCHED(); }
#define K3_MM(k) { _Pragma("unroll") for (int m = 0; m < 4; ++m) _Pragma("unroll") for (int n = 0; n < 4; ++n) \
    acc[m][n] = mfma16(Wf[k][n], Hf[k][m], acc[m][n]); SCHED(); }
  f32x4 acc[4][4];
#pragma unroll
  for (int m = 0; m < 4; ++m)
#pragma unroll
    for (int n = 0; n < 4; ++n) acc[m][n] = (f32x4){0.f, 0.f, 0.f, 0.f};
  K3_LD(0) K3_LD(1) K3_LD(2) K3_LD(3)
  STAMP();
  K3_MM(0) K3_LD(4) K3_MM(1) K3_LD(5) K3_MM(2) K3_LD(6) K3_MM(3) K3_MM(4) K3_MM(5) K3_MM(6)
  STAMP();
#pragma unroll
  for (int m = 1; m < 4; ++m)
#pragma unroll
    for (int n = 0; n < 4; ++n) *(f32x4*)(smem + ((wid * 3 + m - 1) * 4 + n) * 1024 + lane * 16) = acc[m][n];
  __syncthreads();
  STAMP();
  f32x4 pr[3][4];
#pragma unroll
  for (int d = 1; d < 4; ++d)
#pragma unroll
    for (int n = 0; n < 4; ++n) pr[d - 1][n] = *(const f32x4*)(smem + ((((wid - d) & 3) * 3 + d - 1) * 4 + n) * 1024 + lane * 16);
  unsigned short* op = out + (size_t)(tp * 64 + wid * 16 + (lane & 15)) * D_ + dp * 64 + 4 * g;
#pragma unroll
  for (int n = 0; n < 4; ++n) {
    const f32x4 o = (acc[0][n] + pr[0][n]) + (pr[1][n] + pr[2][n]);
    v2u h;
    h[0] = pack_bf16(o[0], o[1]);
    h[1] = pack_bf16(o[2], o[3]);
    *(v2u*)(op + n * 16) = h;
  }
  STAMP(); STAMP_OUT(0);
}

extern "C" void kernel_launch(void* const* d_in, const int* in_sizes, int n_in,
                              void* d_out, int out_size, void* d_ws, size_t ws_size,
                              hipStream_t stream) {
  const unsigned short* x = (const unsigned short*)d_in[0];
  const unsigned short* win = (const unsigned short*)d_in[1];
  const unsigned short* wsp = (const unsigned short*)d_in[2];
  const unsigned short* bsp = (const unsigned short*)d_in[3];
  const unsigned short* wout = (const unsigned short*)d_in[4];
  char* ws = (char*)d_ws;
  (void)hipFuncSetAttribute((const void*)k1_gemm_gelu, hipFuncAttributeMaxDynamicSharedMemorySize, K1_LDS);
  (void)hipFuncSetAttribute((const void*)k2_spatial, hipFuncAttributeMaxDynamicSharedMemorySize, K2_LDS);
  (void)hipFuncSetAttribute((const void*)k3_out, hipFuncAttributeMaxDynamicSharedMemorySize, K3_LDS);
  k1_gemm_gelu<<<256, 640, K1_LDS, stream>>>(x, win, wsp, wout, ws, (const char*)d_out);
  k2_spatial<<<256, 256, K2_LDS, stream>>>(wsp, bsp, ws);
  k3_out<<<224, 256, K3_LDS, stream>>>(ws, (unsigned short*)d_out);
}
```

```cpp
#include <hip/hip_runtime.h>
#include <hip/hip_bf16.h>
#include <stdint.h>

typedef __attribute__((ext_vector_type(8))) short bf16x8;
typedef __attribute__((ext_vector_type(4))) short s16x4;
typedef __attribute__((ext_vector_type(4))) float f32x4;
typedef __attribute__((ext_vector_type(4))) unsigned v4u;
typedef __attribute__((ext_vector_type(2))) unsigned v2u;
typedef __attribute__((address_space(3))) s16x4* lds_s16x4_p;

#define S_ 2048
#define D_ 448
#define F_ 1792
#define FH_ 896

#define WS_Z1 0
#define WS_Z2 (WS_Z1 + 3670016)
#define WS_H  (WS_Z2 + 3670016)
#define WS_WO (WS_H + 3670016)

#define SCHED() __builtin_amdgcn_sched_barrier(0)
#define STAMP_DECL
#define STAMP()
#define STAMP_OUT(kid)

__device__ __forceinline__ f32x4 mfma16(v4u a, v4u b, f32x4 c) {
  return __builtin_amdgcn_mfma_f32_16x16x32_bf16(__builtin_bit_cast(bf16x8, a), __builtin_bit_cast(bf16x8, b), c, 0, 0, 0);
}
typedef __attribute__((ext_vector_type(2))) float f32x2;
typedef __attribute__((ext_vector_type(2))) __bf16 bf16x2;
__device__ __forceinline__ unsigned pack_bf16(float lo, float hi) {
  const bf16x2 r = __builtin_convertvector((f32x2){lo, hi}, bf16x2);
  return __builtin_bit_cast(unsigned, r);
}
__device__ __forceinline__ float asf(unsigned u) { return __builtin_bit_cast(float, u); }
__device__ __forceinline__ float bf_lo(unsigned u) { return __builtin_bit_cast(float, u << 16); }
__device__ __forceinline__ float bf_hi(unsigned u) { return __builtin_bit_cast(float, u & 0xffff0000u); }

__device__ __forceinline__ void glds16(unsigned voff, const void* sbase, unsigned lds) {
  asm volatile("s_mov_b32 m0, %2\n\ts_nop 0\n\tglobal_load_lds_dwordx4 %0, %1" ::"v"(voff), "s"(sbase), "s"(lds) : "memory");
}
template <int OFF>
__device__ __forceinline__ void glds16o(unsigned voff, const void* sbase, unsigned lds) {
  asm volatile("s_mov_b32 m0, %2\n\ts_nop 0\n\tglobal_load_lds_dwordx4 %0, %1 offset:%3" ::"v"(voff), "s"(sbase), "s"(lds - OFF), "i"(OFF) : "memory");
}
__device__ __forceinline__ unsigned lds_addr(const void* p) { return (unsigned)(size_t)(__attribute__((address_space(3))) const char*)p; }

__device__ __forceinline__ void touch(unsigned& dst, const void* p) { asm volatile("global_load_dword %0, %1, off" : "+v"(dst) : "v"(p) : "memory"); }
__device__ __forceinline__ void st_plain8(v2u d, void* p) { asm volatile("global_store_dwordx2 %0, %1, off" ::"v"(p), "v"(d) : "memory"); }

__device__ __forceinline__ float gelu_f(float x) {
  const float c0 = -2.0f * 0.7978845608028654f * 1.4426950408889634f;
  const float c1 = c0 * 0.044715f;
  float t = __builtin_fmaf(x * x, c1, c0);
  float e = __builtin_amdgcn_exp2f(x * t);
  return x * __builtin_amdgcn_rcpf(1.0f + e);
}

#define K1_XS 16384
#define K1_WB (6 * K1_XS)
#define K1_LDS (K1_WB + 8 * 8192)
#define WAITBAR(N) asm volatile("s_waitcnt vmcnt(" #N ") lgkmcnt(0)\n\ts_barrier" ::: "memory")
#define WS_EX (WS_WO + 802816)
#define EX_CH 65536
#define EX_GRP (EX_CH + 2048)

__device__ __forceinline__ void ld8_sc1(v2u& d, unsigned voff, const void* sb) { asm volatile("global_load_dwordx2 %0, %1, %2 sc1" : "=v"(d) : "v"(voff), "s"(sb) : "memory"); }
__device__ __forceinline__ void ld4_sc1(unsigned& d, unsigned voff, const void* sb) { asm volatile("global_load_dword %0, %1, %2 sc1" : "=v"(d) : "v"(voff), "s"(sb) : "memory"); }
__device__ __forceinline__ void st8_sc1(v2u d, unsigned voff, const void* sb) { asm volatile("global_store_dwordx2 %0, %1, %2 sc1" ::"v"(voff), "v"(d), "s"(sb) : "memory"); }
__device__ __forceinline__ void st4_sc1(unsigned d, unsigned voff, const void* sb) { asm volatile("global_store_dword %0, %1, %2 sc1" ::"v"(voff), "v"(d), "s"(sb) : "memory"); }
template <int CTRL>
__device__ __forceinline__ float dpp_f(float v) {
  return __builtin_bit_cast(float, __builtin_amdgcn_update_dpp(0, __builtin_bit_cast(int, v), CTRL, 0xf, 0xf, true));
}
#define DPP_XOR1 0xB1
#define DPP_XOR3 0x1B
#define DPP_XOR7 0x141
#define DPP_XOR15 0x140
#define DPP_XOR8 0x128
#define DPP_BC(n) (0x150 + (n))

__global__ __launch_bounds__(640, 3) void k1_gemm_gelu(const unsigned short* __restrict__ x, const unsigned short* __restrict__ win,
                                                      const unsigned short* __restrict__ wsp, const unsigned short* __restrict__ wout,
                                                      char* __restrict__ ws, const char* __restrict__ outp) {
  extern __shared__ __attribute__((aligned(16))) char smem[];
  const int lane = threadIdx.x & 63;
  const int wid = __builtin_amdgcn_readfirstlane(threadIdx.x >> 6);
  const int xcd = blockIdx.x & 7, idx = blockIdx.x >> 3;
  const int g = lane >> 4, j = lane & 15;
  if (idx >= 28) {
    if (wid >= 8) return;
    STAMP_DECL STAMP();
    const int hw = ((idx - 28) * 8 + xcd) * 8 + wid;
    const char* pb = (const char*)wsp + (size_t)(xcd >> 1) * 512 * S_ * 2 + (size_t)(7168 + (((xcd & 1) * 4 + idx - 28) * 8 + wid) * 144) * 128;
    unsigned tdst = 0;
    {
      const int L0 = hw * 64 + lane, L1 = min(L0 + 16384, 26879);
      touch(tdst, L0 < 12544 ? (const char*)win + (size_t)L0 * 128 : (const char*)x + (size_t)(L0 - 12544) * 128);
      touch(tdst, (const char*)x + (size_t)(L1 - 12544) * 128);
    }
    if (idx == 29) {
      const char* b = wid < 3 ? ws + wid * (64u << 16) : wid < 5 ? (const char*)wsp + (wid - 3) * (64u << 16) : outp;
      const unsigned off = wid < 5 ? lane << 16 : min(lane, 27) << 16;
      asm volatile("global_load_dword %0, %1, %2" : "+v"(tdst) : "v"(off), "s"(b) : "memory");
    }
    if (idx == 28 && wid < 4) {
      unsigned long long pc; int d0, len;
      asm volatile("s_getpc_b64 %0\n.Lpf_here_%=:\n\ts_mov_b32 %1, _Z12k1_gemm_geluPKtS0_S0_S0_PcPKc-.Lpf_here_%=\n\t"
                   "s_mov_b32 %2, _Z6k3_outPcPt-_Z12k1_gemm_geluPKtS0_S0_S0_PcPKc" : "=s"(pc), "=s"(d0), "=s"(len));
      const unsigned off = min((unsigned)(wid * 8192 + lane * 128), (unsigned)(len + 2560));
      asm volatile("global_load_dword %0, %1, %2" : "+v"(tdst) : "v"(off), "s"(pc + (long long)d0) : "memory");
    }
#pragma unroll
    for (int i = 0; i < 3; ++i)
      asm volatile("global_load_dword %0, %1, %2" : "+v"(tdst) : "v"(min(lane, 47) * 128), "s"(pb + i * 6144) : "memory");
    STAMP();
    unsigned short v[4][8];
#pragma unroll
    for (int i = 0; i < 4; ++i) {
      const int b = min(hw + 256 * i, 783), kf = b / 28, dt = b % 28;
#pragma unroll
      for (int e = 0; e < 8; ++e) v[i][e] = wout[(size_t)(32 * kf + 8 * g + e) * D_ + 16 * dt + j];
    }
#pragma unroll
    for (int i = 0; i < 4; ++i) {
      const int b = min(hw + 256 * i, 783);
      v4u o;
#pragma unroll
      for (int q = 0; q < 4; ++q) o[q] = (unsigned)v[i][2 * q] | ((unsigned)v[i][2 * q + 1] << 16);
      *(v4u*)(ws + WS_WO + ((size_t)b * 64 + lane) * 16) = o;
    }
    asm volatile("s_waitcnt vmcnt(0)" : "+v"(tdst)::"memory");
    STAMP(); STAMP_OUT(3);
    return;
  }
  const int rp = (xcd >> 1) * 4 + (idx & 3);
  const int cp = (xcd & 1) * 7 + (idx >> 2);

  const unsigned sm0 = lds_addr(smem);
#define BAR_L() asm volatile("s_waitcnt lgkmcnt(0)\n\ts_barrier" ::: "memory")
#define BAR_N() asm volatile("s_barrier" ::: "memory")
#define WAITBAR_V(N) asm volatile("s_waitcnt vmcnt(" #N ")\n\ts_barrier" ::: "memory")
  if (wid >= 8) {
    const int w0 = (wid - 8) * 4;
    const char* xb0 = (const char*)(x + (size_t)(rp * 128 + w0 * 16) * D_);
    const unsigned xvo0 = (lane >> 3) * (D_ * 2) + (((lane & 7) ^ g) * 16);
    const unsigned xvo1 = (lane >> 3) * (D_ * 2) + (((lane & 7) ^ (g + 4)) * 16) + 8 * D_ * 2;
    const char* w1 = (const char*)(win + cp * 64 + (size_t)(w0 * 8) * F_);
    const char* w2 = w1 + FH_ * 2;
    const unsigned wvoa = (lane >> 3) * (F_ * 2) + (((lane & 7) ^ (2 * (g & 1))) * 16);
    const unsigned wvob = wvoa ^ 64;
    const unsigned xl = sm0 + w0 * 2048, wl = sm0 + K1_WB + w0 * 1024;
#define K1_WL(base, c, dst, dstr) { _Pragma("unroll") for (int q = 0; q < 4; ++q) \
      glds16((q & 1) ? wvob : wvoa, (base) + (size_t)((c) * 64 + q * 8) * (F_ * 2), (dst) + q * (dstr)); }
#define K1_C(c) { _Pragma("unroll") for (int q = 0; q < 4; ++q) { \
      glds16(xvo0, xb0 + q * 16 * D_ * 2 + (c) * 128, xl + (c) * K1_XS + q * 2048); \
      glds16(xvo1, xb0 + q * 16 * D_ * 2 + (c) * 128, xl + (c) * K1_XS + q * 2048 + 1024); } \
      K1_WL(w2, c, wl + (c) * 8192, 1024) }
    K1_C(0) K1_C(1) K1_C(2)
    WAITBAR_V(24);
    K1_C(3)
    WAITBAR_V(24);
    K1_C(4)
    WAITBAR_V(24);
    K1_C(5)
    WAITBAR_V(24);
    K1_WL(w2, 6, wl + 6 * 8192, 1024) K1_WL(w1, 0, wl + 7 * 8192, 1024) K1_WL(w1, 1, xl, 2048)
    WAITBAR_V(24);
    K1_WL(w1, 2, xl + 1024, 2048) K1_WL(w1, 3, xl + K1_XS, 2048) K1_WL(w1, 4, xl + K1_XS + 1024, 2048)
    WAITBAR_V(24);
    K1_WL(w1, 5, xl + 2 * K1_XS, 2048) K1_WL(w1, 6, xl + 2 * K1_XS + 1024, 2048)
    WAITBAR_V(28);
    WAITBAR_V(0);
    {
      unsigned tdst = 0;
      const char* pb = (const char*)wsp + (size_t)(xcd >> 1) * 512 * S_ * 2 + (size_t)((((xcd & 1) * 28 + idx) * 2 + wid - 8) * 64) * 128;
      asm volatile("global_load_dword %0, %1, %2\n\ts_waitcnt vmcnt(0)" : "+v"(tdst) : "v"(lane * 128), "s"(pb) : "memory");
    }
    return;
  }
  STAMP_DECL STAMP();
  const char* ex = ws + WS_EX + (size_t)(rp * 8 + wid) * EX_GRP;
  const unsigned gr_off = (g * 8 + (j >> 1)) * 8;
  const unsigned in_vo = cp * 4096 + (j & 1) * 256 + gr_off;
  const unsigned out_vo = (j & 1) * 4096 + cp * 256 + gr_off;
  const unsigned pin_vo = cp * 4096 + 3584 + j * 8;
  const unsigned pout_vo = j * 4096 + 3584 + cp * 8;
  v2u in[7], pin;
#define K1_LDIN() { _Pragma("unroll") for (int k = 0; k < 7; ++k) ld8_sc1(in[k], in_vo, ex + k * 512); ld8_sc1(pin, pin_vo, ex); }
#define K1_INREGS "+v"(in[0]), "+v"(in[1]), "+v"(in[2]), "+v"(in[3]), "+v"(in[4]), "+v"(in[5]), "+v"(in[6]), "+v"(pin)
  v4u xf[14];
  v4u sc0, sc1_, sc2, sc3;
  asm volatile("global_load_dwordx4 %0, %4, %5 sc1\n\tglobal_load_dwordx4 %1, %4, %5 offset:1024 sc1\n\t"
               "global_load_dwordx4 %2, %4, %5 offset:2048 sc1\n\tglobal_load_dwordx4 %3, %4, %5 offset:3072 sc1"
               : "=&v"(sc0), "=&v"(sc1_), "=&v"(sc2), "=&v"(sc3) : "v"(cp * 4096 + lane * 16), "s"(ex) : "memory");
  asm volatile("global_load_dwordx4 %0, %2, %3 offset:768\n\tglobal_load_dwordx4 %1, %2, %3 offset:832"
               : "=&v"(xf[12]), "=&v"(xf[13]) : "v"(j * (D_ * 2) + g * 16), "s"((const char*)(x + (size_t)(rp * 128 + wid * 16) * D_)) : "memory");
  STAMP();
  const int xrd0 = wid * 2048 + j * 128 + ((g ^ (j >> 1)) * 16), xrd1 = xrd0 ^ 64;
  const int fsw = 2 * (((j >> 3) & 1) + 2 * (g & 1));
  const int tra = g * 1024 + (j >> 2) * 128 + (((j >> 1) & 1) * 16) + 8 * (j & 1);
  const int trb = g * 2048 + (j >> 2) * 128 + (((j >> 1) & 1) * 16) + 8 * (j & 1);
  v4u wf[2][8];
  f32x4 acc[4];
#pragma unroll
  for (int n = 0; n < 4; ++n) acc[n] = (f32x4){0.f, 0.f, 0.f, 0.f};
#define K1_STEP(RDX, c, RDW, b, off, tb, kstr, MM, cm, ZT) { \
    if (RDX) { xf[2 * (c)] = *(const v4u*)(smem + (c) * K1_XS + xrd0); xf[2 * (c) + 1] = *(const v4u*)(smem + (c) * K1_XS + xrd1); } \
    _Pragma("unroll") for (int i = 0; i < 8; ++i) { \
      const int k2 = i >> 2, n = i & 3; \
      if (RDW) { const char* sb = smem + (off) + (tb) + (((2 * n) ^ fsw) * 16) + k2 * (kstr); \
        s16x4 t0 = __builtin_amdgcn_ds_read_tr16_b64_v4i16((lds_s16x4_p)(sb)); \
        s16x4 t1 = __builtin_amdgcn_ds_read_tr16_b64_v4i16((lds_s16x4_p)(sb + 512)); \
        v2u a_ = __builtin_bit_cast(v2u, t0), b_ = __builtin_bit_cast(v2u, t1); \
        wf[b][i] = (v4u){a_[0], a_[1], b_[0], b_[1]}; } \
      if (MM) acc[n] = ZT ? mfma16(xf[2 * (cm) + k2], wf[(b) ^ 1][i], acc[n]) : mfma16(wf[(b) ^ 1][i], xf[2 * (cm) + k2], acc[n]); \
      SCHED(); } }

  BAR_N();
  STAMP();
  K1_STEP(1, 0, 1, 0, K1_WB, tra, 4096, 0, 0, 1)
  BAR_L();
  K1_STEP(1, 1, 1, 1, K1_WB + 8192, tra, 4096, 1, 0, 1)
  asm volatile("s_waitcnt vmcnt(2)" : "+v"(sc0), "+v"(sc1_), "+v"(sc2), "+v"(sc3)::"memory");
  unsigned nonce = 1;
  while (__any(sc0[0] == nonce || sc0[2] == nonce || sc1_[0] == nonce || sc1_[2] == nonce || sc2[0] == nonce || sc2[2] == nonce ||
               sc3[0] == nonce || sc3[2] == nonce)) ++nonce;
  if (lane == 0) st4_sc1(nonce, EX_CH + cp * 128, ex);
  BAR_L();
  K1_STEP(1, 2, 1, 0, K1_WB + 2 * 8192, tra, 4096, 1, 1, 1)
  BAR_L();
  K1_STEP(1, 3, 1, 1, K1_WB + 3 * 8192, tra, 4096, 1, 2, 1)
  BAR_N();
  K1_STEP(1, 4, 1, 0, K1_WB + 4 * 8192, tra, 4096, 1, 3, 1)
  BAR_N();
  unsigned chal;
  ld4_sc1(chal, EX_CH + j * 128, ex);
  K1_STEP(1, 5, 1, 1, K1_WB + 5 * 8192, tra, 4096, 1, 4, 1)
  asm volatile("s_waitcnt vmcnt(2)\n\ts_barrier" : "+v"(xf[12]), "+v"(xf[13])::"memory");
  K1_STEP(0, 6, 1, 0, K1_WB + 6 * 8192, tra, 4096, 1, 5, 1)
  K1_STEP(0, 0, 0, 1, 0, trb, 8192, 1, 6, 1)
  STAMP();
  asm volatile("s_waitcnt vmcnt(0)" : "+v"(chal)::"memory");
  unsigned answered = chal;
  unsigned tg[7];
#pragma unroll
  for (int k = 0; k < 7; ++k) tg[k] = __shfl(answered, 2 * k + (j & 1));
  float v[4][4];
  unsigned myval;
  {
    float sh[4], S[4], Q[4];
#pragma unroll
    for (int n = 0; n < 4; ++n)
#pragma unroll
      for (int r = 0; r < 4; ++r) { v[n][r] = gelu_f(acc[n][r]); acc[n][r] = 0.f; }
#pragma unroll
    for (int r = 0; r < 4; ++r) {
      sh[r] = dpp_f<DPP_BC(0)>(v[0][r]);
      const float d0 = v[0][r] - sh[r], d1 = v[1][r] - sh[r], d2 = v[2][r] - sh[r], d3 = v[3][r] - sh[r];
      S[r] = (d0 + d1) + (d2 + d3);
      Q[r] = (d0 * d0 + d1 * d1) + (d2 * d2 + d3 * d3);
    }
    const bool b3 = j & 8, b2 = j & 4, b1 = j & 2;
    float T[4];
#pragma unroll
    for (int r = 0; r < 4; ++r) T[r] = (b3 ? Q[r] : S[r]) + dpp_f<DPP_XOR15>(b3 ? S[r] : Q[r]);
    const float U0 = (b2 ? T[2] : T[0]) + dpp_f<DPP_XOR7>(b2 ? T[0] : T[2]);
    const float U1 = (b2 ? T[3] : T[1]) + dpp_f<DPP_XOR7>(b2 ? T[1] : T[3]);
    float W = (b1 ? U1 : U0) + dpp_f<DPP_XOR3>(b1 ? U0 : U1);
    W += dpp_f<DPP_XOR1>(W);
    const float other = dpp_f<DPP_XOR8>(W);
    const float shr = b2 ? (b1 ? sh[3] : sh[2]) : (b1 ? sh[1] : sh[0]);
    myval = __builtin_bit_cast(unsigned, b3 ? W - other * other * (1.0f / 64.0f) : shr + W * (1.0f / 64.0f));
  }
#define K1_SEND(ans) { \
    _Pragma("unroll") for (int k = 0; k < 7; ++k) { const unsigned t_ = __shfl(ans, 2 * k + (j & 1)); st8_sc1((v2u){t_, myval}, out_vo, ex + k * 8192); } \
    if (lane < 16) st8_sc1((v2u){ans, nonce}, pout_vo, ex); }
#pragma unroll
  for (int k = 0; k < 7; ++k) st8_sc1((v2u){tg[k], myval}, out_vo, ex + k * 8192);
  if (lane < 16) st8_sc1((v2u){answered, nonce}, pout_vo, ex);
  STAMP();
  BAR_N();
  STAMP();
  K1_STEP(0, 0, 1, 0, K1_WB + 7 * 8192, tra, 4096, 0, 0, 0)
  K1_STEP(0, 0, 1, 1, 0, trb, 8192, 1, 0, 0)
  K1_STEP(0, 0, 1, 0, 1024, trb, 8192, 1, 1, 0)
  K1_STEP(0, 0, 1, 1, K1_XS, trb, 8192, 1, 2, 0)
  K1_STEP(0, 0, 1, 0, K1_XS + 1024, trb, 8192, 1, 3, 0)
  K1_STEP(0, 0, 1, 1, 2 * K1_XS, trb, 8192, 1, 4, 0)
  K1_STEP(0, 0, 1, 0, 2 * K1_XS + 1024, trb, 8192, 1, 5, 0)
  K1_STEP(0, 0, 0, 1, 0, trb, 8192, 1, 6, 0)
  STAMP();
  K1_LDIN()
  ld4_sc1(chal, EX_CH + j * 128, ex);
  {
    char* z1 = ws + WS_Z1 + ((size_t)((8 * rp + wid) * 56 + cp * 4) * 64 + lane) * 8;
#pragma unroll
    for (int n = 0; n < 4; ++n) {
      v2u o;
      o[0] = pack_bf16(gelu_f(acc[n][0]), gelu_f(acc[n][1]));
      o[1] = pack_bf16(gelu_f(acc[n][2]), gelu_f(acc[n][3]));
      st_plain8(o, z1 + n * 512);
    }
  }
  STAMP();
  asm volatile("s_waitcnt vmcnt(4)" : K1_INREGS, "+v"(chal)::"memory");
  for (unsigned spin = 0; spin < (1u << 16); ++spin) {
    bool okd = true;
#pragma unroll
    for (int k = 0; k < 7; ++k) okd = okd && in[k][0] == nonce;
    const bool pv = pin[0] == nonce;
    const unsigned cur = pv ? pin[1] : chal;
    const bool live = j < 14;
    if (__any(live && cur != answered)) { answered = cur; K1_SEND(answered) }
    if (__all(okd && (pv || !live))) break;
    K1_LDIN()
    ld4_sc1(chal, EX_CH + j * 128, ex);
    asm volatile("s_waitcnt vmcnt(0)" : K1_INREGS, "+v"(chal)::"memory");
  }
  STAMP();
  {
    float sm = ((asf(in[0][1]) + asf(in[1][1])) + (asf(in[2][1]) + asf(in[3][1]))) + ((asf(in[4][1]) + asf(in[5][1])) + asf(in[6][1]));
    sm += dpp_f<DPP_XOR1>(sm);
    const float mean = sm * (1.0f / 14.0f);
    float bt = 0.f;
#pragma unroll
    for (int k = 0; k < 7; ++k) { const float e = asf(in[k][1]) - mean; bt = __builtin_fmaf(e, e, bt); }
    bt += dpp_f<DPP_XOR1>(bt);
    const float between = dpp_f<DPP_XOR8>(bt);
    const float rstd = rsqrtf((sm + 64.0f * between) * (1.0f / FH_) + 1e-5f);
    float mr[4], rr[4];
    mr[0] = dpp_f<DPP_BC(0)>(mean); mr[1] = dpp_f<DPP_BC(2)>(mean); mr[2] = dpp_f<DPP_BC(4)>(mean); mr[3] = dpp_f<DPP_BC(6)>(mean);
    rr[0] = dpp_f<DPP_BC(8)>(rstd); rr[1] = dpp_f<DPP_BC(10)>(rstd); rr[2] = dpp_f<DPP_BC(12)>(rstd); rr[3] = dpp_f<DPP_BC(14)>(rstd);
    char* z2 = ws + WS_Z2 + ((size_t)((4 * rp + (wid >> 1)) * 56 + cp * 4) * 64 + j + 16 * (2 * (wid & 1) + (g >> 1))) * 16 + (g & 1) * 8;
#pragma unroll
    for (int n = 0; n < 4; ++n) {
      v2u o;
      o[0] = pack_bf16((v[n][0] - mr[0]) * rr[0], (v[n][1] - mr[1]) * rr[1]);
      o[1] = pack_bf16((v[n][2] - mr[2]) * rr[2], (v[n][3] - mr[3]) * rr[3]);
      *(v2u*)(z2 + n * 1024) = o;
    }
  }
  STAMP(); STAMP_OUT(1);
}

#define K2_LDS (4 * 32768 + 1024)

template <int OFF>
__device__ __forceinline__ void gload_a(v4u& dst, unsigned voff, const void* sbase) {
  asm volatile("global_load_dwordx4 %0, %1, %2 offset:%3" : "=a"(dst) : "v"(voff), "s"(sbase), "i"(OFF) : "memory");
}

__global__ __launch_bounds__(256, 1) void k2_spatial(const unsigned short* __restrict__ wsp, const unsigned short* __restrict__ bsp,
                                                    char* __restrict__ ws) {
  extern __shared__ __attribute__((aligned(16))) char smem[];
  const int lane = threadIdx.x & 63;
  const int wid = __builtin_amdgcn_readfirstlane(threadIdx.x >> 6);
  const int xcd = blockIdx.x & 7, idx = blockIdx.x >> 3;
  const int tp = (xcd >> 1) * 8 + (idx & 7);
  const int fp = (xcd & 1) * 4 + (idx >> 3);
  const int g = lane >> 4;

  STAMP_DECL STAMP();
  const char* abase = (const char*)(wsp + (size_t)(tp * 64) * S_ + wid * 64);
  const unsigned avo0 = (lane >> 3) * (S_ * 2) + (((lane & 7) ^ g) * 16);
  const unsigned avo1 = (lane >> 3) * (S_ * 2) + (((lane & 7) ^ (g + 4)) * 16);
  const unsigned ring = lds_addr(smem) + 1024 + wid * 32768;
  const char* stg = smem + 1024 + wid * 32768;
  const char* bbase = ws + WS_Z2 + ((size_t)(wid * 2) * 56 + fp * 7 + 3) * 1024;
  const unsigned bvo = lane * 16;
  const int rd0 = (lane & 15) * 128 + ((g ^ ((lane & 15) >> 1)) * 16);
  const int rd1 = rd0 ^ 64;

  v4u Bf[2][14];
  v4u Af[2][8];
  f32x4 acc[4][7];
#pragma unroll
  for (int m = 0; m < 4; ++m)
#pragma unroll
    for (int n = 0; n < 7; ++n) acc[m][n] = (f32x4){0.f, 0.f, 0.f, 0.f};

#define K2_ROT 0
  const int rot = K2_ROT;
#define RJ(j) (4 * (j))
#define LOAD_A1(j, i) glds16(((i) & 1) ? avo1 : avo0, abase + (size_t)(i) * 8 * (S_ * 2) + RJ(j) * 128, ring + ((j) & 3) * 8192 + (i) * 1024);
#define LOAD_A(j) LOAD_A1(j, 0) LOAD_A1(j, 1) LOAD_A1(j, 2) LOAD_A1(j, 3) LOAD_A1(j, 4) LOAD_A1(j, 5) LOAD_A1(j, 6) LOAD_A1(j, 7)
#define LOAD_B1(j, ks, n) gload_a<((n) - 3) * 1024>(Bf[(j) & 1][(ks) * 7 + (n)], bvo, bbase + (size_t)(2 * RJ(j) + (ks)) * 56 * 1024);
#define LOAD_Bk(j, ks) LOAD_B1(j, ks, 0) LOAD_B1(j, ks, 1) LOAD_B1(j, ks, 2) LOAD_B1(j, ks, 3) LOAD_B1(j, ks, 4) LOAD_B1(j, ks, 5) LOAD_B1(j, ks, 6)
#define LOAD_B(j) LOAD_Bk(j, 0) LOAD_Bk(j, 1)
#define READ_A(j) _Pragma("unroll") for (int m = 0; m < 4; ++m) _Pragma("unroll") for (int ks = 0; ks < 2; ++ks) \
    Af[(j) & 1][m * 2 + ks] = *(const v4u*)(stg + ((j) & 3) * 8192 + ((m + wid) & 3) * 2048 + (ks ? rd1 : rd0));
#define WAIT_B(N, P) asm volatile("s_waitcnt vmcnt(" #N ")" : "+a"(Bf[P][0]), "+a"(Bf[P][1]), "+a"(Bf[P][2]), "+a"(Bf[P][3]), "+a"(Bf[P][4]), \
    "+a"(Bf[P][5]), "+a"(Bf[P][6]), "+a"(Bf[P][7]), "+a"(Bf[P][8]), "+a"(Bf[P][9]), "+a"(Bf[P][10]), "+a"(Bf[P][11]), "+a"(Bf[P][12]), "+a"(Bf[P][13]) :: "memory");
#define MMA(j, ks, n) _Pragma("unroll") for (int m = 0; m < 4; ++m) acc[m][n] = mfma16(Bf[(j) & 1][(ks) * 7 + (n)], Af[(j) & 1][m * 2 + (ks)], acc[m][n]);
#define STEP_BODY(j, LB, LA) \
  MMA(j, 0, 0) SCHED(); if (LB) { LOAD_B1((j) + 2, 0, 0) } if (LA) { LOAD_A1((j) + 3, 0) } \
  MMA(j, 0, 1) SCHED(); if (LB) { LOAD_B1((j) + 2, 0, 1) } if (LA) { LOAD_A1((j) + 3, 1) } \
  MMA(j, 0, 2) SCHED(); if (LB) { LOAD_B1((j) + 2, 0, 2) } if (LA) { LOAD_A1((j) + 3, 2) } \
  MMA(j, 0, 3) SCHED(); if (LB) { LOAD_B1((j) + 2, 0, 3) } if (LA) { LOAD_A1((j) + 3, 3) } \
  MMA(j, 0, 4) SCHED(); if (LB) { LOAD_B1((j) + 2, 0, 4) } if (LA) { LOAD_A1((j) + 3, 4) } \
  MMA(j, 0, 5) SCHED(); if (LB) { LOAD_B1((j) + 2, 0, 5) } if (LA) { LOAD_A1((j) + 3, 5) } \
  MMA(j, 0, 6) SCHED(); if (LB) { LOAD_B1((j) + 2, 0, 6) } if (LA) { LOAD_A1((j) + 3, 6) } \
  MMA(j, 1, 0) SCHED(); if (LB) { LOAD_B1((j) + 2, 1, 0) } if (LA) { LOAD_A1((j) + 3, 7) } \
  MMA(j, 1, 1) SCHED(); if (LB) { LOAD_B1((j) + 2, 1, 1) } \
  MMA(j, 1, 2) SCHED(); if (LB) { LOAD_B1((j) + 2, 1, 2) } \
  MMA(j, 1, 3) SCHED(); if (LB) { LOAD_B1((j) + 2, 1, 3) } \
  MMA(j, 1, 4) SCHED(); if (LB) { LOAD_B1((j) + 2, 1, 4) } \
  MMA(j, 1, 5) SCHED(); if (LB) { LOAD_B1((j) + 2, 1, 5) } \
  MMA(j, 1, 6) SCHED(); if (LB) { LOAD_B1((j) + 2, 1, 6) } \
  SCHED();

  LOAD_A(0) LOAD_B(0) LOAD_A(1)
  LOAD_B(1) LOAD_A(2)
  STAMP();
  asm volatile("s_waitcnt vmcnt(44)" ::: "memory");
  STAMP();
  READ_A(0)
  SCHED();
  WAIT_B(22, 0) STAMP(); READ_A(1) SCHED(); STEP_BODY(0, 1, 1)
  WAIT_B(22, 1) STAMP(); READ_A(2) SCHED(); STEP_BODY(1, 1, 1)
  WAIT_B(22, 0) STAMP(); READ_A(3) SCHED(); STEP_BODY(2, 1, 1)
  WAIT_B(22, 1) STAMP(); READ_A(4) SCHED(); STEP_BODY(3, 1, 1)
  WAIT_B(22, 0) STAMP(); READ_A(5) SCHED(); STEP_BODY(4, 1, 1)
  WAIT_B(22, 1) STAMP(); READ_A(6) SCHED(); STEP_BODY(5, 1, 0)
  WAIT_B(14, 0) STAMP(); READ_A(7) SCHED();
  v2u z1v[7];
  {
    const v2u* z1 = (const v2u*)(ws + WS_Z1) + ((size_t)((4 * tp + wid) * 56 + fp * 7)) * 64 + lane;
#pragma unroll
    for (int n = 0; n < 7; ++n) z1v[n] = z1[n * 64];
  }
  const float bias = __builtin_bit_cast(float, (unsigned)bsp[tp * 64 + wid * 16 + (lane & 15)] << 16);

 STEP_BODY(6, 0, 0)
  WAIT_B(8, 1) STAMP(); SCHED();
  {
    char* mine = smem + 1024 + wid * 32768 + lane * 16;
#pragma unroll
    for (int mi = 1; mi <= 4; ++mi) {
      const int m = mi & 3;
#pragma unroll
      for (int ks = 0; ks < 2; ++ks)
#pragma unroll
        for (int n = 0; n < 7; ++n) acc[m][n] = mfma16(Bf[1][ks * 7 + n], Af[1][m * 2 + ks], acc[m][n]);
      SCHED();
      if (mi >= 2) {
#pragma unroll
        for (int n = 0; n < 7; ++n) *(f32x4*)(mine + ((mi - 1) * 7 + n) * 1024) = acc[mi - 1][n];
      }
      SCHED();
    }
  }
  STAMP();
  __syncthreads();
  STAMP();
  f32x4 o[7];
  {
    f32x4 pr[3][7];
#pragma unroll
    for (int d = 1; d < 4; ++d) {
      const char* src = smem + 1024 + ((wid - d) & 3) * 32768 + d * 7168 + lane * 16;
#pragma unroll
      for (int n = 0; n < 7; ++n) pr[d - 1][n] = *(const f32x4*)(src + n * 1024);
    }
    SCHED();
#pragma unroll
    for (int n = 0; n < 7; ++n) o[n] = (acc[0][n] + pr[0][n]) + (pr[1][n] + pr[2][n]);
  }
  char* hp = ws + WS_H + ((size_t)(4 * tp + wid) * 28 * 64 + (lane & 15) + 16 * (g >> 1)) * 16 + (g & 1) * 8;
#pragma unroll
  for (int n = 0; n < 7; ++n) {
    const int ft = fp * 7 + n;
    v2u h;
    h[0] = pack_bf16(bf_lo(z1v[n][0]) * (o[n][0] + bias), bf_hi(z1v[n][0]) * (o[n][1] + bias));
    h[1] = pack_bf16(bf_lo(z1v[n][1]) * (o[n][2] + bias), bf_hi(z1v[n][1]) * (o[n][3] + bias));
    *(v2u*)(hp + (ft >> 1) * 1024 + (ft & 1) * 512) = h;
  }
  STAMP(); STAMP_OUT(2);
}

#define K3_LDS (4 * 12 * 1024)
__global__ __launch_bounds__(256, 1) void k3_out(char* __restrict__ ws, unsigned short* __restrict__ out) {
  extern __shared__ __attribute__((aligned(16))) char smem[];
  const int lane = threadIdx.x & 63;
  const int wid = __builtin_amdgcn_readfirstlane(threadIdx.x >> 6);
  const int xcd = blockIdx.x & 7, idx = blockIdx.x >> 3;
  const int tp = xcd * 4 + (idx & 3);
  const int dp = idx >> 2;
  const int g = lane >> 4;
  STAMP_DECL STAMP();
  const v4u* hp = (const v4u*)(ws + WS_H) + ((size_t)(4 * tp) * 28 + wid * 7) * 64 + lane;
  const v4u* wp = (const v4u*)(ws + WS_WO) + ((size_t)(wid * 7) * 28 + dp * 4) * 64 + lane;
  v4u Hf[7][4], Wf[7][4];
#define K3_LD(k) { _Pragma("unroll") for (int n = 0; n < 4; ++n) Wf[k][n] = wp[((k) * 28 + n) * 64]; \
    _Pragma("unroll") for (int m = 0; m < 4; ++m) Hf[k][m] = hp[(((m + wid) & 3) * 28 + (k)) * 64]; SCHED(); }
#define K3_MM(k) { _Pragma("unroll") for (int m = 0; m < 4; ++m) _Pragma("unroll") for (int n = 0; n < 4; ++n) \
    acc[m][n] = mfma16(Wf[k][n], Hf[k][m], acc[m][n]); SCHED(); }
  f32x4 acc[4][4];
#pragma unroll
  for (int m = 0; m < 4; ++m)
#pragma unroll
    for (int n = 0; n < 4; ++n) acc[m][n] = (f32x4){0.f, 0.f, 0.f, 0.f};
  K3_LD(0) K3_LD(1) K3_LD(2) K3_LD(3)
  STAMP();
  K3_MM(0) K3_LD(4) K3_MM(1) K3_LD(5) K3_MM(2) K3_LD(6) K3_MM(3) K3_MM(4) K3_MM(5) K3_MM(6)
  STAMP();
#pragma unroll
  for (int m = 1; m < 4; ++m)
#pragma unroll
    for (int n = 0; n < 4; ++n) *(f32x4*)(smem + ((wid * 3 + m - 1) * 4 + n) * 1024 + lane * 16) = acc[m][n];
  __syncthreads();
  STAMP();
  f32x4 pr[3][4];
#pragma unroll
  for (int d = 1; d < 4; ++d)
#pragma unroll
    for (int n = 0; n < 4; ++n) pr[d - 1][n] = *(const f32x4*)(smem + ((((wid - d) & 3) * 3 + d - 1) * 4 + n) * 1024 + lane * 16);
  unsigned short* op = out + (size_t)(tp * 64 + wid * 16 + (lane & 15)) * D_ + dp * 64 + 4 * g;
#pragma unroll
  for (int n = 0; n < 4; ++n) {
    const f32x4 o = (acc[0][n] + pr[0][n]) + (pr[1][n] + pr[2][n]);
    v2u h;
    h[0] = pack_bf16(o[0], o[1]);
    h[1] = pack_bf16(o[2], o[3]);
    *(v2u*)(op + n * 16) = h;
  }
  STAMP(); STAMP_OUT(0);
}

extern "C" void kernel_launch(void* const* d_in, const int* in_sizes, int n_in,
                              void* d_out, int out_size, void* d_ws, size_t ws_size,
                              hipStream_t stream) {
  const unsigned short* x = (const unsigned short*)d_in[0];
  const unsigned short* win = (const unsigned short*)d_in[1];
  const unsigned short* wsp = (const unsigned short*)d_in[2];
  const unsigned short* bsp = (const unsigned short*)d_in[3];
  const unsigned short* wout = (const unsigned short*)d_in[4];
  char* ws = (char*)d_ws;
  (void)hipFuncSetAttribute((const void*)k1_gemm_gelu, hipFuncAttributeMaxDynamicSharedMemorySize, K1_LDS);
  (void)hipFuncSetAttribute((const void*)k2_spatial, hipFuncAttributeMaxDynamicSharedMemorySize, K2_LDS);
  (void)hipFuncSetAttribute((const void*)k3_out, hipFuncAttributeMaxDynamicSharedMemorySize, K3_LDS);
  k1_gemm_gelu<<<256, 640, K1_LDS, stream>>>(x, win, wsp, wout, ws, (const char*)d_out);
  k2_spatial<<<256, 256, K2_LDS, stream>>>(wsp, bsp, ws);
  k3_out<<<224, 256, K3_LDS, stream>>>(ws, (unsigned short*)d_out);
}
```

```cpp
#include <hip/hip_runtime.h>
#include <hip/hip_bf16.h>
#include <stdint.h>

typedef __attribute__((ext_vector_type(8))) short bf16x8;
typedef __attribute__((ext_vector_type(4))) short s16x4;
typedef __attribute__((ext_vector_type(4))) float f32x4;
typedef __attribute__((ext_vector_type(4))) unsigned v4u;
typedef __attribute__((ext_vector_type(2))) unsigned v2u;
typedef __attribute__((address_space(3))) s16x4* lds_s16x4_p;

#define S_ 2048
#define D_ 448
#define F_ 1792
#define FH_ 896

#define WS_Z1 0
#define WS_Z2 (WS_Z1 + 3670016)
#define WS_H  (WS_Z2 + 3670016)
#define WS_WO (WS_H + 3670016)

#define SCHED() __builtin_amdgcn_sched_barrier(0)
#define STAMP_DECL
#define STAMP()
#define STAMP_OUT(kid)

__device__ __forceinline__ f32x4 mfma16(v4u a, v4u b, f32x4 c) {
  return __builtin_amdgcn_mfma_f32_16x16x32_bf16(__builtin_bit_cast(bf16x8, a), __builtin_bit_cast(bf16x8, b), c, 0, 0, 0);
}
typedef __attribute__((ext_vector_type(2))) float f32x2;
typedef __attribute__((ext_vector_type(2))) __bf16 bf16x2;
__device__ __forceinline__ unsigned pack_bf16(float lo, float hi) {
  const bf16x2 r = __builtin_convertvector((f32x2){lo, hi}, bf16x2);
  return __builtin_bit_cast(unsigned, r);
}
__device__ __forceinline__ float asf(unsigned u) { return __builtin_bit_cast(float, u); }
__device__ __forceinline__ float bf_lo(unsigned u) { return __builtin_bit_cast(float, u << 16); }
__device__ __forceinline__ float bf_hi(unsigned u) { return __builtin_bit_cast(float, u & 0xffff0000u); }

__device__ __forceinline__ void glds16(unsigned voff, const void* sbase, unsigned lds) {
  asm volatile("s_mov_b32 m0, %2\n\ts_nop 0\n\tglobal_load_lds_dwordx4 %0, %1" ::"v"(voff), "s"(sbase), "s"(lds) : "memory");
}
template <int OFF>
__device__ __forceinline__ void glds16o(unsigned voff, const void* sbase, unsigned lds) {
  asm volatile("s_mov_b32 m0, %2\n\ts_nop 0\n\tglobal_load_lds_dwordx4 %0, %1 offset:%3" ::"v"(voff), "s"(sbase), "s"(lds - OFF), "i"(OFF) : "memory");
}
__device__ __forceinline__ unsigned lds_addr(const void* p) { return (unsigned)(size_t)(__attribute__((address_space(3))) const char*)p; }

__device__ __forceinline__ void touch(unsigned& dst, const void* p) { asm volatile("global_load_dword %0, %1, off" : "+v"(dst) : "v"(p) : "memory"); }
__device__ __forceinline__ void st_plain8(v2u d, void* p) { asm volatile("global_store_dwordx2 %0, %1, off" ::"v"(p), "v"(d) : "memory"); }

__device__ __forceinline__ float gelu_f(float x) {
  const float c0 = -2.0f * 0.7978845608028654f * 1.4426950408889634f;
  const float c1 = c0 * 0.044715f;
  float t = __builtin_fmaf(x * x, c1, c0);
  float e = __builtin_amdgcn_exp2f(x * t);
  return x * __builtin_amdgcn_rcpf(1.0f + e);
}

#define K1_XS 16384
#define K1_WB (6 * K1_XS)
#define K1_LDS (K1_WB + 8 * 8192)
#define WAITBAR(N) asm volatile("s_waitcnt vmcnt(" #N ") lgkmcnt(0)\n\ts_barrier" ::: "memory")
#define WS_EX (WS_WO + 802816)
#define EX_EC 4096
#define EX_CH 6144
#define EX_GRP 6400

__device__ __forceinline__ void ld8_sc1(v2u& d, unsigned voff, const void* sb) { asm volatile("global_load_dwordx2 %0, %1, %2 sc1" : "=v"(d) : "v"(voff), "s"(sb) : "memory"); }
__device__ __forceinline__ void ld4_sc1(unsigned& d, unsigned voff, const void* sb) { asm volatile("global_load_dword %0, %1, %2 sc1" : "=v"(d) : "v"(voff), "s"(sb) : "memory"); }
__device__ __forceinline__ void st8_sc1(v2u d, unsigned voff, const void* sb) { asm volatile("global_store_dwordx2 %0, %1, %2 sc1" ::"v"(voff), "v"(d), "s"(sb) : "memory"); }
__device__ __forceinline__ void st4_sc1(unsigned d, unsigned voff, const void* sb) { asm volatile("global_store_dword %0, %1, %2 sc1" ::"v"(voff), "v"(d), "s"(sb) : "memory"); }
template <int CTRL>
__device__ __forceinline__ float dpp_f(float v) {
  return __builtin_bit_cast(float, __builtin_amdgcn_update_dpp(0, __builtin_bit_cast(int, v), CTRL, 0xf, 0xf, true));
}
#define DPP_XOR1 0xB1
#define DPP_XOR3 0x1B
#define DPP_XOR7 0x141
#define DPP_XOR15 0x140
#define DPP_XOR8 0x128
#define DPP_BC(n) (0x150 + (n))

__global__ __launch_bounds__(640, 3) void k1_gemm_gelu(const unsigned short* __restrict__ x, const unsigned short* __restrict__ win,
                                                      const unsigned short* __restrict__ wsp, const unsigned short* __restrict__ wout,
                                                      char* __restrict__ ws, const char* __restrict__ outp) {
  extern __shared__ __attribute__((aligned(16))) char smem[];
  const int lane = threadIdx.x & 63;
  const int wid = __builtin_amdgcn_readfirstlane(threadIdx.x >> 6);
  const int xcd = blockIdx.x & 7, idx = blockIdx.x >> 3;
  const int g = lane >> 4, j = lane & 15;
  if (idx >= 28) {
    if (wid >= 8) return;
    STAMP_DECL STAMP();
    const int hw = ((idx - 28) * 8 + xcd) * 8 + wid;
    const char* pb = (const char*)wsp + ((size_t)((xcd >> 1) * 512 + ((xcd & 1) * 4 + idx - 28) * 64 + wid * 8) * S_) * 2;
    unsigned tdst = 0;
    if (idx == 29) {
      const char* b = wid < 3 ? ws + wid * (64u << 16) : wid < 5 ? (const char*)wsp + (wid - 3) * (64u << 16) : outp;
      const unsigned off = wid < 5 ? lane << 16 : min(lane, 27) << 16;
      asm volatile("global_load_dword %0, %1, %2" : "+v"(tdst) : "v"(off), "s"(b) : "memory");
    }
    if (idx == 28 && wid < 4) {
      unsigned long long pc; int d0, len;
      asm volatile("s_getpc_b64 %0\n.Lpf_here_%=:\n\ts_mov_b32 %1, _Z12k1_gemm_geluPKtS0_S0_S0_PcPKc-.Lpf_here_%=\n\t"
                   "s_mov_b32 %2, _Z6k3_outPcPt-_Z12k1_gemm_geluPKtS0_S0_S0_PcPKc" : "=s"(pc), "=s"(d0), "=s"(len));
      const unsigned off = min((unsigned)(wid * 8192 + lane * 128), (unsigned)(len + 2560));
      asm volatile("global_load_dword %0, %1, %2" : "+v"(tdst) : "v"(off), "s"(pc + (long long)d0) : "memory");
    }
#pragma unroll
    for (int i = 0; i < 4; ++i)
      asm volatile("global_load_dword %0, %1, %2" : "+v"(tdst) : "v"(lane * 128), "s"(pb + i * 8192) : "memory");
    STAMP();
    unsigned short v[4][8];
#pragma unroll
    for (int i = 0; i < 4; ++i) {
      const int b = min(hw + 256 * i, 783), kf = b / 28, dt = b % 28;
#pragma unroll
      for (int e = 0; e < 8; ++e) v[i][e] = wout[(size_t)(32 * kf + 8 * g + e) * D_ + 16 * dt + j];
    }
#pragma unroll
    for (int i = 0; i < 4; ++i) {
      const int b = min(hw + 256 * i, 783);
      v4u o;
#pragma unroll
      for (int q = 0; q < 4; ++q) o[q] = (unsigned)v[i][2 * q] | ((unsigned)v[i][2 * q + 1] << 16);
      *(v4u*)(ws + WS_WO + ((size_t)b * 64 + lane) * 16) = o;
    }
    asm volatile("s_waitcnt vmcnt(0)" : "+v"(tdst)::"memory");
    STAMP(); STAMP_OUT(3);
    return;
  }
  const int rp = (xcd >> 1) * 4 + (idx & 3);
  const int cp = (xcd & 1) * 7 + (idx >> 2);

  const unsigned sm0 = lds_addr(smem);
#define BAR_L() asm volatile("s_waitcnt lgkmcnt(0)\n\ts_barrier" ::: "memory")
#define BAR_N() asm volatile("s_barrier" ::: "memory")
#define WAITBAR_V(N) asm volatile("s_waitcnt vmcnt(" #N ")\n\ts_barrier" ::: "memory")
  if (wid >= 8) {
    const int w0 = (wid - 8) * 4;
    const char* xb0 = (const char*)(x + (size_t)(rp * 128 + w0 * 16) * D_);
    const unsigned xvo0 = (lane >> 3) * (D_ * 2) + (((lane & 7) ^ g) * 16);
    const unsigned xvo1 = (lane >> 3) * (D_ * 2) + (((lane & 7) ^ (g + 4)) * 16) + 8 * D_ * 2;
    const char* w1 = (const char*)(win + cp * 64 + (size_t)(w0 * 8) * F_);
    const char* w2 = w1 + FH_ * 2;
    const unsigned wvoa = (lane >> 3) * (F_ * 2) + (((lane & 7) ^ (2 * (g & 1))) * 16);
    const unsigned wvob = wvoa ^ 64;
    const unsigned xl = sm0 + w0 * 2048, wl = sm0 + K1_WB + w0 * 1024;
#define K1_WL(base, c, dst, dstr) { _Pragma("unroll") for (int q = 0; q < 4; ++q) \
      glds16((q & 1) ? wvob : wvoa, (base) + (size_t)((c) * 64 + q * 8) * (F_ * 2), (dst) + q * (dstr)); }
#define K1_C(c) { _Pragma("unroll") for (int q = 0; q < 4; ++q) { \
      glds16(xvo0, xb0 + q * 16 * D_ * 2 + (c) * 128, xl + (c) * K1_XS + q * 2048); \
      glds16(xvo1, xb0 + q * 16 * D_ * 2 + (c) * 128, xl + (c) * K1_XS + q * 2048 + 1024); } \
      K1_WL(w2, c, wl + (c) * 8192, 1024) }
    K1_C(0) K1_C(1) K1_C(2)
    WAITBAR_V(24);
    K1_C(3)
    WAITBAR_V(24);
    K1_C(4)
    WAITBAR_V(24);
    K1_C(5)
    WAITBAR_V(24);
    K1_WL(w2, 6, wl + 6 * 8192, 1024) K1_WL(w1, 0, wl + 7 * 8192, 1024) K1_WL(w1, 1, xl, 2048)
    WAITBAR_V(24);
    K1_WL(w1, 2, xl + 1024, 2048) K1_WL(w1, 3, xl + K1_XS, 2048) K1_WL(w1, 4, xl + K1_XS + 1024, 2048)
    WAITBAR_V(24);
    K1_WL(w1, 5, xl + 2 * K1_XS, 2048) K1_WL(w1, 6, xl + 2 * K1_XS + 1024, 2048)
    WAITBAR_V(28);
    WAITBAR_V(0);
    return;
  }
  STAMP_DECL STAMP();
  const char* ex = ws + WS_EX + (size_t)(rp * 8 + wid) * EX_GRP;
  const unsigned gr_off = (g * 8 + (j >> 1)) * 8;
  const unsigned in_vo = (j & 1) * 256 + gr_off;
  const unsigned pin_vo = EX_EC + cp * 128 + j * 8;
  const unsigned pout_vo = EX_EC + j * 128 + cp * 8;
  v2u in[7], pin;
#define K1_LDIN() { _Pragma("unroll") for (int k = 0; k < 7; ++k) ld8_sc1(in[k], in_vo, ex + k * 512); }
#define K1_INREGS "+v"(in[0]), "+v"(in[1]), "+v"(in[2]), "+v"(in[3]), "+v"(in[4]), "+v"(in[5]), "+v"(in[6])
  v4u xf[14];
  v2u scn;
  ld8_sc1(scn, lane < 32 ? cp * 256 + lane * 8 : pin_vo, ex);
  asm volatile("global_load_dwordx4 %0, %2, %3 offset:768\n\tglobal_load_dwordx4 %1, %2, %3 offset:832"
               : "=&v"(xf[12]), "=&v"(xf[13]) : "v"(j * (D_ * 2) + g * 16), "s"((const char*)(x + (size_t)(rp * 128 + wid * 16) * D_)) : "memory");
  STAMP();
  const int xrd0 = wid * 2048 + j * 128 + ((g ^ (j >> 1)) * 16), xrd1 = xrd0 ^ 64;
  const int fsw = 2 * (((j >> 3) & 1) + 2 * (g & 1));
  const int tra = g * 1024 + (j >> 2) * 128 + (((j >> 1) & 1) * 16) + 8 * (j & 1);
  const int trb = g * 2048 + (j >> 2) * 128 + (((j >> 1) & 1) * 16) + 8 * (j & 1);
  v4u wf[2][8];
  f32x4 acc[4];
#pragma unroll
  for (int n = 0; n < 4; ++n) acc[n] = (f32x4){0.f, 0.f, 0.f, 0.f};
#define K1_STEP(RDX, c, RDW, b, off, tb, kstr, MM, cm, ZT) { \
    if (RDX) { xf[2 * (c)] = *(const v4u*)(smem + (c) * K1_XS + xrd0); xf[2 * (c) + 1] = *(const v4u*)(smem + (c) * K1_XS + xrd1); } \
    _Pragma("unroll") for (int i = 0; i < 8; ++i) { \
      const int k2 = i >> 2, n = i & 3; \
      if (RDW) { const char* sb = smem + (off) + (tb) + (((2 * n) ^ fsw) * 16) + k2 * (kstr); \
        s16x4 t0 = __builtin_amdgcn_ds_read_tr16_b64_v4i16((lds_s16x4_p)(sb)); \
        s16x4 t1 = __builtin_amdgcn_ds_read_tr16_b64_v4i16((lds_s16x4_p)(sb + 512)); \
        v2u a_ = __builtin_bit_cast(v2u, t0), b_ = __builtin_bit_cast(v2u, t1); \
        wf[b][i] = (v4u){a_[0], a_[1], b_[0], b_[1]}; } \
      if (MM) acc[n] = ZT ? mfma16(xf[2 * (cm) + k2], wf[(b) ^ 1][i], acc[n]) : mfma16(wf[(b) ^ 1][i], xf[2 * (cm) + k2], acc[n]); \
      SCHED(); } }

  BAR_N();
  STAMP();
  K1_STEP(1, 0, 1, 0, K1_WB, tra, 4096, 0, 0, 1)
  asm volatile("s_waitcnt vmcnt(2)" : "+v"(scn)::"memory");
  unsigned nonce = 1;
  while (__any(scn[0] == nonce)) ++nonce;
  if (lane == 0) st4_sc1(nonce, EX_CH + cp * 4, ex);
  BAR_L();
  K1_STEP(1, 1, 1, 1, K1_WB + 8192, tra, 4096, 1, 0, 1)
  BAR_L();
  K1_STEP(1, 2, 1, 0, K1_WB + 2 * 8192, tra, 4096, 1, 1, 1)
  BAR_L();
  K1_STEP(1, 3, 1, 1, K1_WB + 3 * 8192, tra, 4096, 1, 2, 1)
  BAR_N();
  unsigned chal;
  ld4_sc1(chal, EX_CH + j * 4, ex);
  K1_STEP(1, 4, 1, 0, K1_WB + 4 * 8192, tra, 4096, 1, 3, 1)
  BAR_N();
  K1_STEP(1, 5, 1, 1, K1_WB + 5 * 8192, tra, 4096, 1, 4, 1)
  asm volatile("s_waitcnt vmcnt(2)\n\ts_barrier" : "+v"(xf[12]), "+v"(xf[13])::"memory");
  K1_STEP(0, 6, 1, 0, K1_WB + 6 * 8192, tra, 4096, 1, 5, 1)
  asm volatile("s_waitcnt vmcnt(0)" : "+v"(chal)::"memory");
  unsigned answered = chal;
  if (lane < 16) st8_sc1((v2u){answered, nonce}, pout_vo, ex);
  K1_STEP(0, 0, 0, 1, 0, trb, 8192, 1, 6, 1)
  STAMP();
  float v[4][4];
  unsigned myval;
  {
    float sh[4], S[4], Q[4];
#pragma unroll
    for (int n = 0; n < 4; ++n)
#pragma unroll
      for (int r = 0; r < 4; ++r) { v[n][r] = gelu_f(acc[n][r]); acc[n][r] = 0.f; }
#pragma unroll
    for (int r = 0; r < 4; ++r) {
      sh[r] = dpp_f<DPP_BC(0)>(v[0][r]);
      const float d0 = v[0][r] - sh[r], d1 = v[1][r] - sh[r], d2 = v[2][r] - sh[r], d3 = v[3][r] - sh[r];
      S[r] = (d0 + d1) + (d2 + d3);
      Q[r] = (d0 * d0 + d1 * d1) + (d2 * d2 + d3 * d3);
    }
    const bool b3 = j & 8, b2 = j & 4, b1 = j & 2;
    float T[4];
#pragma unroll
    for (int r = 0; r < 4; ++r) T[r] = (b3 ? Q[r] : S[r]) + dpp_f<DPP_XOR15>(b3 ? S[r] : Q[r]);
    const float U0 = (b2 ? T[2] : T[0]) + dpp_f<DPP_XOR7>(b2 ? T[0] : T[2]);
    const float U1 = (b2 ? T[3] : T[1]) + dpp_f<DPP_XOR7>(b2 ? T[1] : T[3]);
    float W = (b1 ? U1 : U0) + dpp_f<DPP_XOR3>(b1 ? U0 : U1);
    W += dpp_f<DPP_XOR1>(W);
    const float other = dpp_f<DPP_XOR8>(W);
    const float shr = b2 ? (b1 ? sh[3] : sh[2]) : (b1 ? sh[1] : sh[0]);
    myval = __builtin_bit_cast(unsigned, b3 ? W - other * other * (1.0f / 64.0f) : shr + W * (1.0f / 64.0f));
  }
  if (!(j & 1)) st8_sc1((v2u){nonce, myval}, cp * 256 + gr_off, ex);
  ld8_sc1(pin, pin_vo, ex);
  ld4_sc1(chal, EX_CH + j * 4, ex);
  STAMP();
  BAR_N();
  STAMP();
  K1_STEP(0, 0, 1, 0, K1_WB + 7 * 8192, tra, 4096, 0, 0, 0)
  K1_STEP(0, 0, 1, 1, 0, trb, 8192, 1, 0, 0)
  K1_STEP(0, 0, 1, 0, 1024, trb, 8192, 1, 1, 0)
  K1_STEP(0, 0, 1, 1, K1_XS, trb, 8192, 1, 2, 0)
  K1_STEP(0, 0, 1, 0, K1_XS + 1024, trb, 8192, 1, 3, 0)
  K1_STEP(0, 0, 1, 1, 2 * K1_XS, trb, 8192, 1, 4, 0)
  K1_STEP(0, 0, 1, 0, 2 * K1_XS + 1024, trb, 8192, 1, 5, 0)
  K1_STEP(0, 0, 0, 1, 0, trb, 8192, 1, 6, 0)
  STAMP();
  asm volatile("s_waitcnt vmcnt(0)" : "+v"(pin), "+v"(chal)::"memory");
  for (unsigned spin = 0; spin < (1u << 16); ++spin) {
    const bool pv = pin[0] == nonce, live = j < 14;
    const unsigned cur = pv ? pin[1] : chal;
    if (__any(live && cur != answered)) { answered = cur; if (lane < 16) st8_sc1((v2u){answered, nonce}, pout_vo, ex); }
    if (__all(pv || !live)) break;
    ld8_sc1(pin, pin_vo, ex);
    ld4_sc1(chal, EX_CH + j * 4, ex);
    asm volatile("s_waitcnt vmcnt(0)" : "+v"(pin), "+v"(chal)::"memory");
  }
  K1_LDIN()
  unsigned tg[7];
#pragma unroll
  for (int k = 0; k < 7; ++k) tg[k] = __shfl(answered, 2 * k + (j & 1));
  {
    char* z1 = ws + WS_Z1 + ((size_t)((8 * rp + wid) * 56 + cp * 4) * 64 + lane) * 8;
#pragma unroll
    for (int n = 0; n < 4; ++n) {
      v2u o;
      o[0] = pack_bf16(gelu_f(acc[n][0]), gelu_f(acc[n][1]));
      o[1] = pack_bf16(gelu_f(acc[n][2]), gelu_f(acc[n][3]));
      st_plain8(o, z1 + n * 512);
    }
  }
  STAMP();
  asm volatile("s_waitcnt vmcnt(4)" : K1_INREGS::"memory");
  for (unsigned spin = 0; spin < (1u << 16); ++spin) {
    bool okd = true;
#pragma unroll
    for (int k = 0; k < 7; ++k) okd = okd && in[k][0] == tg[k];
    if (__all(okd)) break;
    K1_LDIN()
    asm volatile("s_waitcnt vmcnt(0)" : K1_INREGS::"memory");
  }
  STAMP();
  {
    float sm = ((asf(in[0][1]) + asf(in[1][1])) + (asf(in[2][1]) + asf(in[3][1]))) + ((asf(in[4][1]) + asf(in[5][1])) + asf(in[6][1]));
    sm += dpp_f<DPP_XOR1>(sm);
    const float mean = sm * (1.0f / 14.0f);
    float bt = 0.f;
#pragma unroll
    for (int k = 0; k < 7; ++k) { const float e = asf(in[k][1]) - mean; bt = __builtin_fmaf(e, e, bt); }
    bt += dpp_f<DPP_XOR1>(bt);
    const float between = dpp_f<DPP_XOR8>(bt);
    const float rstd = rsqrtf((sm + 64.0f * between) * (1.0f / FH_) + 1e-5f);
    float mr[4], rr[4];
    mr[0] = dpp_f<DPP_BC(0)>(mean); mr[1] = dpp_f<DPP_BC(2)>(mean); mr[2] = dpp_f<DPP_BC(4)>(mean); mr[3] = dpp_f<DPP_BC(6)>(mean);
    rr[0] = dpp_f<DPP_BC(8)>(rstd); rr[1] = dpp_f<DPP_BC(10)>(rstd); rr[2] = dpp_f<DPP_BC(12)>(rstd); rr[3] = dpp_f<DPP_BC(14)>(rstd);
    char* z2 = ws + WS_Z2 + ((size_t)((4 * rp + (wid >> 1)) * 56 + cp * 4) * 64 + j + 16 * (2 * (wid & 1) + (g >> 1))) * 16 + (g & 1) * 8;
#pragma unroll
    for (int n = 0; n < 4; ++n) {
      v2u o;
      o[0] = pack_bf16((v[n][0] - mr[0]) * rr[0], (v[n][1] - mr[1]) * rr[1]);
      o[1] = pack_bf16((v[n][2] - mr[2]) * rr[2], (v[n][3] - mr[3]) * rr[3]);
      *(v2u*)(z2 + n * 1024) = o;
    }
  }
  STAMP(); STAMP_OUT(1);
}

#define K2_LDS (4 * 32768 + 1024)

template <int OFF>
__device__ __forceinline__ void gload_a(v4u& dst, unsigned voff, const void* sbase) {
  asm volatile("global_load_dwordx4 %0, %1, %2 offset:%3" : "=a"(dst) : "v"(voff), "s"(sbase), "i"(OFF) : "memory");
}

__global__ __launch_bounds__(256, 1) void k2_spatial(const unsigned short* __restrict__ wsp, const unsigned short* __restrict__ bsp,
                                                    char* __restrict__ ws) {
  extern __shared__ __attribute__((aligned(16))) char smem[];
  const int lane = threadIdx.x & 63;
  const int wid = __builtin_amdgcn_readfirstlane(threadIdx.x >> 6);
  const int xcd = blockIdx.x & 7, idx = blockIdx.x >> 3;
  const int tp = (xcd >> 1) * 8 + (idx & 7);
  const int fp = (xcd & 1) * 4 + (idx >> 3);
  const int g = lane >> 4;

  STAMP_DECL STAMP();
  const char* abase = (const char*)(wsp + (size_t)(tp * 64) * S_ + wid * 64);
  const unsigned avo0 = (lane >> 3) * (S_ * 2) + (((lane & 7) ^ g) * 16);
  const unsigned avo1 = (lane >> 3) * (S_ * 2) + (((lane & 7) ^ (g + 4)) * 16);
  const unsigned ring = lds_addr(smem) + 1024 + wid * 32768;
  const char* stg = smem + 1024 + wid * 32768;
  const char* bbase = ws + WS_Z2 + ((size_t)(wid * 2) * 56 + fp * 7 + 3) * 1024;
  const unsigned bvo = lane * 16;
  const int rd0 = (lane & 15) * 128 + ((g ^ ((lane & 15) >> 1)) * 16);
  const int rd1 = rd0 ^ 64;

  v4u Bf[2][14];
  v4u Af[2][8];
  f32x4 acc[4][7];
#pragma unroll
  for (int m = 0; m < 4; ++m)
#pragma unroll
    for (int n = 0; n < 7; ++n) acc[m][n] = (f32x4){0.f, 0.f, 0.f, 0.f};

#define K2_ROT 0
  const int rot = K2_ROT;
#define RJ(j) (4 * (j))
#define LOAD_A1(j, i) glds16(((i) & 1) ? avo1 : avo0, abase + (size_t)(i) * 8 * (S_ * 2) + RJ(j) * 128, ring + ((j) & 3) * 8192 + (i) * 1024);
#define LOAD_A(j) LOAD_A1(j, 0) LOAD_A1(j, 1) LOAD_A1(j, 2) LOAD_A1(j, 3) LOAD_A1(j, 4) LOAD_A1(j, 5) LOAD_A1(j, 6) LOAD_A1(j, 7)
#define LOAD_B1(j, ks, n) gload_a<((n) - 3) * 1024>(Bf[(j) & 1][(ks) * 7 + (n)], bvo, bbase + (size_t)(2 * RJ(j) + (ks)) * 56 * 1024);
#define LOAD_Bk(j, ks) LOAD_B1(j, ks, 0) LOAD_B1(j, ks, 1) LOAD_B1(j, ks, 2) LOAD_B1(j, ks, 3) LOAD_B1(j, ks, 4) LOAD_B1(j, ks, 5) LOAD_B1(j, ks, 6)
#define LOAD_B(j) LOAD_Bk(j, 0) LOAD_Bk(j, 1)
#define READ_A(j) _Pragma("unroll") for (int m = 0; m < 4; ++m) _Pragma("unroll") for (int ks = 0; ks < 2; ++ks) \
    Af[(j) & 1][m * 2 + ks] = *(const v4u*)(stg + ((j) & 3) * 8192 + ((m + wid) & 3) * 2048 + (ks ? rd1 : rd0));
#define WAIT_B(N, P) asm volatile("s_waitcnt vmcnt(" #N ")" : "+a"(Bf[P][0]), "+a"(Bf[P][1]), "+a"(Bf[P][2]), "+a"(Bf[P][3]), "+a"(Bf[P][4]), \
    "+a"(Bf[P][5]), "+a"(Bf[P][6]), "+a"(Bf[P][7]), "+a"(Bf[P][8]), "+a"(Bf[P][9]), "+a"(Bf[P][10]), "+a"(Bf[P][11]), "+a"(Bf[P][12]), "+a"(Bf[P][13]) :: "memory");
#define MMA(j, ks, n) _Pragma("unroll") for (int m = 0; m < 4; ++m) acc[m][n] = mfma16(Bf[(j) & 1][(ks) * 7 + (n)], Af[(j) & 1][m * 2 + (ks)], acc[m][n]);
#define STEP_BODY(j, LB, LA) \
  MMA(j, 0, 0) SCHED(); if (LB) { LOAD_B1((j) + 2, 0, 0) } if (LA) { LOAD_A1((j) + 3, 0) } \
  MMA(j, 0, 1) SCHED(); if (LB) { LOAD_B1((j) + 2, 0, 1) } if (LA) { LOAD_A1((j) + 3, 1) } \
  MMA(j, 0, 2) SCHED(); if (LB) { LOAD_B1((j) + 2, 0, 2) } if (LA) { LOAD_A1((j) + 3, 2) } \
  MMA(j, 0, 3) SCHED(); if (LB) { LOAD_B1((j) + 2, 0, 3) } if (LA) { LOAD_A1((j) + 3, 3) } \
  MMA(j, 0, 4) SCHED(); if (LB) { LOAD_B1((j) + 2, 0, 4) } if (LA) { LOAD_A1((j) + 3, 4) } \
  MMA(j, 0, 5) SCHED(); if (LB) { LOAD_B1((j) + 2, 0, 5) } if (LA) { LOAD_A1((j) + 3, 5) } \
  MMA(j, 0, 6) SCHED(); if (LB) { LOAD_B1((j) + 2, 0, 6) } if (LA) { LOAD_A1((j) + 3, 6) } \
  MMA(j, 1, 0) SCHED(); if (LB) { LOAD_B1((j) + 2, 1, 0) } if (LA) { LOAD_A1((j) + 3, 7) } \
  MMA(j, 1, 1) SCHED(); if (LB) { LOAD_B1((j) + 2, 1, 1) } \
  MMA(j, 1, 2) SCHED(); if (LB) { LOAD_B1((j) + 2, 1, 2) } \
  MMA(j, 1, 3) SCHED(); if (LB) { LOAD_B1((j) + 2, 1, 3) } \
  MMA(j, 1, 4) SCHED(); if (LB) { LOAD_B1((j) + 2, 1, 4) } \
  MMA(j, 1, 5) SCHED(); if (LB) { LOAD_B1((j) + 2, 1, 5) } \
  MMA(j, 1, 6) SCHED(); if (LB) { LOAD_B1((j) + 2, 1, 6) } \
  SCHED();

  LOAD_A(0) LOAD_B(0) LOAD_A(1)
  LOAD_B(1) LOAD_A(2)
  STAMP();
  asm volatile("s_waitcnt vmcnt(44)" ::: "memory");
  STAMP();
  READ_A(0)
  SCHED();
  WAIT_B(22, 0) STAMP(); READ_A(1) SCHED(); STEP_BODY(0, 1, 1)
  WAIT_B(22, 1) STAMP(); READ_A(2) SCHED(); STEP_BODY(1, 1, 1)
  WAIT_B(22, 0) STAMP(); READ_A(3) SCHED(); STEP_BODY(2, 1, 1)
  WAIT_B(22, 1) STAMP(); READ_A(4) SCHED(); STEP_BODY(3, 1, 1)
  WAIT_B(22, 0) STAMP(); READ_A(5) SCHED(); STEP_BODY(4, 1, 1)
  WAIT_B(22, 1) STAMP(); READ_A(6) SCHED(); STEP_BODY(5, 1, 0)
  WAIT_B(14, 0) STAMP(); READ_A(7) SCHED();
  v2u z1v[7];
  {
    const v2u* z1 = (const v2u*)(ws + WS_Z1) + ((size_t)((4 * tp + wid) * 56 + fp * 7)) * 64 + lane;
#pragma unroll
    for (int n = 0; n < 7; ++n) z1v[n] = z1[n * 64];
  }
  const float bias = __builtin_bit_cast(float, (unsigned)bsp[tp * 64 + wid * 16 + (lane & 15)] << 16);

 STEP_BODY(6, 0, 0)
  WAIT_B(8, 1) STAMP(); SCHED();
  {
    char* mine = smem + 1024 + wid * 32768 + lane * 16;
#pragma unroll
    for (int mi = 1; mi <= 4; ++mi) {
      const int m = mi & 3;
#pragma unroll
      for (int ks = 0; ks < 2; ++ks)
#pragma unroll
        for (int n = 0; n < 7; ++n) acc[m][n] = mfma16(Bf[1][ks * 7 + n], Af[1][m * 2 + ks], acc[m][n]);
      SCHED();
      if (mi >= 2) {
#pragma unroll
        for (int n = 0; n < 7; ++n) *(f32x4*)(mine + ((mi - 1) * 7 + n) * 1024) = acc[mi - 1][n];
      }
      SCHED();
    }
  }
  STAMP();
  __syncthreads();
  STAMP();
  f32x4 o[7];
  {
    f32x4 pr[3][7];
#pragma unroll
    for (int d = 1; d < 4; ++d) {
      const char* src = smem + 1024 + ((wid - d) & 3) * 32768 + d * 7168 + lane * 16;
#pragma unroll
      for (int n = 0; n < 7; ++n) pr[d - 1][n] = *(const f32x4*)(src + n * 1024);
    }
    SCHED();
#pragma unroll
    for (int n = 0; n < 7; ++n) o[n] = (acc[0][n] + pr[0][n]) + (pr[1][n] + pr[2][n]);
  }
  char* hp = ws + WS_H + ((size_t)(4 * tp + wid) * 28 * 64 + (lane & 15) + 16 * (g >> 1)) * 16 + (g & 1) * 8;
#pragma unroll
  for (int n = 0; n < 7; ++n) {
    const int ft = fp * 7 + n;
    v2u h;
    h[0] = pack_bf16(bf_lo(z1v[n][0]) * (o[n][0] + bias), bf_hi(z1v[n][0]) * (o[n][1] + bias));
    h[1] = pack_bf16(bf_lo(z1v[n][1]) * (o[n][2] + bias), bf_hi(z1v[n][1]) * (o[n][3] + bias));
    *(v2u*)(hp + (ft >> 1) * 1024 + (ft & 1) * 512) = h;
  }
  STAMP(); STAMP_OUT(2);
}

#define K3_LDS (4 * 12 * 1024)
__global__ __launch_bounds__(256, 1) void k3_out(char* __restrict__ ws, unsigned short* __restrict__ out) {
  extern __shared__ __attribute__((aligned(16))) char smem[];
  const int lane = threadIdx.x & 63;
  const int wid = __builtin_amdgcn_readfirstlane(threadIdx.x >> 6);
  const int xcd = blockIdx.x & 7, idx = blockIdx.x >> 3;
  const int tp = xcd * 4 + (idx & 3);
  const int dp = idx >> 2;
  const int g = lane >> 4;
  STAMP_DECL STAMP();
  const v4u* hp = (const v4u*)(ws + WS_H) + ((size_t)(4 * tp) * 28 + wid * 7) * 64 + lane;
  const v4u* wp = (const v4u*)(ws + WS_WO) + ((size_t)(wid * 7) * 28 + dp * 4) * 64 + lane;
  v4u Hf[7][4], Wf[7][4];
#define K3_LD(k) { _Pragma("unroll") for (int n = 0; n < 4; ++n) Wf[k][n] = wp[((k) * 28 + n) * 64]; \
    _Pragma("unroll") for (int m = 0; m < 4; ++m) Hf[k][m] = hp[(((m + wid) & 3) * 28 + (k)) * 64]; SCHED(); }
#define K3_MM(k) { _Pragma("unroll") for (int m = 0; m < 4; ++m) _Pragma("unroll") for (int n = 0; n < 4; ++n) \
    acc[m][n] = mfma16(Wf[k][n], Hf[k][m], acc[m][n]); SCHED(); }
  f32x4 acc[4][4];
#pragma unroll
  for (int m = 0; m < 4; ++m)
#pragma unroll
    for (int n = 0; n < 4; ++n) acc[m][n] = (f32x4){0.f, 0.f, 0.f, 0.f};
  K3_LD(0) K3_LD(1) K3_LD(2) K3_LD(3)
  STAMP();
  K3_MM(0) K3_LD(4) K3_MM(1) K3_LD(5) K3_MM(2) K3_LD(6) K3_MM(3) K3_MM(4) K3_MM(5) K3_MM(6)
  STAMP();
#pragma unroll
  for (int m = 1; m < 4; ++m)
#pragma unroll
    for (int n = 0; n < 4; ++n) *(f32x4*)(smem + ((wid * 3 + m - 1) * 4 + n) * 1024 + lane * 16) = acc[m][n];
  __syncthreads();
  STAMP();
  f32x4 pr[3][4];
#pragma unroll
  for (int d = 1; d < 4; ++d)
#pragma unroll
    for (int n = 0; n < 4; ++n) pr[d - 1][n] = *(const f32x4*)(smem + ((((wid - d) & 3) * 3 + d - 1) * 4 + n) * 1024 + lane * 16);
  unsigned short* op = out + (size_t)(tp * 64 + wid * 16 + (lane & 15)) * D_ + dp * 64 + 4 * g;
#pragma unroll
  for (int n = 0; n < 4; ++n) {
    const f32x4 o = (acc[0][n] + pr[0][n]) + (pr[1][n] + pr[2][n]);
    v2u h;
    h[0] = pack_bf16(o[0], o[1]);
    h[1] = pack_bf16(o[2], o[3]);
    *(v2u*)(op + n * 16) = h;
  }
  STAMP(); STAMP_OUT(0);
}

extern "C" void kernel_launch(void* const* d_in, const int* in_sizes, int n_in,
                              void* d_out, int out_size, void* d_ws, size_t ws_size,
                              hipStream_t stream) {
  const unsigned short* x = (const unsigned short*)d_in[0];
  const unsigned short* win = (const unsigned short*)d_in[1];
  const unsigned short* wsp = (const unsigned short*)d_in[2];
  const unsigned short* bsp = (const unsigned short*)d_in[3];
  const unsigned short* wout = (const unsigned short*)d_in[4];
  char* ws = (char*)d_ws;
  (void)hipFuncSetAttribute((const void*)k1_gemm_gelu, hipFuncAttributeMaxDynamicSharedMemorySize, K1_LDS);
  (void)hipFuncSetAttribute((const void*)k2_spatial, hipFuncAttributeMaxDynamicSharedMemorySize, K2_LDS);
  (void)hipFuncSetAttribute((const void*)k3_out, hipFuncAttributeMaxDynamicSharedMemorySize, K3_LDS);
  k1_gemm_gelu<<<256, 640, K1_LDS, stream>>>(x, win, wsp, wout, ws, (const char*)d_out);
  k2_spatial<<<256, 256, K2_LDS, stream>>>(wsp, bsp, ws);
  k3_out<<<224, 256, K3_LDS, stream>>>(ws, (unsigned short*)d_out);
}
```

```cpp
#include <hip/hip_runtime.h>
#include <hip/hip_bf16.h>
#include <stdint.h>

typedef __attribute__((ext_vector_type(8))) short bf16x8;
typedef __attribute__((ext_vector_type(4))) short s16x4;
typedef __attribute__((ext_vector_type(4))) float f32x4;
typedef __attribute__((ext_vector_type(4))) unsigned v4u;
typedef __attribute__((ext_vector_type(2))) unsigned v2u;
typedef __attribute__((address_space(3))) s16x4* lds_s16x4_p;

#define S_ 2048
#define D_ 448
#define F_ 1792
#define FH_ 896

#define WS_Z1 0
#define WS_Z2 (WS_Z1 + 3670016)
#define WS_H  (WS_Z2 + 3670016)
#define WS_WO (WS_H + 3670016)

#define SCHED() __builtin_amdgcn_sched_barrier(0)
#define STAMP_DECL
#define STAMP()
#define STAMP_OUT(kid)

__device__ __forceinline__ f32x4 mfma16(v4u a, v4u b, f32x4 c) {
  return __builtin_amdgcn_mfma_f32_16x16x32_bf16(__builtin_bit_cast(bf16x8, a), __builtin_bit_cast(bf16x8, b), c, 0, 0, 0);
}
typedef __attribute__((ext_vector_type(2))) float f32x2;
typedef __attribute__((ext_vector_type(2))) __bf16 bf16x2;
__device__ __forceinline__ unsigned pack_bf16(float lo, float hi) {
  const bf16x2 r = __builtin_convertvector((f32x2){lo, hi}, bf16x2);
  return __builtin_bit_cast(unsigned, r);
}
__device__ __forceinline__ float asf(unsigned u) { return __builtin_bit_cast(float, u); }
__device__ __forceinline__ float bf_lo(unsigned u) { return __builtin_bit_cast(float, u << 16); }
__device__ __forceinline__ float bf_hi(unsigned u) { return __builtin_bit_cast(float, u & 0xffff0000u); }

__device__ __forceinline__ void glds16(unsigned voff, const void* sbase, unsigned lds) {
  asm volatile("s_mov_b32 m0, %2\n\ts_nop 0\n\tglobal_load_lds_dwordx4 %0, %1" ::"v"(voff), "s"(sbase), "s"(lds) : "memory");
}
template <int OFF>
__device__ __forceinline__ void glds16o(unsigned voff, const void* sbase, unsigned lds) {
  asm volatile("s_mov_b32 m0, %2\n\ts_nop 0\n\tglobal_load_lds_dwordx4 %0, %1 offset:%3" ::"v"(voff), "s"(sbase), "s"(lds - OFF), "i"(OFF) : "memory");
}
__device__ __forceinline__ unsigned lds_addr(const void* p) { return (unsigned)(size_t)(__attribute__((address_space(3))) const char*)p; }

__device__ __forceinline__ void touch(unsigned& dst, const void* p) { asm volatile("global_load_dword %0, %1, off" : "+v"(dst) : "v"(p) : "memory"); }
__device__ __forceinline__ void st_plain8(v2u d, void* p) { asm volatile("global_store_dwordx2 %0, %1, off" ::"v"(p), "v"(d) : "memory"); }

__device__ __forceinline__ float gelu_f(float x) {
  const float c0 = -2.0f * 0.7978845608028654f * 1.4426950408889634f;
  const float c1 = c0 * 0.044715f;
  float t = __builtin_fmaf(x * x, c1, c0);
  float e = __builtin_amdgcn_exp2f(x * t);
  return x * __builtin_amdgcn_rcpf(1.0f + e);
}

#define K1_XS 16384
#define K1_WB (6 * K1_XS)
#define K1_LDS (K1_WB + 8 * 8192)
#define WAITBAR(N) asm volatile("s_waitcnt vmcnt(" #N ") lgkmcnt(0)\n\ts_barrier" ::: "memory")
#define WS_EX (WS_WO + 802816)
#define EX_EC 4096
#define EX_CH 6144
#define EX_GRP 6400

__device__ __forceinline__ void ld8_sc1(v2u& d, unsigned voff, const void* sb) { asm volatile("global_load_dwordx2 %0, %1, %2 sc1" : "=v"(d) : "v"(voff), "s"(sb) : "memory"); }
__device__ __forceinline__ void ld4_sc1(unsigned& d, unsigned voff, const void* sb) { asm volatile("global_load_dword %0, %1, %2 sc1" : "=v"(d) : "v"(voff), "s"(sb) : "memory"); }
__device__ __forceinline__ void st8_sc1(v2u d, unsigned voff, const void* sb) { asm volatile("global_store_dwordx2 %0, %1, %2 sc1" ::"v"(voff), "v"(d), "s"(sb) : "memory"); }
__device__ __forceinline__ void st4_sc1(unsigned d, unsigned voff, const void* sb) { asm volatile("global_store_dword %0, %1, %2 sc1" ::"v"(voff), "v"(d), "s"(sb) : "memory"); }
template <int CTRL>
__device__ __forceinline__ float dpp_f(float v) {
  return __builtin_bit_cast(float, __builtin_amdgcn_update_dpp(0, __builtin_bit_cast(int, v), CTRL, 0xf, 0xf, true));
}
#define DPP_XOR1 0xB1
#define DPP_XOR3 0x1B
#define DPP_XOR7 0x141
#define DPP_XOR15 0x140
#define DPP_XOR8 0x128
#define DPP_BC(n) (0x150 + (n))

__global__ __launch_bounds__(640, 3) void k1_gemm_gelu(const unsigned short* __restrict__ x, const unsigned short* __restrict__ win,
                                                      const unsigned short* __restrict__ wsp, const unsigned short* __restrict__ wout,
                                                      char* __restrict__ ws, const char* __restrict__ outp) {
  extern __shared__ __attribute__((aligned(16))) char smem[];
  const int lane = threadIdx.x & 63;
  const int wid = __builtin_amdgcn_readfirstlane(threadIdx.x >> 6);
  const int xcd = blockIdx.x & 7, idx = blockIdx.x >> 3;
  const int g = lane >> 4, j = lane & 15;
  if (idx >= 28) {
    if (wid >= 8) return;
    STAMP_DECL STAMP();
    const int hw = ((idx - 28) * 8 + xcd) * 8 + wid;
    const char* pb = (const char*)wsp + (size_t)(xcd >> 1) * 512 * S_ * 2 + (size_t)(14336 + (((xcd & 1) * 4 + idx - 28) * 8 + wid) * 32) * 128;
    unsigned tdst = 0;
    if (idx == 29) {
      const char* b = wid < 3 ? ws + wid * (64u << 16) : wid < 5 ? (const char*)wsp + (wid - 3) * (64u << 16) : outp;
      const unsigned off = wid < 5 ? lane << 16 : min(lane, 27) << 16;
      asm volatile("global_load_dword %0, %1, %2" : "+v"(tdst) : "v"(off), "s"(b) : "memory");
    }
    if (idx == 28 && wid < 4) {
      unsigned long long pc; int d0, len;
      asm volatile("s_getpc_b64 %0\n.Lpf_here_%=:\n\ts_mov_b32 %1, _Z12k1_gemm_geluPKtS0_S0_S0_PcPKc-.Lpf_here_%=\n\t"
                   "s_mov_b32 %2, _Z6k3_outPcPt-_Z12k1_gemm_geluPKtS0_S0_S0_PcPKc" : "=s"(pc), "=s"(d0), "=s"(len));
      const unsigned off = min((unsigned)(wid * 8192 + lane * 128), (unsigned)(len + 2560));
      asm volatile("global_load_dword %0, %1, %2" : "+v"(tdst) : "v"(off), "s"(pc + (long long)d0) : "memory");
    }
    asm volatile("global_load_dword %0, %1, %2" : "+v"(tdst) : "v"((lane & 31) * 128), "s"(pb) : "memory");
    STAMP();
    unsigned short v[4][8];
#pragma unroll
    for (int i = 0; i < 4; ++i) {
      const int b = min(hw + 256 * i, 783), kf = b / 28, dt = b % 28;
#pragma unroll
      for (int e = 0; e < 8; ++e) v[i][e] = wout[(size_t)(32 * kf + 8 * g + e) * D_ + 16 * dt + j];
    }
#pragma unroll
    for (int i = 0; i < 4; ++i) {
      const int b = min(hw + 256 * i, 783);
      v4u o;
#pragma unroll
      for (int q = 0; q < 4; ++q) o[q] = (unsigned)v[i][2 * q] | ((unsigned)v[i][2 * q + 1] << 16);
      *(v4u*)(ws + WS_WO + ((size_t)b * 64 + lane) * 16) = o;
    }
    asm volatile("s_waitcnt vmcnt(0)" : "+v"(tdst)::"memory");
    STAMP(); STAMP_OUT(3);
    return;
  }
  const int rp = (xcd >> 1) * 4 + (idx & 3);
  const int cp = (xcd & 1) * 7 + (idx >> 2);

  const unsigned sm0 = lds_addr(smem);
#define BAR_L() asm volatile("s_waitcnt lgkmcnt(0)\n\ts_barrier" ::: "memory")
#define BAR_N() asm volatile("s_barrier" ::: "memory")
#define WAITBAR_V(N) asm volatile("s_waitcnt vmcnt(" #N ")\n\ts_barrier" ::: "memory")
  if (wid >= 8) {
    const int w0 = (wid - 8) * 4;
    const char* xb0 = (const char*)(x + (size_t)(rp * 128 + w0 * 16) * D_);
    const unsigned xvo0 = (lane >> 3) * (D_ * 2) + (((lane & 7) ^ g) * 16);
    const unsigned xvo1 = (lane >> 3) * (D_ * 2) + (((lane & 7) ^ (g + 4)) * 16) + 8 * D_ * 2;
    const char* w1 = (const char*)(win + cp * 64 + (size_t)(w0 * 8) * F_);
    const char* w2 = w1 + FH_ * 2;
    const unsigned wvoa = (lane >> 3) * (F_ * 2) + (((lane & 7) ^ (2 * (g & 1))) * 16);
    const unsigned wvob = wvoa ^ 64;
    const unsigned xl = sm0 + w0 * 2048, wl = sm0 + K1_WB + w0 * 1024;
#define K1_WL(base, c, dst, dstr) { _Pragma("unroll") for (int q = 0; q < 4; ++q) \
      glds16((q & 1) ? wvob : wvoa, (base) + (size_t)((c) * 64 + q * 8) * (F_ * 2), (dst) + q * (dstr)); }
#define K1_C(c) { _Pragma("unroll") for (int q = 0; q < 4; ++q) { \
      glds16(xvo0, xb0 + q * 16 * D_ * 2 + (c) * 128, xl + (c) * K1_XS + q * 2048); \
      glds16(xvo1, xb0 + q * 16 * D_ * 2 + (c) * 128, xl + (c) * K1_XS + q * 2048 + 1024); } \
      K1_WL(w2, c, wl + (c) * 8192, 1024) }
    K1_C(0) K1_C(1) K1_C(2)
    WAITBAR_V(24);
    K1_C(3)
    WAITBAR_V(24);
    K1_C(4)
    WAITBAR_V(24);
    K1_C(5)
    WAITBAR_V(24);
    K1_WL(w2, 6, wl + 6 * 8192, 1024) K1_WL(w1, 0, wl + 7 * 8192, 1024) K1_WL(w1, 1, xl, 2048)
    WAITBAR_V(24);
    K1_WL(w1, 2, xl + 1024, 2048) K1_WL(w1, 3, xl + K1_XS, 2048) K1_WL(w1, 4, xl + K1_XS + 1024, 2048)
    WAITBAR_V(24);
    K1_WL(w1, 5, xl + 2 * K1_XS, 2048) K1_WL(w1, 6, xl + 2 * K1_XS + 1024, 2048)
    WAITBAR_V(28);
    WAITBAR_V(0);
    {
      unsigned tdst = 0;
      const char* pb = (const char*)wsp + (size_t)(xcd >> 1) * 512 * S_ * 2 + (size_t)((((xcd & 1) * 28 + idx) * 2 + wid - 8) * 128) * 128;
      asm volatile("global_load_dword %0, %1, %2" : "+v"(tdst) : "v"(lane * 128), "s"(pb + 0) : "memory");
      asm volatile("global_load_dword %0, %1, %2" : "+v"(tdst) : "v"(lane * 128), "s"(pb + 8192) : "memory");
      asm volatile("s_waitcnt vmcnt(0)" : "+v"(tdst)::"memory");
    }
    return;
  }
  STAMP_DECL STAMP();
  const char* ex = ws + WS_EX + (size_t)(rp * 8 + wid) * EX_GRP;
  const unsigned gr_off = (g * 8 + (j >> 1)) * 8;
  const unsigned in_vo = (j & 1) * 256 + gr_off;
  const unsigned pin_vo = EX_EC + cp * 128 + j * 8;
  const unsigned pout_vo = EX_EC + j * 128 + cp * 8;
  v2u in[7], pin;
#define K1_LDIN() { _Pragma("unroll") for (int k = 0; k < 7; ++k) ld8_sc1(in[k], in_vo, ex + k * 512); }
#define K1_INREGS "+v"(in[0]), "+v"(in[1]), "+v"(in[2]), "+v"(in[3]), "+v"(in[4]), "+v"(in[5]), "+v"(in[6])
  v4u xf[14];
  v2u scn;
  ld8_sc1(scn, lane < 32 ? cp * 256 + lane * 8 : pin_vo, ex);
  asm volatile("global_load_dwordx4 %0, %2, %3 offset:768\n\tglobal_load_dwordx4 %1, %2, %3 offset:832"
               : "=&v"(xf[12]), "=&v"(xf[13]) : "v"(j * (D_ * 2) + g * 16), "s"((const char*)(x + (size_t)(rp * 128 + wid * 16) * D_)) : "memory");
  STAMP();
  const int xrd0 = wid * 2048 + j * 128 + ((g ^ (j >> 1)) * 16), xrd1 = xrd0 ^ 64;
  const int fsw = 2 * (((j >> 3) & 1) + 2 * (g & 1));
  const int tra = g * 1024 + (j >> 2) * 128 + (((j >> 1) & 1) * 16) + 8 * (j & 1);
  const int trb = g * 2048 + (j >> 2) * 128 + (((j >> 1) & 1) * 16) + 8 * (j & 1);
  v4u wf[2][8];
  f32x4 acc[4];
#pragma unroll
  for (int n = 0; n < 4; ++n) acc[n] = (f32x4){0.f, 0.f, 0.f, 0.f};
#define K1_STEP(RDX, c, RDW, b, off, tb, kstr, MM, cm, ZT) { \
    if (RDX) { xf[2 * (c)] = *(const v4u*)(smem + (c) * K1_XS + xrd0); xf[2 * (c) + 1] = *(const v4u*)(smem + (c) * K1_XS + xrd1); } \
    _Pragma("unroll") for (int i = 0; i < 8; ++i) { \
      const int k2 = i >> 2, n = i & 3; \
      if (RDW) { const char* sb = smem + (off) + (tb) + (((2 * n) ^ fsw) * 16) + k2 * (kstr); \
        s16x4 t0 = __builtin_amdgcn_ds_read_tr16_b64_v4i16((lds_s16x4_p)(sb)); \
        s16x4 t1 = __builtin_amdgcn_ds_read_tr16_b64_v4i16((lds_s16x4_p)(sb + 512)); \
        v2u a_ = __builtin_bit_cast(v2u, t0), b_ = __builtin_bit_cast(v2u, t1); \
        wf[b][i] = (v4u){a_[0], a_[1], b_[0], b_[1]}; } \
      if (MM) acc[n] = ZT ? mfma16(xf[2 * (cm) + k2], wf[(b) ^ 1][i], acc[n]) : mfma16(wf[(b) ^ 1][i], xf[2 * (cm) + k2], acc[n]); \
      SCHED(); } }

  BAR_N();
  STAMP();
  K1_STEP(1, 0, 1, 0, K1_WB, tra, 4096, 0, 0, 1)
  asm volatile("s_waitcnt vmcnt(2)" : "+v"(scn)::"memory");
  unsigned nonce = 1;
  while (__any(scn[0] == nonce)) ++nonce;
  if (lane == 0) st4_sc1(nonce, EX_CH + cp * 4, ex);
  BAR_L();
  K1_STEP(1, 1, 1, 1, K1_WB + 8192, tra, 4096, 1, 0, 1)
  BAR_L();
  K1_STEP(1, 2, 1, 0, K1_WB + 2 * 8192, tra, 4096, 1, 1, 1)
  BAR_L();
  K1_STEP(1, 3, 1, 1, K1_WB + 3 * 8192, tra, 4096, 1, 2, 1)
  BAR_N();
  unsigned chal;
  ld4_sc1(chal, EX_CH + j * 4, ex);
  K1_STEP(1, 4, 1, 0, K1_WB + 4 * 8192, tra, 4096, 1, 3, 1)
  BAR_N();
  K1_STEP(1, 5, 1, 1, K1_WB + 5 * 8192, tra, 4096, 1, 4, 1)
  asm volatile("s_waitcnt vmcnt(2)\n\ts_barrier" : "+v"(xf[12]), "+v"(xf[13])::"memory");
  K1_STEP(0, 6, 1, 0, K1_WB + 6 * 8192, tra, 4096, 1, 5, 1)
  asm volatile("s_waitcnt vmcnt(0)" : "+v"(chal)::"memory");
  unsigned answered = chal;
  if (lane < 16) st8_sc1((v2u){answered, nonce}, pout_vo, ex);
  K1_STEP(0, 0, 0, 1, 0, trb, 8192, 1, 6, 1)
  STAMP();
  float v[4][4];
  unsigned myval;
  {
    float sh[4], S[4], Q[4];
#pragma unroll
    for (int n = 0; n < 4; ++n)
#pragma unroll
      for (int r = 0; r < 4; ++r) { v[n][r] = gelu_f(acc[n][r]); acc[n][r] = 0.f; }
#pragma unroll
    for (int r = 0; r < 4; ++r) {
      sh[r] = dpp_f<DPP_BC(0)>(v[0][r]);
      const float d0 = v[0][r] - sh[r], d1 = v[1][r] - sh[r], d2 = v[2][r] - sh[r], d3 = v[3][r] - sh[r];
      S[r] = (d0 + d1) + (d2 + d3);
      Q[r] = (d0 * d0 + d1 * d1) + (d2 * d2 + d3 * d3);
    }
    const bool b3 = j & 8, b2 = j & 4, b1 = j & 2;
    float T[4];
#pragma unroll
    for (int r = 0; r < 4; ++r) T[r] = (b3 ? Q[r] : S[r]) + dpp_f<DPP_XOR15>(b3 ? S[r] : Q[r]);
    const float U0 = (b2 ? T[2] : T[0]) + dpp_f<DPP_XOR7>(b2 ? T[0] : T[2]);
    const float U1 = (b2 ? T[3] : T[1]) + dpp_f<DPP_XOR7>(b2 ? T[1] : T[3]);
    float W = (b1 ? U1 : U0) + dpp_f<DPP_XOR3>(b1 ? U0 : U1);
    W += dpp_f<DPP_XOR1>(W);
    const float other = dpp_f<DPP_XOR8>(W);
    const float shr = b2 ? (b1 ? sh[3] : sh[2]) : (b1 ? sh[1] : sh[0]);
    myval = __builtin_bit_cast(unsigned, b3 ? W - other * other * (1.0f / 64.0f) : shr + W * (1.0f / 64.0f));
  }
  if (!(j & 1)) st8_sc1((v2u){nonce, myval}, cp * 256 + gr_off, ex);
  ld8_sc1(pin, pin_vo, ex);
  ld4_sc1(chal, EX_CH + j * 4, ex);
  STAMP();
  BAR_N();
  STAMP();
  K1_STEP(0, 0, 1, 0, K1_WB + 7 * 8192, tra, 4096, 0, 0, 0)
  K1_STEP(0, 0, 1, 1, 0, trb, 8192, 1, 0, 0)
  K1_STEP(0, 0, 1, 0, 1024, trb, 8192, 1, 1, 0)
  K1_STEP(0, 0, 1, 1, K1_XS, trb, 8192, 1, 2, 0)
  K1_STEP(0, 0, 1, 0, K1_XS + 1024, trb, 8192, 1, 3, 0)
  K1_STEP(0, 0, 1, 1, 2 * K1_XS, trb, 8192, 1, 4, 0)
  K1_STEP(0, 0, 1, 0, 2 * K1_XS + 1024, trb, 8192, 1, 5, 0)
  K1_STEP(0, 0, 0, 1, 0, trb, 8192, 1, 6, 0)
  STAMP();
  asm volatile("s_waitcnt vmcnt(0)" : "+v"(pin), "+v"(chal)::"memory");
  for (unsigned spin = 0; spin < (1u << 16); ++spin) {
    const bool pv = pin[0] == nonce, live = j < 14;
    const unsigned cur = pv ? pin[1] : chal;
    if (__any(live && cur != answered)) { answered = cur; if (lane < 16) st8_sc1((v2u){answered, nonce}, pout_vo, ex); }
    if (__all(pv || !live)) break;
    ld8_sc1(pin, pin_vo, ex);
    ld4_sc1(chal, EX_CH + j * 4, ex);
    asm volatile("s_waitcnt vmcnt(0)" : "+v"(pin), "+v"(chal)::"memory");
  }
  K1_LDIN()
  unsigned tg[7];
#pragma unroll
  for (int k = 0; k < 7; ++k) tg[k] = __shfl(answered, 2 * k + (j & 1));
  {
    char* z1 = ws + WS_Z1 + ((size_t)((8 * rp + wid) * 56 + cp * 4) * 64 + lane) * 8;
#pragma unroll
    for (int n = 0; n < 4; ++n) {
      v2u o;
      o[0] = pack_bf16(gelu_f(acc[n][0]), gelu_f(acc[n][1]));
      o[1] = pack_bf16(gelu_f(acc[n][2]), gelu_f(acc[n][3]));
      st_plain8(o, z1 + n * 512);
    }
  }
  STAMP();
  asm volatile("s_waitcnt vmcnt(4)" : K1_INREGS::"memory");
  for (unsigned spin = 0; spin < (1u << 16); ++spin) {
    bool okd = true;
#pragma unroll
    for (int k = 0; k < 7; ++k) okd = okd && in[k][0] == tg[k];
    if (__all(okd)) break;
    K1_LDIN()
    asm volatile("s_waitcnt vmcnt(0)" : K1_INREGS::"memory");
  }
  STAMP();
  {
    float sm = ((asf(in[0][1]) + asf(in[1][1])) + (asf(in[2][1]) + asf(in[3][1]))) + ((asf(in[4][1]) + asf(in[5][1])) + asf(in[6][1]));
    sm += dpp_f<DPP_XOR1>(sm);
    const float mean = sm * (1.0f / 14.0f);
    float bt = 0.f;
#pragma unroll
    for (int k = 0; k < 7; ++k) { const float e = asf(in[k][1]) - mean; bt = __builtin_fmaf(e, e, bt); }
    bt += dpp_f<DPP_XOR1>(bt);
    const float between = dpp_f<DPP_XOR8>(bt);
    const float rstd = rsqrtf((sm + 64.0f * between) * (1.0f / FH_) + 1e-5f);
    float mr[4], rr[4];
    mr[0] = dpp_f<DPP_BC(0)>(mean); mr[1] = dpp_f<DPP_BC(2)>(mean); mr[2] = dpp_f<DPP_BC(4)>(mean); mr[3] = dpp_f<DPP_BC(6)>(mean);
    rr[0] = dpp_f<DPP_BC(8)>(rstd); rr[1] = dpp_f<DPP_BC(10)>(rstd); rr[2] = dpp_f<DPP_BC(12)>(rstd); rr[3] = dpp_f<DPP_BC(14)>(rstd);
    char* z2 = ws + WS_Z2 + ((size_t)((4 * rp + (wid >> 1)) * 56 + cp * 4) * 64 + j + 16 * (2 * (wid & 1) + (g >> 1))) * 16 + (g & 1) * 8;
#pragma unroll
    for (int n = 0; n < 4; ++n) {
      v2u o;
      o[0] = pack_bf16((v[n][0] - mr[0]) * rr[0], (v[n][1] - mr[1]) * rr[1]);
      o[1] = pack_bf16((v[n][2] - mr[2]) * rr[2], (v[n][3] - mr[3]) * rr[3]);
      *(v2u*)(z2 + n * 1024) = o;
    }
  }
  STAMP(); STAMP_OUT(1);
}

#define K2_LDS (4 * 32768 + 1024)

template <int OFF>
__device__ __forceinline__ void gload_a(v4u& dst, unsigned voff, const void* sbase) {
  asm volatile("global_load_dwordx4 %0, %1, %2 offset:%3" : "=a"(dst) : "v"(voff), "s"(sbase), "i"(OFF) : "memory");
}

__global__ __launch_bounds__(256, 1) void k2_spatial(const unsigned short* __restrict__ wsp, const unsigned short* __restrict__ bsp,
                                                    char* __restrict__ ws) {
  extern __shared__ __attribute__((aligned(16))) char smem[];
  const int lane = threadIdx.x & 63;
  const int wid = __builtin_amdgcn_readfirstlane(threadIdx.x >> 6);
  const int xcd = blockIdx.x & 7, idx = blockIdx.x >> 3;
  const int tp = (xcd >> 1) * 8 + (idx & 7);
  const int fp = (xcd & 1) * 4 + (idx >> 3);
  const int g = lane >> 4;

  STAMP_DECL STAMP();
  const char* abase = (const char*)(wsp + (size_t)(tp * 64) * S_ + wid * 64);
  const unsigned avo0 = (lane >> 3) * (S_ * 2) + (((lane & 7) ^ g) * 16);
  const unsigned avo1 = (lane >> 3) * (S_ * 2) + (((lane & 7) ^ (g + 4)) * 16);
  const unsigned ring = lds_addr(smem) + 1024 + wid * 32768;
  const char* stg = smem + 1024 + wid * 32768;
  const char* bbase = ws + WS_Z2 + ((size_t)(wid * 2) * 56 + fp * 7 + 3) * 1024;
  const unsigned bvo = lane * 16;
  const int rd0 = (lane & 15) * 128 + ((g ^ ((lane & 15) >> 1)) * 16);
  const int rd1 = rd0 ^ 64;

  v4u Bf[2][14];
  v4u Af[2][8];
  f32x4 acc[4][7];
#pragma unroll
  for (int m = 0; m < 4; ++m)
#pragma unroll
    for (int n = 0; n < 7; ++n) acc[m][n] = (f32x4){0.f, 0.f, 0.f, 0.f};

#define K2_ROT 0
  const int rot = K2_ROT;
#define RJ(j) (4 * (j))
#define LOAD_A1(j, i) glds16(((i) & 1) ? avo1 : avo0, abase + (size_t)(i) * 8 * (S_ * 2) + RJ(j) * 128, ring + ((j) & 3) * 8192 + (i) * 1024);
#define LOAD_A(j) LOAD_A1(j, 0) LOAD_A1(j, 1) LOAD_A1(j, 2) LOAD_A1(j, 3) LOAD_A1(j, 4) LOAD_A1(j, 5) LOAD_A1(j, 6) LOAD_A1(j, 7)
#define LOAD_B1(j, ks, n) gload_a<((n) - 3) * 1024>(Bf[(j) & 1][(ks) * 7 + (n)], bvo, bbase + (size_t)(2 * RJ(j) + (ks)) * 56 * 1024);
#define LOAD_Bk(j, ks) LOAD_B1(j, ks, 0) LOAD_B1(j, ks, 1) LOAD_B1(j, ks, 2) LOAD_B1(j, ks, 3) LOAD_B1(j, ks, 4) LOAD_B1(j, ks, 5) LOAD_B1(j, ks, 6)
#define LOAD_B(j) LOAD_Bk(j, 0) LOAD_Bk(j, 1)
#define READ_A(j) _Pragma("unroll") for (int m = 0; m < 4; ++m) _Pragma("unroll") for (int ks = 0; ks < 2; ++ks) \
    Af[(j) & 1][m * 2 + ks] = *(const v4u*)(stg + ((j) & 3) * 8192 + ((m + wid) & 3) * 2048 + (ks ? rd1 : rd0));
#define WAIT_B(N, P) asm volatile("s_waitcnt vmcnt(" #N ")" : "+a"(Bf[P][0]), "+a"(Bf[P][1]), "+a"(Bf[P][2]), "+a"(Bf[P][3]), "+a"(Bf[P][4]), \
    "+a"(Bf[P][5]), "+a"(Bf[P][6]), "+a"(Bf[P][7]), "+a"(Bf[P][8]), "+a"(Bf[P][9]), "+a"(Bf[P][10]), "+a"(Bf[P][11]), "+a"(Bf[P][12]), "+a"(Bf[P][13]) :: "memory");
#define MMA(j, ks, n) _Pragma("unroll") for (int m = 0; m < 4; ++m) acc[m][n] = mfma16(Bf[(j) & 1][(ks) * 7 + (n)], Af[(j) & 1][m * 2 + (ks)], acc[m][n]);
#define STEP_BODY(j, LB, LA) \
  MMA(j, 0, 0) SCHED(); if (LB) { LOAD_B1((j) + 2, 0, 0) } if (LA) { LOAD_A1((j) + 3, 0) } \
  MMA(j, 0, 1) SCHED(); if (LB) { LOAD_B1((j) + 2, 0, 1) } if (LA) { LOAD_A1((j) + 3, 1) } \
  MMA(j, 0, 2) SCHED(); if (LB) { LOAD_B1((j) + 2, 0, 2) } if (LA) { LOAD_A1((j) + 3, 2) } \
  MMA(j, 0, 3) SCHED(); if (LB) { LOAD_B1((j) + 2, 0, 3) } if (LA) { LOAD_A1((j) + 3, 3) } \
  MMA(j, 0, 4) SCHED(); if (LB) { LOAD_B1((j) + 2, 0, 4) } if (LA) { LOAD_A1((j) + 3, 4) } \
  MMA(j, 0, 5) SCHED(); if (LB) { LOAD_B1((j) + 2, 0, 5) } if (LA) { LOAD_A1((j) + 3, 5) } \
  MMA(j, 0, 6) SCHED(); if (LB) { LOAD_B1((j) + 2, 0, 6) } if (LA) { LOAD_A1((j) + 3, 6) } \
  MMA(j, 1, 0) SCHED(); if (LB) { LOAD_B1((j) + 2, 1, 0) } if (LA) { LOAD_A1((j) + 3, 7) } \
  MMA(j, 1, 1) SCHED(); if (LB) { LOAD_B1((j) + 2, 1, 1) } \
  MMA(j, 1, 2) SCHED(); if (LB) { LOAD_B1((j) + 2, 1, 2) } \
  MMA(j, 1, 3) SCHED(); if (LB) { LOAD_B1((j) + 2, 1, 3) } \
  MMA(j, 1, 4) SCHED(); if (LB) { LOAD_B1((j) + 2, 1, 4) } \
  MMA(j, 1, 5) SCHED(); if (LB) { LOAD_B1((j) + 2, 1, 5) } \
  MMA(j, 1, 6) SCHED(); if (LB) { LOAD_B1((j) + 2, 1, 6) } \
  SCHED();

  LOAD_A(0) LOAD_B(0) LOAD_A(1)
  LOAD_B(1) LOAD_A(2)
  STAMP();
  asm volatile("s_waitcnt vmcnt(44)" ::: "memory");
  STAMP();
  READ_A(0)
  SCHED();
  WAIT_B(22, 0) STAMP(); READ_A(1) SCHED(); STEP_BODY(0, 1, 1)
  WAIT_B(22, 1) STAMP(); READ_A(2) SCHED(); STEP_BODY(1, 1, 1)
  WAIT_B(22, 0) STAMP(); READ_A(3) SCHED(); STEP_BODY(2, 1, 1)
  WAIT_B(22, 1) STAMP(); READ_A(4) SCHED(); STEP_BODY(3, 1, 1)
  WAIT_B(22, 0) STAMP(); READ_A(5) SCHED(); STEP_BODY(4, 1, 1)
  WAIT_B(22, 1) STAMP(); READ_A(6) SCHED(); STEP_BODY(5, 1, 0)
  WAIT_B(14, 0) STAMP(); READ_A(7) SCHED();
  v2u z1v[7];
  {
    const v2u* z1 = (const v2u*)(ws + WS_Z1) + ((size_t)((4 * tp + wid) * 56 + fp * 7)) * 64 + lane;
#pragma unroll
    for (int n = 0; n < 7; ++n) z1v[n] = z1[n * 64];
  }
  const float bias = __builtin_bit_cast(float, (unsigned)bsp[tp * 64 + wid * 16 + (lane & 15)] << 16);

 STEP_BODY(6, 0, 0)
  WAIT_B(8, 1) STAMP(); SCHED();
  {
    char* mine = smem + 1024 + wid * 32768 + lane * 16;
#pragma unroll
    for (int mi = 1; mi <= 4; ++mi) {
      const int m = mi & 3;
#pragma unroll
      for (int ks = 0; ks < 2; ++ks)
#pragma unroll
        for (int n = 0; n < 7; ++n) acc[m][n] = mfma16(Bf[1][ks * 7 + n], Af[1][m * 2 + ks], acc[m][n]);
      SCHED();
      if (mi >= 2) {
#pragma unroll
        for (int n = 0; n < 7; ++n) *(f32x4*)(mine + ((mi - 1) * 7 + n) * 1024) = acc[mi - 1][n];
      }
      SCHED();
    }
  }
  STAMP();
  __syncthreads();
  STAMP();
  f32x4 o[7];
  {
    f32x4 pr[3][7];
#pragma unroll
    for (int d = 1; d < 4; ++d) {
      const char* src = smem + 1024 + ((wid - d) & 3) * 32768 + d * 7168 + lane * 16;
#pragma unroll
      for (int n = 0; n < 7; ++n) pr[d - 1][n] = *(const f32x4*)(src + n * 1024);
    }
    SCHED();
#pragma unroll
    for (int n = 0; n < 7; ++n) o[n] = (acc[0][n] + pr[0][n]) + (pr[1][n] + pr[2][n]);
  }
  char* hp = ws + WS_H + ((size_t)(4 * tp + wid) * 28 * 64 + (lane & 15) + 16 * (g >> 1)) * 16 + (g & 1) * 8;
#pragma unroll
  for (int n = 0; n < 7; ++n) {
    const int ft = fp * 7 + n;
    v2u h;
    h[0] = pack_bf16(bf_lo(z1v[n][0]) * (o[n][0] + bias), bf_hi(z1v[n][0]) * (o[n][1] + bias));
    h[1] = pack_bf16(bf_lo(z1v[n][1]) * (o[n][2] + bias), bf_hi(z1v[n][1]) * (o[n][3] + bias));
    *(v2u*)(hp + (ft >> 1) * 1024 + (ft & 1) * 512) = h;
  }
  STAMP(); STAMP_OUT(2);
}

#define K3_LDS (4 * 12 * 1024)
__global__ __launch_bounds__(256, 1) void k3_out(char* __restrict__ ws, unsigned short* __restrict__ out) {
  extern __shared__ __attribute__((aligned(16))) char smem[];
  const int lane = threadIdx.x & 63;
  const int wid = __builtin_amdgcn_readfirstlane(threadIdx.x >> 6);
  const int xcd = blockIdx.x & 7, idx = blockIdx.x >> 3;
  const int tp = xcd * 4 + (idx & 3);
  const int dp = idx >> 2;
  const int g = lane >> 4;
  STAMP_DECL STAMP();
  const v4u* hp = (const v4u*)(ws + WS_H) + ((size_t)(4 * tp) * 28 + wid * 7) * 64 + lane;
  const v4u* wp = (const v4u*)(ws + WS_WO) + ((size_t)(wid * 7) * 28 + dp * 4) * 64 + lane;
  v4u Hf[7][4], Wf[7][4];
#define K3_LD(k) { _Pragma("unroll") for (int n = 0; n < 4; ++n) Wf[k][n] = wp[((k) * 28 + n) * 64]; \
    _Pragma("unroll") for (int m = 0; m < 4; ++m) Hf[k][m] = hp[(((m + wid) & 3) * 28 + (k)) * 64]; SCHED(); }
#define K3_MM(k) { _Pragma("unroll") for (int m = 0; m < 4; ++m) _Pragma("unroll") for (int n = 0; n < 4; ++n) \
    acc[m][n] = mfma16(Wf[k][n], Hf[k][m], acc[m][n]); SCHED(); }
  f32x4 acc[4][4];
#pragma unroll
  for (int m = 0; m < 4; ++m)
#pragma unroll
    for (int n = 0; n < 4; ++n) acc[m][n] = (f32x4){0.f, 0.f, 0.f, 0.f};
  K3_LD(0) K3_LD(1) K3_LD(2) K3_LD(3)
  STAMP();
  K3_MM(0) K3_LD(4) K3_MM(1) K3_LD(5) K3_MM(2) K3_LD(6) K3_MM(3) K3_MM(4) K3_MM(5) K3_MM(6)
  STAMP();
#pragma unroll
  for (int m = 1; m < 4; ++m)
#pragma unroll
    for (int n = 0; n < 4; ++n) *(f32x4*)(smem + ((wid * 3 + m - 1) * 4 + n) * 1024 + lane * 16) = acc[m][n];
  __syncthreads();
  STAMP();
  f32x4 pr[3][4];
#pragma unroll
  for (int d = 1; d < 4; ++d)
#pragma unroll
    for (int n = 0; n < 4; ++n) pr[d - 1][n] = *(const f32x4*)(smem + ((((wid - d) & 3) * 3 + d - 1) * 4 + n) * 1024 + lane * 16);
  unsigned short* op = out + (size_t)(tp * 64 + wid * 16 + (lane & 15)) * D_ + dp * 64 + 4 * g;
#pragma unroll
  for (int n = 0; n < 4; ++n) {
    const f32x4 o = (acc[0][n] + pr[0][n]) + (pr[1][n] + pr[2][n]);
    v2u h;
    h[0] = pack_bf16(o[0], o[1]);
    h[1] = pack_bf16(o[2], o[3]);
    *(v2u*)(op + n * 16) = h;
  }
  STAMP(); STAMP_OUT(0);
}

extern "C" void kernel_launch(void* const* d_in, const int* in_sizes, int n_in,
                              void* d_out, int out_size, void* d_ws, size_t ws_size,
                              hipStream_t stream) {
  const unsigned short* x = (const unsigned short*)d_in[0];
  const unsigned short* win = (const unsigned short*)d_in[1];
  const unsigned short* wsp = (const unsigned short*)d_in[2];
  const unsigned short* bsp = (const unsigned short*)d_in[3];
  const unsigned short* wout = (const unsigned short*)d_in[4];
  char* ws = (char*)d_ws;
  (void)hipFuncSetAttribute((const void*)k1_gemm_gelu, hipFuncAttributeMaxDynamicSharedMemorySize, K1_LDS);
  (void)hipFuncSetAttribute((const void*)k2_spatial, hipFuncAttributeMaxDynamicSharedMemorySize, K2_LDS);
  (void)hipFuncSetAttribute((const void*)k3_out, hipFuncAttributeMaxDynamicSharedMemorySize, K3_LDS);
  k1_gemm_gelu<<<256, 640, K1_LDS, stream>>>(x, win, wsp, wout, ws, (const char*)d_out);
  k2_spatial<<<256, 256, K2_LDS, stream>>>(wsp, bsp, ws);
  k3_out<<<224, 256, K3_LDS, stream>>>(ws, (unsigned short*)d_out);
}
```

```cpp
#include <hip/hip_runtime.h>
#include <hip/hip_bf16.h>
#include <stdint.h>

typedef __attribute__((ext_vector_type(8))) short bf16x8;
typedef __attribute__((ext_vector_type(4))) short s16x4;
typedef __attribute__((ext_vector_type(4))) float f32x4;
typedef __attribute__((ext_vector_type(4))) unsigned v4u;
typedef __attribute__((ext_vector_type(2))) unsigned v2u;
typedef __attribute__((address_space(3))) s16x4* lds_s16x4_p;

#define S_ 2048
#define D_ 448
#define F_ 1792
#define FH_ 896

#define WS_Z1 0
#define WS_Z2 (WS_Z1 + 3670016)
#define WS_H  (WS_Z2 + 3670016)
#define WS_WO (WS_H + 3670016)

#define SCHED() __builtin_amdgcn_sched_barrier(0)
#define STAMP_DECL
#define STAMP()
#define STAMP_OUT(kid)

__device__ __forceinline__ f32x4 mfma16(v4u a, v4u b, f32x4 c) {
  return __builtin_amdgcn_mfma_f32_16x16x32_bf16(__builtin_bit_cast(bf16x8, a), __builtin_bit_cast(bf16x8, b), c, 0, 0, 0);
}
typedef __attribute__((ext_vector_type(2))) float f32x2;
typedef __attribute__((ext_vector_type(2))) __bf16 bf16x2;
__device__ __forceinline__ unsigned pack_bf16(float lo, float hi) {
  const bf16x2 r = __builtin_convertvector((f32x2){lo, hi}, bf16x2);
  return __builtin_bit_cast(unsigned, r);
}
__device__ __forceinline__ float asf(unsigned u) { return __builtin_bit_cast(float, u); }
__device__ __forceinline__ float bf_lo(unsigned u) { return __builtin_bit_cast(float, u << 16); }
__device__ __forceinline__ float bf_hi(unsigned u) { return __builtin_bit_cast(float, u & 0xffff0000u); }

__device__ __forceinline__ void glds16(unsigned voff, const void* sbase, unsigned lds) {
  asm volatile("s_mov_b32 m0, %2\n\ts_nop 0\n\tglobal_load_lds_dwordx4 %0, %1" ::"v"(voff), "s"(sbase), "s"(lds) : "memory");
}
template <int OFF>
__device__ __forceinline__ void glds16o(unsigned voff, const void* sbase, unsigned lds) {
  asm volatile("s_mov_b32 m0, %2\n\ts_nop 0\n\tglobal_load_lds_dwordx4 %0, %1 offset:%3" ::"v"(voff), "s"(sbase), "s"(lds - OFF), "i"(OFF) : "memory");
}
__device__ __forceinline__ unsigned lds_addr(const void* p) { return (unsigned)(size_t)(__attribute__((address_space(3))) const char*)p; }

__device__ __forceinline__ void touch(unsigned& dst, const void* p) { asm volatile("global_load_dword %0, %1, off" : "+v"(dst) : "v"(p) : "memory"); }
__device__ __forceinline__ void st_plain8(v2u d, void* p) { asm volatile("global_store_dwordx2 %0, %1, off sc1" ::"v"(p), "v"(d) : "memory"); }

__device__ __forceinline__ float gelu_f(float x) {
  const float c0 = -2.0f * 0.7978845608028654f * 1.4426950408889634f;
  const float c1 = c0 * 0.044715f;
  float t = __builtin_fmaf(x * x, c1, c0);
  float e = __builtin_amdgcn_exp2f(x * t);
  return x * __builtin_amdgcn_rcpf(1.0f + e);
}

#define K1_XS 16384
#define K1_WB (6 * K1_XS)
#define K1_LDS (K1_WB + 8 * 8192)
#define WAITBAR(N) asm volatile("s_waitcnt vmcnt(" #N ") lgkmcnt(0)\n\ts_barrier" ::: "memory")
#define WS_EX (WS_WO + 802816)
#define EX_EC 4096
#define EX_CH 6144
#define EX_GRP 6400

__device__ __forceinline__ void ld8_sc1(v2u& d, unsigned voff, const void* sb) { asm volatile("global_load_dwordx2 %0, %1, %2 sc1" : "=v"(d) : "v"(voff), "s"(sb) : "memory"); }
__device__ __forceinline__ void ld4_sc1(unsigned& d, unsigned voff, const void* sb) { asm volatile("global_load_dword %0, %1, %2 sc1" : "=v"(d) : "v"(voff), "s"(sb) : "memory"); }
__device__ __forceinline__ void st8_sc1(v2u d, unsigned voff, const void* sb) { asm volatile("global_store_dwordx2 %0, %1, %2 sc1" ::"v"(voff), "v"(d), "s"(sb) : "memory"); }
__device__ __forceinline__ void st4_sc1(unsigned d, unsigned voff, const void* sb) { asm volatile("global_store_dword %0, %1, %2 sc1" ::"v"(voff), "v"(d), "s"(sb) : "memory"); }
template <int CTRL>
__device__ __forceinline__ float dpp_f(float v) {
  return __builtin_bit_cast(float, __builtin_amdgcn_update_dpp(0, __builtin_bit_cast(int, v), CTRL, 0xf, 0xf, true));
}
#define DPP_XOR1 0xB1
#define DPP_XOR3 0x1B
#define DPP_XOR7 0x141
#define DPP_XOR15 0x140
#define DPP_XOR8 0x128
#define DPP_BC(n) (0x150 + (n))

__global__ __launch_bounds__(640, 3) void k1_gemm_gelu(const unsigned short* __restrict__ x, const unsigned short* __restrict__ win,
                                                      const unsigned short* __restrict__ wsp, const unsigned short* __restrict__ wout,
                                                      char* __restrict__ ws, const char* __restrict__ outp) {
  extern __shared__ __attribute__((aligned(16))) char smem[];
  const int lane = threadIdx.x & 63;
  const int wid = __builtin_amdgcn_readfirstlane(threadIdx.x >> 6);
  const int xcd = blockIdx.x & 7, idx = blockIdx.x >> 3;
  const int g = lane >> 4, j = lane & 15;
  if (idx >= 28) {
    if (wid >= 8) return;
    STAMP_DECL STAMP();
    const int hw = ((idx - 28) * 8 + xcd) * 8 + wid;
    const char* pb = (const char*)wsp + ((size_t)((xcd >> 1) * 512 + ((xcd & 1) * 4 + idx - 28) * 64 + wid * 8) * S_) * 2;
    unsigned tdst = 0;
    if (idx == 29) {
      const char* b = wid < 3 ? ws + wid * (64u << 16) : wid < 5 ? (const char*)wsp + (wid - 3) * (64u << 16) : outp;
      const unsigned off = wid < 5 ? lane << 16 : min(lane, 27) << 16;
      asm volatile("global_load_dword %0, %1, %2" : "+v"(tdst) : "v"(off), "s"(b) : "memory");
    }
    if (idx == 28 && wid < 4) {
      unsigned long long pc; int d0, len;
      asm volatile("s_getpc_b64 %0\n.Lpf_here_%=:\n\ts_mov_b32 %1, _Z12k1_gemm_geluPKtS0_S0_S0_PcPKc-.Lpf_here_%=\n\t"
                   "s_mov_b32 %2, _Z6k3_outPcPt-_Z12k1_gemm_geluPKtS0_S0_S0_PcPKc" : "=s"(pc), "=s"(d0), "=s"(len));
      const unsigned off = min((unsigned)(wid * 8192 + lane * 128), (unsigned)(len + 2560));
      asm volatile("global_load_dword %0, %1, %2" : "+v"(tdst) : "v"(off), "s"(pc + (long long)d0) : "memory");
    }
#pragma unroll
    for (int i = 0; i < 4; ++i)
      asm volatile("global_load_dword %0, %1, %2" : "+v"(tdst) : "v"(lane * 128), "s"(pb + i * 8192) : "memory");
    STAMP();
    unsigned short v[4][8];
#pragma unroll
    for (int i = 0; i < 4; ++i) {
      const int b = min(hw + 256 * i, 783), kf = b / 28, dt = b % 28;
#pragma unroll
      for (int e = 0; e < 8; ++e) v[i][e] = wout[(size_t)(32 * kf + 8 * g + e) * D_ + 16 * dt + j];
    }
#pragma unroll
    for (int i = 0; i < 4; ++i) {
      const int b = min(hw + 256 * i, 783);
      v4u o;
#pragma unroll
      for (int q = 0; q < 4; ++q) o[q] = (unsigned)v[i][2 * q] | ((unsigned)v[i][2 * q + 1] << 16);
      *(v4u*)(ws + WS_WO + ((size_t)b * 64 + lane) * 16) = o;
    }
    asm volatile("s_waitcnt vmcnt(0)" : "+v"(tdst)::"memory");
    STAMP(); STAMP_OUT(3);
    return;
  }
  const int rp = (xcd >> 1) * 4 + (idx & 3);
  const int cp = (xcd & 1) * 7 + (idx >> 2);

  const unsigned sm0 = lds_addr(smem);
#define BAR_L() asm volatile("s_waitcnt lgkmcnt(0)\n\ts_barrier" ::: "memory")
#define BAR_N() asm volatile("s_barrier" ::: "memory")
#define WAITBAR_V(N) asm volatile("s_waitcnt vmcnt(" #N ")\n\ts_barrier" ::: "memory")
  if (wid >= 8) {
    const int w0 = (wid - 8) * 4;
    const char* xb0 = (const char*)(x + (size_t)(rp * 128 + w0 * 16) * D_);
    const unsigned xvo0 = (lane >> 3) * (D_ * 2) + (((lane & 7) ^ g) * 16);
    const unsigned xvo1 = (lane >> 3) * (D_ * 2) + (((lane & 7) ^ (g + 4)) * 16) + 8 * D_ * 2;
    const char* w1 = (const char*)(win + cp * 64 + (size_t)(w0 * 8) * F_);
    const char* w2 = w1 + FH_ * 2;
    const unsigned wvoa = (lane >> 3) * (F_ * 2) + (((lane & 7) ^ (2 * (g & 1))) * 16);
    const unsigned wvob = wvoa ^ 64;
    const unsigned xl = sm0 + w0 * 2048, wl = sm0 + K1_WB + w0 * 1024;
#define K1_WL(base, c, dst, dstr) { _Pragma("unroll") for (int q = 0; q < 4; ++q) \
      glds16((q & 1) ? wvob : wvoa, (base) + (size_t)((c) * 64 + q * 8) * (F_ * 2), (dst) + q * (dstr)); }
#define K1_C(c) { _Pragma("unroll") for (int q = 0; q < 4; ++q) { \
      glds16(xvo0, xb0 + q * 16 * D_ * 2 + (c) * 128, xl + (c) * K1_XS + q * 2048); \
      glds16(xvo1, xb0 + q * 16 * D_ * 2 + (c) * 128, xl + (c) * K1_XS + q * 2048 + 1024); } \
      K1_WL(w2, c, wl + (c) * 8192, 1024) }
    K1_C(0) K1_C(1) K1_C(2)
    WAITBAR_V(24);
    K1_C(3)
    WAITBAR_V(24);
    K1_C(4)
    WAITBAR_V(24);
    K1_C(5)
    WAITBAR_V(24);
    K1_WL(w2, 6, wl + 6 * 8192, 1024) K1_WL(w1, 0, wl + 7 * 8192, 1024) K1_WL(w1, 1, xl, 2048)
    WAITBAR_V(24);
    K1_WL(w1, 2, xl + 1024, 2048) K1_WL(w1, 3, xl + K1_XS, 2048) K1_WL(w1, 4, xl + K1_XS + 1024, 2048)
    WAITBAR_V(24);
    K1_WL(w1, 5, xl + 2 * K1_XS, 2048) K1_WL(w1, 6, xl + 2 * K1_XS + 1024, 2048)
    WAITBAR_V(28);
    WAITBAR_V(0);
    return;
  }
  STAMP_DECL STAMP();
  const char* ex = ws + WS_EX + (size_t)(rp * 8 + wid) * EX_GRP;
  const unsigned gr_off = (g * 8 + (j >> 1)) * 8;
  const unsigned in_vo = (j & 1) * 256 + gr_off;
  const unsigned pin_vo = EX_EC + cp * 128 + j * 8;
  const unsigned pout_vo = EX_EC + j * 128 + cp * 8;
  v2u in[7], pin;
#define K1_LDIN() { _Pragma("unroll") for (int k = 0; k < 7; ++k) ld8_sc1(in[k], in_vo, ex + k * 512); }
#define K1_INREGS "+v"(in[0]), "+v"(in[1]), "+v"(in[2]), "+v"(in[3]), "+v"(in[4]), "+v"(in[5]), "+v"(in[6])
  v4u xf[14];
  v2u scn;
  ld8_sc1(scn, lane < 32 ? cp * 256 + lane * 8 : pin_vo, ex);
  asm volatile("global_load_dwordx4 %0, %2, %3 offset:768\n\tglobal_load_dwordx4 %1, %2, %3 offset:832"
               : "=&v"(xf[12]), "=&v"(xf[13]) : "v"(j * (D_ * 2) + g * 16), "s"((const char*)(x + (size_t)(rp * 128 + wid * 16) * D_)) : "memory");
  STAMP();
  const int xrd0 = wid * 2048 + j * 128 + ((g ^ (j >> 1)) * 16), xrd1 = xrd0 ^ 64;
  const int fsw = 2 * (((j >> 3) & 1) + 2 * (g & 1));
  const int tra = g * 1024 + (j >> 2) * 128 + (((j >> 1) & 1) * 16) + 8 * (j & 1);
  const int trb = g * 2048 + (j >> 2) * 128 + (((j >> 1) & 1) * 16) + 8 * (j & 1);
  v4u wf[2][8];
  f32x4 acc[4];
#pragma unroll
  for (int n = 0; n < 4; ++n) acc[n] = (f32x4){0.f, 0.f, 0.f, 0.f};
#define K1_STEP(RDX, c, RDW, b, off, tb, kstr, MM, cm, ZT) { \
    if (RDX) { xf[2 * (c)] = *(const v4u*)(smem + (c) * K1_XS + xrd0); xf[2 * (c) + 1] = *(const v4u*)(smem + (c) * K1_XS + xrd1); } \
    _Pragma("unroll") for (int i = 0; i < 8; ++i) { \
      const int k2 = i >> 2, n = i & 3; \
      if (RDW) { const char* sb = smem + (off) + (tb) + (((2 * n) ^ fsw) * 16) + k2 * (kstr); \
        s16x4 t0 = __builtin_amdgcn_ds_read_tr16_b64_v4i16((lds_s16x4_p)(sb)); \
        s16x4 t1 = __builtin_amdgcn_ds_read_tr16_b64_v4i16((lds_s16x4_p)(sb + 512)); \
        v2u a_ = __builtin_bit_cast(v2u, t0), b_ = __builtin_bit_cast(v2u, t1); \
        wf[b][i] = (v4u){a_[0], a_[1], b_[0], b_[1]}; } \
      if (MM) acc[n] = ZT ? mfma16(xf[2 * (cm) + k2], wf[(b) ^ 1][i], acc[n]) : mfma16(wf[(b) ^ 1][i], xf[2 * (cm) + k2], acc[n]); \
      SCHED(); } }

  BAR_N();
  STAMP();
  K1_STEP(1, 0, 1, 0, K1_WB, tra, 4096, 0, 0, 1)
  asm volatile("s_waitcnt vmcnt(2)" : "+v"(scn)::"memory");
  unsigned nonce = 1;
  while (__any(scn[0] == nonce)) ++nonce;
  if (lane == 0) st4_sc1(nonce, EX_CH + cp * 4, ex);
  BAR_L();
  K1_STEP(1, 1, 1, 1, K1_WB + 8192, tra, 4096, 1, 0, 1)
  BAR_L();
  K1_STEP(1, 2, 1, 0, K1_WB + 2 * 8192, tra, 4096, 1, 1, 1)
  BAR_L();
  K1_STEP(1, 3, 1, 1, K1_WB + 3 * 8192, tra, 4096, 1, 2, 1)
  BAR_N();
  unsigned chal;
  ld4_sc1(chal, EX_CH + j * 4, ex);
  K1_STEP(1, 4, 1, 0, K1_WB + 4 * 8192, tra, 4096, 1, 3, 1)
  BAR_N();
  K1_STEP(1, 5, 1, 1, K1_WB + 5 * 8192, tra, 4096, 1, 4, 1)
  asm volatile("s_waitcnt vmcnt(2)\n\ts_barrier" : "+v"(xf[12]), "+v"(xf[13])::"memory");
  K1_STEP(0, 6, 1, 0, K1_WB + 6 * 8192, tra, 4096, 1, 5, 1)
  asm volatile("s_waitcnt vmcnt(0)" : "+v"(chal)::"memory");
  unsigned answered = chal;
  if (lane < 16) st8_sc1((v2u){answered, nonce}, pout_vo, ex);
  K1_STEP(0, 0, 0, 1, 0, trb, 8192, 1, 6, 1)
  STAMP();
  float v[4][4];
  unsigned myval;
  {
    float sh[4], S[4], Q[4];
#pragma unroll
    for (int n = 0; n < 4; ++n)
#pragma unroll
      for (int r = 0; r < 4; ++r) { v[n][r] = gelu_f(acc[n][r]); acc[n][r] = 0.f; }
#pragma unroll
    for (int r = 0; r < 4; ++r) {
      sh[r] = dpp_f<DPP_BC(0)>(v[0][r]);
      const float d0 = v[0][r] - sh[r], d1 = v[1][r] - sh[r], d2 = v[2][r] - sh[r], d3 = v[3][r] - sh[r];
      S[r] = (d0 + d1) + (d2 + d3);
      Q[r] = (d0 * d0 + d1 * d1) + (d2 * d2 + d3 * d3);
    }
    const bool b3 = j & 8, b2 = j & 4, b1 = j & 2;
    float T[4];
#pragma unroll
    for (int r = 0; r < 4; ++r) T[r] = (b3 ? Q[r] : S[r]) + dpp_f<DPP_XOR15>(b3 ? S[r] : Q[r]);
    const float U0 = (b2 ? T[2] : T[0]) + dpp_f<DPP_XOR7>(b2 ? T[0] : T[2]);
    const float U1 = (b2 ? T[3] : T[1]) + dpp_f<DPP_XOR7>(b2 ? T[1] : T[3]);
    float W = (b1 ? U1 : U0) + dpp_f<DPP_XOR3>(b1 ? U0 : U1);
    W += dpp_f<DPP_XOR1>(W);
    const float other = dpp_f<DPP_XOR8>(W);
    const float shr = b2 ? (b1 ? sh[3] : sh[2]) : (b1 ? sh[1] : sh[0]);
    myval = __builtin_bit_cast(unsigned, b3 ? W - other * other * (1.0f / 64.0f) : shr + W * (1.0f / 64.0f));
  }
  if (!(j & 1)) st8_sc1((v2u){nonce, myval}, cp * 256 + gr_off, ex);
  ld8_sc1(pin, pin_vo, ex);
  ld4_sc1(chal, EX_CH + j * 4, ex);
  STAMP();
  BAR_N();
  STAMP();
  K1_STEP(0, 0, 1, 0, K1_WB + 7 * 8192, tra, 4096, 0, 0, 0)
  K1_STEP(0, 0, 1, 1, 0, trb, 8192, 1, 0, 0)
  K1_STEP(0, 0, 1, 0, 1024, trb, 8192, 1, 1, 0)
  K1_STEP(0, 0, 1, 1, K1_XS, trb, 8192, 1, 2, 0)
  K1_STEP(0, 0, 1, 0, K1_XS + 1024, trb, 8192, 1, 3, 0)
  K1_STEP(0, 0, 1, 1, 2 * K1_XS, trb, 8192, 1, 4, 0)
  K1_STEP(0, 0, 1, 0, 2 * K1_XS + 1024, trb, 8192, 1, 5, 0)
  K1_STEP(0, 0, 0, 1, 0, trb, 8192, 1, 6, 0)
  STAMP();
  asm volatile("s_waitcnt vmcnt(0)" : "+v"(pin), "+v"(chal)::"memory");
  for (unsigned spin = 0; spin < (1u << 16); ++spin) {
    const bool pv = pin[0] == nonce, live = j < 14;
    const unsigned cur = pv ? pin[1] : chal;
    if (__any(live && cur != answered)) { answered = cur; if (lane < 16) st8_sc1((v2u){answered, nonce}, pout_vo, ex); }
    if (__all(pv || !live)) break;
    ld8_sc1(pin, pin_vo, ex);
    ld4_sc1(chal, EX_CH + j * 4, ex);
    asm volatile("s_waitcnt vmcnt(0)" : "+v"(pin), "+v"(chal)::"memory");
  }
  K1_LDIN()
  unsigned tg[7];
#pragma unroll
  for (int k = 0; k < 7; ++k) tg[k] = __shfl(answered, 2 * k + (j & 1));
  {
    char* z1 = ws + WS_Z1 + ((size_t)((8 * rp + wid) * 56 + cp * 4) * 64 + lane) * 8;
#pragma unroll
    for (int n = 0; n < 4; ++n) {
      v2u o;
      o[0] = pack_bf16(gelu_f(acc[n][0]), gelu_f(acc[n][1]));
      o[1] = pack_bf16(gelu_f(acc[n][2]), gelu_f(acc[n][3]));
      st_plain8(o, z1 + n * 512);
    }
  }
  STAMP();
  asm volatile("s_waitcnt vmcnt(4)" : K1_INREGS::"memory");
  for (unsigned spin = 0; spin < (1u << 16); ++spin) {
    bool okd = true;
#pragma unroll
    for (int k = 0; k < 7; ++k) okd = okd && in[k][0] == tg[k];
    if (__all(okd)) break;
    K1_LDIN()
    asm volatile("s_waitcnt vmcnt(0)" : K1_INREGS::"memory");
  }
  STAMP();
  {
    float sm = ((asf(in[0][1]) + asf(in[1][1])) + (asf(in[2][1]) + asf(in[3][1]))) + ((asf(in[4][1]) + asf(in[5][1])) + asf(in[6][1]));
    sm += dpp_f<DPP_XOR1>(sm);
    const float mean = sm * (1.0f / 14.0f);
    float bt = 0.f;
#pragma unroll
    for (int k = 0; k < 7; ++k) { const float e = asf(in[k][1]) - mean; bt = __builtin_fmaf(e, e, bt); }
    bt += dpp_f<DPP_XOR1>(bt);
    const float between = dpp_f<DPP_XOR8>(bt);
    const float rstd = rsqrtf((sm + 64.0f * between) * (1.0f / FH_) + 1e-5f);
    float mr[4], rr[4];
    mr[0] = dpp_f<DPP_BC(0)>(mean); mr[1] = dpp_f<DPP_BC(2)>(mean); mr[2] = dpp_f<DPP_BC(4)>(mean); mr[3] = dpp_f<DPP_BC(6)>(mean);
    rr[0] = dpp_f<DPP_BC(8)>(rstd); rr[1] = dpp_f<DPP_BC(10)>(rstd); rr[2] = dpp_f<DPP_BC(12)>(rstd); rr[3] = dpp_f<DPP_BC(14)>(rstd);
    char* z2 = ws + WS_Z2 + ((size_t)((4 * rp + (wid >> 1)) * 56 + cp * 4) * 64 + j + 16 * (2 * (wid & 1) + (g >> 1))) * 16 + (g & 1) * 8;
#pragma unroll
    for (int n = 0; n < 4; ++n) {
      v2u o;
      o[0] = pack_bf16((v[n][0] - mr[0]) * rr[0], (v[n][1] - mr[1]) * rr[1]);
      o[1] = pack_bf16((v[n][2] - mr[2]) * rr[2], (v[n][3] - mr[3]) * rr[3]);
      st_plain8(o, z2 + n * 1024);
    }
  }
  STAMP(); STAMP_OUT(1);
}

#define K2_LDS (4 * 32768 + 1024)

template <int OFF>
__device__ __forceinline__ void gload_a(v4u& dst, unsigned voff, const void* sbase) {
  asm volatile("global_load_dwordx4 %0, %1, %2 offset:%3" : "=a"(dst) : "v"(voff), "s"(sbase), "i"(OFF) : "memory");
}

__global__ __launch_bounds__(256, 1) void k2_spatial(const unsigned short* __restrict__ wsp, const unsigned short* __restrict__ bsp,
                                                    char* __restrict__ ws) {
  extern __shared__ __attribute__((aligned(16))) char smem[];
  const int lane = threadIdx.x & 63;
  const int wid = __builtin_amdgcn_readfirstlane(threadIdx.x >> 6);
  const int xcd = blockIdx.x & 7, idx = blockIdx.x >> 3;
  const int tp = (xcd >> 1) * 8 + (idx & 7);
  const int fp = (xcd & 1) * 4 + (idx >> 3);
  const int g = lane >> 4;

  STAMP_DECL STAMP();
  const char* abase = (const char*)(wsp + (size_t)(tp * 64) * S_ + wid * 64);
  const unsigned avo0 = (lane >> 3) * (S_ * 2) + (((lane & 7) ^ g) * 16);
  const unsigned avo1 = (lane >> 3) * (S_ * 2) + (((lane & 7) ^ (g + 4)) * 16);
  const unsigned ring = lds_addr(smem) + 1024 + wid * 32768;
  const char* stg = smem + 1024 + wid * 32768;
  const char* bbase = ws + WS_Z2 + ((size_t)(wid * 2) * 56 + fp * 7 + 3) * 1024;
  const unsigned bvo = lane * 16;
  const int rd0 = (lane & 15) * 128 + ((g ^ ((lane & 15) >> 1)) * 16);
  const int rd1 = rd0 ^ 64;

  v4u Bf[2][14];
  v4u Af[2][8];
  f32x4 acc[4][7];
#pragma unroll
  for (int m = 0; m < 4; ++m)
#pragma unroll
    for (int n = 0; n < 7; ++n) acc[m][n] = (f32x4){0.f, 0.f, 0.f, 0.f};

#define K2_ROT 0
  const int rot = K2_ROT;
#define RJ(j) (4 * (j))
#define LOAD_A1(j, i) glds16(((i) & 1) ? avo1 : avo0, abase + (size_t)(i) * 8 * (S_ * 2) + RJ(j) * 128, ring + ((j) & 3) * 8192 + (i) * 1024);
#define LOAD_A(j) LOAD_A1(j, 0) LOAD_A1(j, 1) LOAD_A1(j, 2) LOAD_A1(j, 3) LOAD_A1(j, 4) LOAD_A1(j, 5) LOAD_A1(j, 6) LOAD_A1(j, 7)
#define LOAD_B1(j, ks, n) gload_a<((n) - 3) * 1024>(Bf[(j) & 1][(ks) * 7 + (n)], bvo, bbase + (size_t)(2 * RJ(j) + (ks)) * 56 * 1024);
#define LOAD_Bk(j, ks) LOAD_B1(j, ks, 0) LOAD_B1(j, ks, 1) LOAD_B1(j, ks, 2) LOAD_B1(j, ks, 3) LOAD_B1(j, ks, 4) LOAD_B1(j, ks, 5) LOAD_B1(j, ks, 6)
#define LOAD_B(j) LOAD_Bk(j, 0) LOAD_Bk(j, 1)
#define READ_A(j) _Pragma("unroll") for (int m = 0; m < 4; ++m) _Pragma("unroll") for (int ks = 0; ks < 2; ++ks) \
    Af[(j) & 1][m * 2 + ks] = *(const v4u*)(stg + ((j) & 3) * 8192 + ((m + wid) & 3) * 2048 + (ks ? rd1 : rd0));
#define WAIT_B(N, P) asm volatile("s_waitcnt vmcnt(" #N ")" : "+a"(Bf[P][0]), "+a"(Bf[P][1]), "+a"(Bf[P][2]), "+a"(Bf[P][3]), "+a"(Bf[P][4]), \
    "+a"(Bf[P][5]), "+a"(Bf[P][6]), "+a"(Bf[P][7]), "+a"(Bf[P][8]), "+a"(Bf[P][9]), "+a"(Bf[P][10]), "+a"(Bf[P][11]), "+a"(Bf[P][12]), "+a"(Bf[P][13]) :: "memory");
#define MMA(j, ks, n) _Pragma("unroll") for (int m = 0; m < 4; ++m) acc[m][n] = mfma16(Bf[(j) & 1][(ks) * 7 + (n)], Af[(j) & 1][m * 2 + (ks)], acc[m][n]);
#define STEP_BODY(j, LB, LA) \
  MMA(j, 0, 0) SCHED(); if (LB) { LOAD_B1((j) + 2, 0, 0) } if (LA) { LOAD_A1((j) + 3, 0) } \
  MMA(j, 0, 1) SCHED(); if (LB) { LOAD_B1((j) + 2, 0, 1) } if (LA) { LOAD_A1((j) + 3, 1) } \
  MMA(j, 0, 2) SCHED(); if (LB) { LOAD_B1((j) + 2, 0, 2) } if (LA) { LOAD_A1((j) + 3, 2) } \
  MMA(j, 0, 3) SCHED(); if (LB) { LOAD_B1((j) + 2, 0, 3) } if (LA) { LOAD_A1((j) + 3, 3) } \
  MMA(j, 0, 4) SCHED(); if (LB) { LOAD_B1((j) + 2, 0, 4) } if (LA) { LOAD_A1((j) + 3, 4) } \
  MMA(j, 0, 5) SCHED(); if (LB) { LOAD_B1((j) + 2, 0, 5) } if (LA) { LOAD_A1((j) + 3, 5) } \
  MMA(j, 0, 6) SCHED(); if (LB) { LOAD_B1((j) + 2, 0, 6) } if (LA) { LOAD_A1((j) + 3, 6) } \
  MMA(j, 1, 0) SCHED(); if (LB) { LOAD_B1((j) + 2, 1, 0) } if (LA) { LOAD_A1((j) + 3, 7) } \
  MMA(j, 1, 1) SCHED(); if (LB) { LOAD_B1((j) + 2, 1, 1) } \
  MMA(j, 1, 2) SCHED(); if (LB) { LOAD_B1((j) + 2, 1, 2) } \
  MMA(j, 1, 3) SCHED(); if (LB) { LOAD_B1((j) + 2, 1, 3) } \
  MMA(j, 1, 4) SCHED(); if (LB) { LOAD_B1((j) + 2, 1, 4) } \
  MMA(j, 1, 5) SCHED(); if (LB) { LOAD_B1((j) + 2, 1, 5) } \
  MMA(j, 1, 6) SCHED(); if (LB) { LOAD_B1((j) + 2, 1, 6) } \
  SCHED();

  LOAD_A(0) LOAD_B(0) LOAD_A(1)
  LOAD_B(1) LOAD_A(2)
  STAMP();
  asm volatile("s_waitcnt vmcnt(44)" ::: "memory");
  STAMP();
  READ_A(0)
  SCHED();
  WAIT_B(22, 0) STAMP(); READ_A(1) SCHED(); STEP_BODY(0, 1, 1)
  WAIT_B(22, 1) STAMP(); READ_A(2) SCHED(); STEP_BODY(1, 1, 1)
  WAIT_B(22, 0) STAMP(); READ_A(3) SCHED(); STEP_BODY(2, 1, 1)
  WAIT_B(22, 1) STAMP(); READ_A(4) SCHED(); STEP_BODY(3, 1, 1)
  WAIT_B(22, 0) STAMP(); READ_A(5) SCHED(); STEP_BODY(4, 1, 1)
  WAIT_B(22, 1) STAMP(); READ_A(6) SCHED(); STEP_BODY(5, 1, 0)
  WAIT_B(14, 0) STAMP(); READ_A(7) SCHED();
  v2u z1v[7];
  {
    const v2u* z1 = (const v2u*)(ws + WS_Z1) + ((size_t)((4 * tp + wid) * 56 + fp * 7)) * 64 + lane;
#pragma unroll
    for (int n = 0; n < 7; ++n) z1v[n] = z1[n * 64];
  }
  const float bias = __builtin_bit_cast(float, (unsigned)bsp[tp * 64 + wid * 16 + (lane & 15)] << 16);

 STEP_BODY(6, 0, 0)
  WAIT_B(8, 1) STAMP(); SCHED();
  {
    char* mine = smem + 1024 + wid * 32768 + lane * 16;
#pragma unroll
    for (int mi = 1; mi <= 4; ++mi) {
      const int m = mi & 3;
#pragma unroll
      for (int ks = 0; ks < 2; ++ks)
#pragma unroll
        for (int n = 0; n < 7; ++n) acc[m][n] = mfma16(Bf[1][ks * 7 + n], Af[1][m * 2 + ks], acc[m][n]);
      SCHED();
      if (mi >= 2) {
#pragma unroll
        for (int n = 0; n < 7; ++n) *(f32x4*)(mine + ((mi - 1) * 7 + n) * 1024) = acc[mi - 1][n];
      }
      SCHED();
    }
  }
  STAMP();
  __syncthreads();
  STAMP();
  f32x4 o[7];
  {
    f32x4 pr[3][7];
#pragma unroll
    for (int d = 1; d < 4; ++d) {
      const char* src = smem + 1024 + ((wid - d) & 3) * 32768 + d * 7168 + lane * 16;
#pragma unroll
      for (int n = 0; n < 7; ++n) pr[d - 1][n] = *(const f32x4*)(src + n * 1024);
    }
    SCHED();
#pragma unroll
    for (int n = 0; n < 7; ++n) o[n] = (acc[0][n] + pr[0][n]) + (pr[1][n] + pr[2][n]);
  }
  char* hp = ws + WS_H + ((size_t)(4 * tp + wid) * 28 * 64 + (lane & 15) + 16 * (g >> 1)) * 16 + (g & 1) * 8;
#pragma unroll
  for (int n = 0; n < 7; ++n) {
    const int ft = fp * 7 + n;
    v2u h;
    h[0] = pack_bf16(bf_lo(z1v[n][0]) * (o[n][0] + bias), bf_hi(z1v[n][0]) * (o[n][1] + bias));
    h[1] = pack_bf16(bf_lo(z1v[n][1]) * (o[n][2] + bias), bf_hi(z1v[n][1]) * (o[n][3] + bias));
    st_plain8(h, hp + (ft >> 1) * 1024 + (ft & 1) * 512);
  }
  STAMP(); STAMP_OUT(2);
}

#define K3_LDS (4 * 12 * 1024)
__global__ __launch_bounds__(256, 1) void k3_out(char* __restrict__ ws, unsigned short* __restrict__ out) {
  extern __shared__ __attribute__((aligned(16))) char smem[];
  const int lane = threadIdx.x & 63;
  const int wid = __builtin_amdgcn_readfirstlane(threadIdx.x >> 6);
  const int xcd = blockIdx.x & 7, idx = blockIdx.x >> 3;
  const int tp = xcd * 4 + (idx & 3);
  const int dp = idx >> 2;
  const int g = lane >> 4;
  STAMP_DECL STAMP();
  const v4u* hp = (const v4u*)(ws + WS_H) + ((size_t)(4 * tp) * 28 + wid * 7) * 64 + lane;
  const v4u* wp = (const v4u*)(ws + WS_WO) + ((size_t)(wid * 7) * 28 + dp * 4) * 64 + lane;
  v4u Hf[7][4], Wf[7][4];
#define K3_LD(k) { _Pragma("unroll") for (int n = 0; n < 4; ++n) Wf[k][n] = wp[((k) * 28 + n) * 64]; \
    _Pragma("unroll") for (int m = 0; m < 4; ++m) Hf[k][m] = hp[(((m + wid) & 3) * 28 + (k)) * 64]; SCHED(); }
#define K3_MM(k) { _Pragma("unroll") for (int m = 0; m < 4; ++m) _Pragma("unroll") for (int n = 0; n < 4; ++n) \
    acc[m][n] = mfma16(Wf[k][n], Hf[k][m], acc[m][n]); SCHED(); }
  f32x4 acc[4][4];
#pragma unroll
  for (int m = 0; m < 4; ++m)
#pragma unroll
    for (int n = 0; n < 4; ++n) acc[m][n] = (f32x4){0.f, 0.f, 0.f, 0.f};
  K3_LD(0) K3_LD(1) K3_LD(2) K3_LD(3)
  STAMP();
  K3_MM(0) K3_LD(4) K3_MM(1) K3_LD(5) K3_MM(2) K3_LD(6) K3_MM(3) K3_MM(4) K3_MM(5) K3_MM(6)
  STAMP();
#pragma unroll
  for (int m = 1; m < 4; ++m)
#pragma unroll
    for (int n = 0; n < 4; ++n) *(f32x4*)(smem + ((wid * 3 + m - 1) * 4 + n) * 1024 + lane * 16) = acc[m][n];
  __syncthreads();
  STAMP();
  f32x4 pr[3][4];
#pragma unroll
  for (int d = 1; d < 4; ++d)
#pragma unroll
    for (int n = 0; n < 4; ++n) pr[d - 1][n] = *(const f32x4*)(smem + ((((wid - d) & 3) * 3 + d - 1) * 4 + n) * 1024 + lane * 16);
  unsigned short* op = out + (size_t)(tp * 64 + wid * 16 + (lane & 15)) * D_ + dp * 64 + 4 * g;
#pragma unroll
  for (int n = 0; n < 4; ++n) {
    const f32x4 o = (acc[0][n] + pr[0][n]) + (pr[1][n] + pr[2][n]);
    v2u h;
    h[0] = pack_bf16(o[0], o[1]);
    h[1] = pack_bf16(o[2], o[3]);
    st_plain8(h, op + n * 16);
  }
  STAMP(); STAMP_OUT(0);
}

extern "C" void kernel_launch(void* const* d_in, const int* in_sizes, int n_in,
                              void* d_out, int out_size, void* d_ws, size_t ws_size,
                              hipStream_t stream) {
  const unsigned short* x = (const unsigned short*)d_in[0];
  const unsigned short* win = (const unsigned short*)d_in[1];
  const unsigned short* wsp = (const unsigned short*)d_in[2];
  const unsigned short* bsp = (const unsigned short*)d_in[3];
  const unsigned short* wout = (const unsigned short*)d_in[4];
  char* ws = (char*)d_ws;
  (void)hipFuncSetAttribute((const void*)k1_gemm_gelu, hipFuncAttributeMaxDynamicSharedMemorySize, K1_LDS);
  (void)hipFuncSetAttribute((const void*)k2_spatial, hipFuncAttributeMaxDynamicSharedMemorySize, K2_LDS);
  (void)hipFuncSetAttribute((const void*)k3_out, hipFuncAttributeMaxDynamicSharedMemorySize, K3_LDS);
  k1_gemm_gelu<<<256, 640, K1_LDS, stream>>>(x, win, wsp, wout, ws, (const char*)d_out);
  k2_spatial<<<256, 256, K2_LDS, stream>>>(wsp, bsp, ws);
  k3_out<<<224, 256, K3_LDS, stream>>>(ws, (unsigned short*)d_out);
}
```

```cpp
#include <hip/hip_runtime.h>
#include <hip/hip_bf16.h>
#include <stdint.h>

typedef __attribute__((ext_vector_type(8))) short bf16x8;
typedef __attribute__((ext_vector_type(4))) short s16x4;
typedef __attribute__((ext_vector_type(4))) float f32x4;
typedef __attribute__((ext_vector_type(4))) unsigned v4u;
typedef __attribute__((ext_vector_type(2))) unsigned v2u;
typedef __attribute__((address_space(3))) s16x4* lds_s16x4_p;

#define S_ 2048
#define D_ 448
#define F_ 1792
#define FH_ 896

#define WS_Z1 0
#define WS_Z2 (WS_Z1 + 3670016)
#define WS_H  (WS_Z2 + 3670016)
#define WS_WO (WS_H + 3670016)

#define SCHED() __builtin_amdgcn_sched_barrier(0)
#define STAMP_DECL
#define STAMP()
#define STAMP_OUT(kid)

__device__ __forceinline__ f32x4 mfma16(v4u a, v4u b, f32x4 c) {
  return __builtin_amdgcn_mfma_f32_16x16x32_bf16(__builtin_bit_cast(bf16x8, a), __builtin_bit_cast(bf16x8, b), c, 0, 0, 0);
}
typedef __attribute__((ext_vector_type(2))) float f32x2;
typedef __attribute__((ext_vector_type(2))) __bf16 bf16x2;
__device__ __forceinline__ unsigned pack_bf16(float lo, float hi) {
  const bf16x2 r = __builtin_convertvector((f32x2){lo, hi}, bf16x2);
  return __builtin_bit_cast(unsigned, r);
}
__device__ __forceinline__ float asf(unsigned u) { return __builtin_bit_cast(float, u); }
__device__ __forceinline__ float bf_lo(unsigned u) { return __builtin_bit_cast(float, u << 16); }
__device__ __forceinline__ float bf_hi(unsigned u) { return __builtin_bit_cast(float, u & 0xffff0000u); }

__device__ __forceinline__ void glds16(unsigned voff, const void* sbase, unsigned lds) {
  asm volatile("s_mov_b32 m0, %2\n\ts_nop 0\n\tglobal_load_lds_dwordx4 %0, %1" ::"v"(voff), "s"(sbase), "s"(lds) : "memory");
}
template <int OFF>
__device__ __forceinline__ void glds16o(unsigned voff, const void* sbase, unsigned lds) {
  asm volatile("s_mov_b32 m0, %2\n\ts_nop 0\n\tglobal_load_lds_dwordx4 %0, %1 offset:%3" ::"v"(voff), "s"(sbase), "s"(lds - OFF), "i"(OFF) : "memory");
}
__device__ __forceinline__ unsigned lds_addr(const void* p) { return (unsigned)(size_t)(__attribute__((address_space(3))) const char*)p; }

__device__ __forceinline__ void touch(unsigned& dst, const void* p) { asm volatile("global_load_dword %0, %1, off" : "+v"(dst) : "v"(p) : "memory"); }
__device__ __forceinline__ void st_wt16(v4u d, void* p) { asm volatile("global_store_dwordx4 %0, %1, off sc1\n\ts_nop 1" ::"v"(p), "v"(d) : "memory"); }
__device__ __forceinline__ v4u pair16(v2u a, v2u b) {
  const auto r0 = __builtin_amdgcn_permlane16_swap(a[0], b[0], false, false);
  const auto r1 = __builtin_amdgcn_permlane16_swap(a[1], b[1], false, false);
  return (v4u){r0[0], r1[0], r0[1], r1[1]};
}
__device__ __forceinline__ void st_plain8(v2u d, void* p) { asm volatile("global_store_dwordx2 %0, %1, off sc1" ::"v"(p), "v"(d) : "memory"); }

__device__ __forceinline__ float gelu_f(float x) {
  const float c0 = -2.0f * 0.7978845608028654f * 1.4426950408889634f;
  const float c1 = c0 * 0.044715f;
  float t = __builtin_fmaf(x * x, c1, c0);
  float e = __builtin_amdgcn_exp2f(x * t);
  return x * __builtin_amdgcn_rcpf(1.0f + e);
}

#define K1_XS 16384
#define K1_WB (6 * K1_XS)
#define K1_LDS (K1_WB + 8 * 8192)
#define WAITBAR(N) asm volatile("s_waitcnt vmcnt(" #N ") lgkmcnt(0)\n\ts_barrier" ::: "memory")
#define WS_EX (WS_WO + 802816)
#define EX_EC 4096
#define EX_CH 6144
#define EX_GRP 6400

__device__ __forceinline__ void ld8_sc1(v2u& d, unsigned voff, const void* sb) { asm volatile("global_load_dwordx2 %0, %1, %2 sc1" : "=v"(d) : "v"(voff), "s"(sb) : "memory"); }
__device__ __forceinline__ void ld4_sc1(unsigned& d, unsigned voff, const void* sb) { asm volatile("global_load_dword %0, %1, %2 sc1" : "=v"(d) : "v"(voff), "s"(sb) : "memory"); }
__device__ __forceinline__ void st8_sc1(v2u d, unsigned voff, const void* sb) { asm volatile("global_store_dwordx2 %0, %1, %2 sc1" ::"v"(voff), "v"(d), "s"(sb) : "memory"); }
__device__ __forceinline__ void st4_sc1(unsigned d, unsigned voff, const void* sb) { asm volatile("global_store_dword %0, %1, %2 sc1" ::"v"(voff), "v"(d), "s"(sb) : "memory"); }
template <int CTRL>
__device__ __forceinline__ float dpp_f(float v) {
  return __builtin_bit_cast(float, __builtin_amdgcn_update_dpp(0, __builtin_bit_cast(int, v), CTRL, 0xf, 0xf, true));
}
#define DPP_XOR1 0xB1
#define DPP_XOR3 0x1B
#define DPP_XOR7 0x141
#define DPP_XOR15 0x140
#define DPP_XOR8 0x128
#define DPP_BC(n) (0x150 + (n))

__global__ __launch_bounds__(640, 3) void k1_gemm_gelu(const unsigned short* __restrict__ x, const unsigned short* __restrict__ win,
                                                      const unsigned short* __restrict__ wsp, const unsigned short* __restrict__ wout,
                                                      char* __restrict__ ws, const char* __restrict__ outp) {
  extern __shared__ __attribute__((aligned(16))) char smem[];
  const int lane = threadIdx.x & 63;
  const int wid = __builtin_amdgcn_readfirstlane(threadIdx.x >> 6);
  const int xcd = blockIdx.x & 7, idx = blockIdx.x >> 3;
  const int g = lane >> 4, j = lane & 15;
  if (idx >= 28) {
    if (wid >= 8) return;
    STAMP_DECL STAMP();
    const int hw = ((idx - 28) * 8 + xcd) * 8 + wid;
    const char* pb = (const char*)wsp + ((size_t)((xcd >> 1) * 512 + ((xcd & 1) * 4 + idx - 28) * 64 + wid * 8) * S_) * 2;
    unsigned tdst = 0;
    if (idx == 29) {
      const char* b = wid < 3 ? ws + wid * (64u << 16) : wid < 5 ? (const char*)wsp + (wid - 3) * (64u << 16) : outp;
      const unsigned off = wid < 5 ? lane << 16 : min(lane, 27) << 16;
      asm volatile("global_load_dword %0, %1, %2" : "+v"(tdst) : "v"(off), "s"(b) : "memory");
    }
    if (idx == 28 && wid < 4) {
      unsigned long long pc; int d0, len;
      asm volatile("s_getpc_b64 %0\n.Lpf_here_%=:\n\ts_mov_b32 %1, _Z12k1_gemm_geluPKtS0_S0_S0_PcPKc-.Lpf_here_%=\n\t"
                   "s_mov_b32 %2, _Z6k3_outPcPt-_Z12k1_gemm_geluPKtS0_S0_S0_PcPKc" : "=s"(pc), "=s"(d0), "=s"(len));
      const unsigned off = min((unsigned)(wid * 8192 + lane * 128), (unsigned)(len + 2560));
      asm volatile("global_load_dword %0, %1, %2" : "+v"(tdst) : "v"(off), "s"(pc + (long long)d0) : "memory");
    }
#pragma unroll
    for (int i = 0; i < 4; ++i)
      asm volatile("global_load_dword %0, %1, %2" : "+v"(tdst) : "v"(lane * 128), "s"(pb + i * 8192) : "memory");
    STAMP();
    unsigned short v[4][8];
#pragma unroll
    for (int i = 0; i < 4; ++i) {
      const int b = min(hw + 256 * i, 783), kf = b / 28, dt = b % 28;
#pragma unroll
      for (int e = 0; e < 8; ++e) v[i][e] = wout[(size_t)(32 * kf + 8 * g + e) * D_ + 16 * dt + j];
    }
#pragma unroll
    for (int i = 0; i < 4; ++i) {
      const int b = min(hw + 256 * i, 783);
      v4u o;
#pragma unroll
      for (int q = 0; q < 4; ++q) o[q] = (unsigned)v[i][2 * q] | ((unsigned)v[i][2 * q + 1] << 16);
      *(v4u*)(ws + WS_WO + ((size_t)b * 64 + lane) * 16) = o;
    }
    asm volatile("s_waitcnt vmcnt(0)" : "+v"(tdst)::"memory");
    STAMP(); STAMP_OUT(3);
    return;
  }
  const int rp = (xcd >> 1) * 4 + (idx & 3);
  const int cp = (xcd & 1) * 7 + (idx >> 2);

  const unsigned sm0 = lds_addr(smem);
#define BAR_L() asm volatile("s_waitcnt lgkmcnt(0)\n\ts_barrier" ::: "memory")
#define BAR_N() asm volatile("s_barrier" ::: "memory")
#define WAITBAR_V(N) asm volatile("s_waitcnt vmcnt(" #N ")\n\ts_barrier" ::: "memory")
  if (wid >= 8) {
    const int w0 = (wid - 8) * 4;
    const char* xb0 = (const char*)(x + (size_t)(rp * 128 + w0 * 16) * D_);
    const unsigned xvo0 = (lane >> 3) * (D_ * 2) + (((lane & 7) ^ g) * 16);
    const unsigned xvo1 = (lane >> 3) * (D_ * 2) + (((lane & 7) ^ (g + 4)) * 16) + 8 * D_ * 2;
    const char* w1 = (const char*)(win + cp * 64 + (size_t)(w0 * 8) * F_);
    const char* w2 = w1 + FH_ * 2;
    const unsigned wvoa = (lane >> 3) * (F_ * 2) + (((lane & 7) ^ (2 * (g & 1))) * 16);
    const unsigned wvob = wvoa ^ 64;
    const unsigned xl = sm0 + w0 * 2048, wl = sm0 + K1_WB + w0 * 1024;
#define K1_WL(base, c, dst, dstr) { _Pragma("unroll") for (int q = 0; q < 4; ++q) \
      glds16((q & 1) ? wvob : wvoa, (base) + (size_t)((c) * 64 + q * 8) * (F_ * 2), (dst) + q * (dstr)); }
#define K1_C(c) { _Pragma("unroll") for (int q = 0; q < 4; ++q) { \
      glds16(xvo0, xb0 + q * 16 * D_ * 2 + (c) * 128, xl + (c) * K1_XS + q * 2048); \
      glds16(xvo1, xb0 + q * 16 * D_ * 2 + (c) * 128, xl + (c) * K1_XS + q * 2048 + 1024); } \
      K1_WL(w2, c, wl + (c) * 8192, 1024) }
    K1_C(0) K1_C(1) K1_C(2)
    WAITBAR_V(24);
    K1_C(3)
    WAITBAR_V(24);
    K1_C(4)
    WAITBAR_V(24);
    K1_C(5)
    WAITBAR_V(24);
    K1_WL(w2, 6, wl + 6 * 8192, 1024) K1_WL(w1, 0, wl + 7 * 8192, 1024) K1_WL(w1, 1, xl, 2048)
    WAITBAR_V(24);
    K1_WL(w1, 2, xl + 1024, 2048) K1_WL(w1, 3, xl + K1_XS, 2048) K1_WL(w1, 4, xl + K1_XS + 1024, 2048)
    WAITBAR_V(24);
    K1_WL(w1, 5, xl + 2 * K1_XS, 2048) K1_WL(w1, 6, xl + 2 * K1_XS + 1024, 2048)
    WAITBAR_V(28);
    WAITBAR_V(0);
    return;
  }
  STAMP_DECL STAMP();
  const char* ex = ws + WS_EX + (size_t)(rp * 8 + wid) * EX_GRP;
  const unsigned gr_off = (g * 8 + (j >> 1)) * 8;
  const unsigned in_vo = (j & 1) * 256 + gr_off;
  const unsigned pin_vo = EX_EC + cp * 128 + j * 8;
  const unsigned pout_vo = EX_EC + j * 128 + cp * 8;
  v2u in[7], pin;
#define K1_LDIN() { _Pragma("unroll") for (int k = 0; k < 7; ++k) ld8_sc1(in[k], in_vo, ex + k * 512); }
#define K1_INREGS "+v"(in[0]), "+v"(in[1]), "+v"(in[2]), "+v"(in[3]), "+v"(in[4]), "+v"(in[5]), "+v"(in[6])
  v4u xf[14];
  v2u scn;
  ld8_sc1(scn, lane < 32 ? cp * 256 + lane * 8 : pin_vo, ex);
  asm volatile("global_load_dwordx4 %0, %2, %3 offset:768\n\tglobal_load_dwordx4 %1, %2, %3 offset:832"
               : "=&v"(xf[12]), "=&v"(xf[13]) : "v"(j * (D_ * 2) + g * 16), "s"((const char*)(x + (size_t)(rp * 128 + wid * 16) * D_)) : "memory");
  STAMP();
  const int xrd0 = wid * 2048 + j * 128 + ((g ^ (j >> 1)) * 16), xrd1 = xrd0 ^ 64;
  const int fsw = 2 * (((j >> 3) & 1) + 2 * (g & 1));
  const int tra = g * 1024 + (j >> 2) * 128 + (((j >> 1) & 1) * 16) + 8 * (j & 1);
  const int trb = g * 2048 + (j >> 2) * 128 + (((j >> 1) & 1) * 16) + 8 * (j & 1);
  v4u wf[2][8];
  f32x4 acc[4];
#pragma unroll
  for (int n = 0; n < 4; ++n) acc[n] = (f32x4){0.f, 0.f, 0.f, 0.f};
#define K1_STEP(RDX, c, RDW, b, off, tb, kstr, MM, cm, ZT) { \
    if (RDX) { xf[2 * (c)] = *(const v4u*)(smem + (c) * K1_XS + xrd0); xf[2 * (c) + 1] = *(const v4u*)(smem + (c) * K1_XS + xrd1); } \
    _Pragma("unroll") for (int i = 0; i < 8; ++i) { \
      const int k2 = i >> 2, n = i & 3; \
      if (RDW) { const char* sb = smem + (off) + (tb) + (((2 * n) ^ fsw) * 16) + k2 * (kstr); \
        s16x4 t0 = __builtin_amdgcn_ds_read_tr16_b64_v4i16((lds_s16x4_p)(sb)); \
        s16x4 t1 = __builtin_amdgcn_ds_read_tr16_b64_v4i16((lds_s16x4_p)(sb + 512)); \
        v2u a_ = __builtin_bit_cast(v2u, t0), b_ = __builtin_bit_cast(v2u, t1); \
        wf[b][i] = (v4u){a_[0], a_[1], b_[0], b_[1]}; } \
      if (MM) acc[n] = ZT ? mfma16(xf[2 * (cm) + k2], wf[(b) ^ 1][i], acc[n]) : mfma16(wf[(b) ^ 1][i], xf[2 * (cm) + k2], acc[n]); \
      SCHED(); } }

  BAR_N();
  STAMP();
  K1_STEP(1, 0, 1, 0, K1_WB, tra, 4096, 0, 0, 1)
  asm volatile("s_waitcnt vmcnt(2)" : "+v"(scn)::"memory");
  unsigned nonce = 1;
  while (__any(scn[0] == nonce)) ++nonce;
  if (lane == 0) st4_sc1(nonce, EX_CH + cp * 4, ex);
  BAR_L();
  K1_STEP(1, 1, 1, 1, K1_WB + 8192, tra, 4096, 1, 0, 1)
  BAR_L();
  K1_STEP(1, 2, 1, 0, K1_WB + 2 * 8192, tra, 4096, 1, 1, 1)
  BAR_L();
  K1_STEP(1, 3, 1, 1, K1_WB + 3 * 8192, tra, 4096, 1, 2, 1)
  BAR_N();
  unsigned chal;
  ld4_sc1(chal, EX_CH + j * 4, ex);
  K1_STEP(1, 4, 1, 0, K1_WB + 4 * 8192, tra, 4096, 1, 3, 1)
  BAR_N();
  K1_STEP(1, 5, 1, 1, K1_WB + 5 * 8192, tra, 4096, 1, 4, 1)
  asm volatile("s_waitcnt vmcnt(2)\n\ts_barrier" : "+v"(xf[12]), "+v"(xf[13])::"memory");
  K1_STEP(0, 6, 1, 0, K1_WB + 6 * 8192, tra, 4096, 1, 5, 1)
  asm volatile("s_waitcnt vmcnt(0)" : "+v"(chal)::"memory");
  unsigned answered = chal;
  if (lane < 16) st8_sc1((v2u){answered, nonce}, pout_vo, ex);
  K1_STEP(0, 0, 0, 1, 0, trb, 8192, 1, 6, 1)
  STAMP();
  float v[4][4];
  unsigned myval;
  {
    float sh[4], S[4], Q[4];
#pragma unroll
    for (int n = 0; n < 4; ++n)
#pragma unroll
      for (int r = 0; r < 4; ++r) { v[n][r] = gelu_f(acc[n][r]); acc[n][r] = 0.f; }
#pragma unroll
    for (int r = 0; r < 4; ++r) {
      sh[r] = dpp_f<DPP_BC(0)>(v[0][r]);
      const float d0 = v[0][r] - sh[r], d1 = v[1][r] - sh[r], d2 = v[2][r] - sh[r], d3 = v[3][r] - sh[r];
      S[r] = (d0 + d1) + (d2 + d3);
      Q[r] = (d0 * d0 + d1 * d1) + (d2 * d2 + d3 * d3);
    }
    const bool b3 = j & 8, b2 = j & 4, b1 = j & 2;
    float T[4];
#pragma unroll
    for (int r = 0; r < 4; ++r) T[r] = (b3 ? Q[r] : S[r]) + dpp_f<DPP_XOR15>(b3 ? S[r] : Q[r]);
    const float U0 = (b2 ? T[2] : T[0]) + dpp_f<DPP_XOR7>(b2 ? T[0] : T[2]);
    const float U1 = (b2 ? T[3] : T[1]) + dpp_f<DPP_XOR7>(b2 ? T[1] : T[3]);
    float W = (b1 ? U1 : U0) + dpp_f<DPP_XOR3>(b1 ? U0 : U1);
    W += dpp_f<DPP_XOR1>(W);
    const float other = dpp_f<DPP_XOR8>(W);
    const float shr = b2 ? (b1 ? sh[3] : sh[2]) : (b1 ? sh[1] : sh[0]);
    myval = __builtin_bit_cast(unsigned, b3 ? W - other * other * (1.0f / 64.0f) : shr + W * (1.0f / 64.0f));
  }
  if (!(j & 1)) st8_sc1((v2u){nonce, myval}, cp * 256 + gr_off, ex);
  ld8_sc1(pin, pin_vo, ex);
  ld4_sc1(chal, EX_CH + j * 4, ex);
  STAMP();
  BAR_N();
  STAMP();
  K1_STEP(0, 0, 1, 0, K1_WB + 7 * 8192, tra, 4096, 0, 0, 0)
  K1_STEP(0, 0, 1, 1, 0, trb, 8192, 1, 0, 0)
  K1_STEP(0, 0, 1, 0, 1024, trb, 8192, 1, 1, 0)
  K1_STEP(0, 0, 1, 1, K1_XS, trb, 8192, 1, 2, 0)
  K1_STEP(0, 0, 1, 0, K1_XS + 1024, trb, 8192, 1, 3, 0)
  K1_STEP(0, 0, 1, 1, 2 * K1_XS, trb, 8192, 1, 4, 0)
  K1_STEP(0, 0, 1, 0, 2 * K1_XS + 1024, trb, 8192, 1, 5, 0)
  K1_STEP(0, 0, 0, 1, 0, trb, 8192, 1, 6, 0)
  STAMP();
  asm volatile("s_waitcnt vmcnt(0)" : "+v"(pin), "+v"(chal)::"memory");
  for (unsigned spin = 0; spin < (1u << 16); ++spin) {
    const bool pv = pin[0] == nonce, live = j < 14;
    const unsigned cur = pv ? pin[1] : chal;
    if (__any(live && cur != answered)) { answered = cur; if (lane < 16) st8_sc1((v2u){answered, nonce}, pout_vo, ex); }
    if (__all(pv || !live)) break;
    ld8_sc1(pin, pin_vo, ex);
    ld4_sc1(chal, EX_CH + j * 4, ex);
    asm volatile("s_waitcnt vmcnt(0)" : "+v"(pin), "+v"(chal)::"memory");
  }
  K1_LDIN()
  unsigned tg[7];
#pragma unroll
  for (int k = 0; k < 7; ++k) tg[k] = __shfl(answered, 2 * k + (j & 1));
  {
    char* z1 = ws + WS_Z1 + ((size_t)((8 * rp + wid) * 28 + cp * 2) * 64 + lane) * 16;
#pragma unroll
    for (int n = 0; n < 4; n += 2) {
      v4u o;
      o[0] = pack_bf16(gelu_f(acc[n][0]), gelu_f(acc[n][1]));
      o[1] = pack_bf16(gelu_f(acc[n][2]), gelu_f(acc[n][3]));
      o[2] = pack_bf16(gelu_f(acc[n + 1][0]), gelu_f(acc[n + 1][1]));
      o[3] = pack_bf16(gelu_f(acc[n + 1][2]), gelu_f(acc[n + 1][3]));
      st_wt16(o, z1 + n * 512);
    }
  }
  STAMP();
  asm volatile("s_waitcnt vmcnt(2)" : K1_INREGS::"memory");
  for (unsigned spin = 0; spin < (1u << 16); ++spin) {
    bool okd = true;
#pragma unroll
    for (int k = 0; k < 7; ++k) okd = okd && in[k][0] == tg[k];
    if (__all(okd)) break;
    K1_LDIN()
    asm volatile("s_waitcnt vmcnt(0)" : K1_INREGS::"memory");
  }
  STAMP();
  {
    float sm = ((asf(in[0][1]) + asf(in[1][1])) + (asf(in[2][1]) + asf(in[3][1]))) + ((asf(in[4][1]) + asf(in[5][1])) + asf(in[6][1]));
    sm += dpp_f<DPP_XOR1>(sm);
    const float mean = sm * (1.0f / 14.0f);
    float bt = 0.f;
#pragma unroll
    for (int k = 0; k < 7; ++k) { const float e = asf(in[k][1]) - mean; bt = __builtin_fmaf(e, e, bt); }
    bt += dpp_f<DPP_XOR1>(bt);
    const float between = dpp_f<DPP_XOR8>(bt);
    const float rstd = rsqrtf((sm + 64.0f * between) * (1.0f / FH_) + 1e-5f);
    float mr[4], rr[4];
    mr[0] = dpp_f<DPP_BC(0)>(mean); mr[1] = dpp_f<DPP_BC(2)>(mean); mr[2] = dpp_f<DPP_BC(4)>(mean); mr[3] = dpp_f<DPP_BC(6)>(mean);
    rr[0] = dpp_f<DPP_BC(8)>(rstd); rr[1] = dpp_f<DPP_BC(10)>(rstd); rr[2] = dpp_f<DPP_BC(12)>(rstd); rr[3] = dpp_f<DPP_BC(14)>(rstd);
    char* z2 = ws + WS_Z2 + ((size_t)((4 * rp + (wid >> 1)) * 56 + cp * 4 + (g & 1)) * 64 + j + 16 * (2 * (wid & 1) + (g >> 1))) * 16;
    v2u o[4];
#pragma unroll
    for (int n = 0; n < 4; ++n) {
      o[n][0] = pack_bf16((v[n][0] - mr[0]) * rr[0], (v[n][1] - mr[1]) * rr[1]);
      o[n][1] = pack_bf16((v[n][2] - mr[2]) * rr[2], (v[n][3] - mr[3]) * rr[3]);
    }
    st_wt16(pair16(o[0], o[1]), z2);
    st_wt16(pair16(o[2], o[3]), z2 + 2048);
  }
  STAMP(); STAMP_OUT(1);
}

#define K2_LDS (4 * 32768 + 1024)

template <int OFF>
__device__ __forceinline__ void gload_a(v4u& dst, unsigned voff, const void* sbase) {
  asm volatile("global_load_dwordx4 %0, %1, %2 offset:%3" : "=a"(dst) : "v"(voff), "s"(sbase), "i"(OFF) : "memory");
}

__global__ __launch_bounds__(256, 1) void k2_spatial(const unsigned short* __restrict__ wsp, const unsigned short* __restrict__ bsp,
                                                    char* __restrict__ ws) {
  extern __shared__ __attribute__((aligned(16))) char smem[];
  const int lane = threadIdx.x & 63;
  const int wid = __builtin_amdgcn_readfirstlane(threadIdx.x >> 6);
  const int xcd = blockIdx.x & 7, idx = blockIdx.x >> 3;
  const int tp = (xcd >> 1) * 8 + (idx & 7);
  const int fp = (xcd & 1) * 4 + (idx >> 3);
  const int g = lane >> 4;

  STAMP_DECL STAMP();
  const char* abase = (const char*)(wsp + (size_t)(tp * 64) * S_ + wid * 64);
  const unsigned avo0 = (lane >> 3) * (S_ * 2) + (((lane & 7) ^ g) * 16);
  const unsigned avo1 = (lane >> 3) * (S_ * 2) + (((lane & 7) ^ (g + 4)) * 16);
  const unsigned ring = lds_addr(smem) + 1024 + wid * 32768;
  const char* stg = smem + 1024 + wid * 32768;
  const char* bbase = ws + WS_Z2 + ((size_t)(wid * 2) * 56 + fp * 7 + 3) * 1024;
  const unsigned bvo = lane * 16;
  const int rd0 = (lane & 15) * 128 + ((g ^ ((lane & 15) >> 1)) * 16);
  const int rd1 = rd0 ^ 64;

  v4u Bf[2][14];
  v4u Af[2][8];
  f32x4 acc[4][7];
#pragma unroll
  for (int m = 0; m < 4; ++m)
#pragma unroll
    for (int n = 0; n < 7; ++n) acc[m][n] = (f32x4){0.f, 0.f, 0.f, 0.f};

#define K2_ROT 0
  const int rot = K2_ROT;
#define RJ(j) (4 * (j))
#define LOAD_A1(j, i) glds16(((i) & 1) ? avo1 : avo0, abase + (size_t)(i) * 8 * (S_ * 2) + RJ(j) * 128, ring + ((j) & 3) * 8192 + (i) * 1024);
#define LOAD_A(j) LOAD_A1(j, 0) LOAD_A1(j, 1) LOAD_A1(j, 2) LOAD_A1(j, 3) LOAD_A1(j, 4) LOAD_A1(j, 5) LOAD_A1(j, 6) LOAD_A1(j, 7)
#define LOAD_B1(j, ks, n) gload_a<((n) - 3) * 1024>(Bf[(j) & 1][(ks) * 7 + (n)], bvo, bbase + (size_t)(2 * RJ(j) + (ks)) * 56 * 1024);
#define LOAD_Bk(j, ks) LOAD_B1(j, ks, 0) LOAD_B1(j, ks, 1) LOAD_B1(j, ks, 2) LOAD_B1(j, ks, 3) LOAD_B1(j, ks, 4) LOAD_B1(j, ks, 5) LOAD_B1(j, ks, 6)
#define LOAD_B(j) LOAD_Bk(j, 0) LOAD_Bk(j, 1)
#define READ_A(j) _Pragma("unroll") for (int m = 0; m < 4; ++m) _Pragma("unroll") for (int ks = 0; ks < 2; ++ks) \
    Af[(j) & 1][m * 2 + ks] = *(const v4u*)(stg + ((j) & 3) * 8192 + ((m + wid) & 3) * 2048 + (ks ? rd1 : rd0));
#define WAIT_B(N, P) asm volatile("s_waitcnt vmcnt(" #N ")" : "+a"(Bf[P][0]), "+a"(Bf[P][1]), "+a"(Bf[P][2]), "+a"(Bf[P][3]), "+a"(Bf[P][4]), \
    "+a"(Bf[P][5]), "+a"(Bf[P][6]), "+a"(Bf[P][7]), "+a"(Bf[P][8]), "+a"(Bf[P][9]), "+a"(Bf[P][10]), "+a"(Bf[P][11]), "+a"(Bf[P][12]), "+a"(Bf[P][13]) :: "memory");
#define MMA(j, ks, n) _Pragma("unroll") for (int m = 0; m < 4; ++m) acc[m][n] = mfma16(Bf[(j) & 1][(ks) * 7 + (n)], Af[(j) & 1][m * 2 + (ks)], acc[m][n]);
#define STEP_BODY(j, LB, LA) \
  MMA(j, 0, 0) SCHED(); if (LB) { LOAD_B1((j) + 2, 0, 0) } if (LA) { LOAD_A1((j) + 3, 0) } \
  MMA(j, 0, 1) SCHED(); if (LB) { LOAD_B1((j) + 2, 0, 1) } if (LA) { LOAD_A1((j) + 3, 1) } \
  MMA(j, 0, 2) SCHED(); if (LB) { LOAD_B1((j) + 2, 0, 2) } if (LA) { LOAD_A1((j) + 3, 2) } \
  MMA(j, 0, 3) SCHED(); if (LB) { LOAD_B1((j) + 2, 0, 3) } if (LA) { LOAD_A1((j) + 3, 3) } \
  MMA(j, 0, 4) SCHED(); if (LB) { LOAD_B1((j) + 2, 0, 4) } if (LA) { LOAD_A1((j) + 3, 4) } \
  MMA(j, 0, 5) SCHED(); if (LB) { LOAD_B1((j) + 2, 0, 5) } if (LA) { LOAD_A1((j) + 3, 5) } \
  MMA(j, 0, 6) SCHED(); if (LB) { LOAD_B1((j) + 2, 0, 6) } if (LA) { LOAD_A1((j) + 3, 6) } \
  MMA(j, 1, 0) SCHED(); if (LB) { LOAD_B1((j) + 2, 1, 0) } if (LA) { LOAD_A1((j) + 3, 7) } \
  MMA(j, 1, 1) SCHED(); if (LB) { LOAD_B1((j) + 2, 1, 1) } \
  MMA(j, 1, 2) SCHED(); if (LB) { LOAD_B1((j) + 2, 1, 2) } \
  MMA(j, 1, 3) SCHED(); if (LB) { LOAD_B1((j) + 2, 1, 3) } \
  MMA(j, 1, 4) SCHED(); if (LB) { LOAD_B1((j) + 2, 1, 4) } \
  MMA(j, 1, 5) SCHED(); if (LB) { LOAD_B1((j) + 2, 1, 5) } \
  MMA(j, 1, 6) SCHED(); if (LB) { LOAD_B1((j) + 2, 1, 6) } \
  SCHED();

  LOAD_A(0) LOAD_B(0) LOAD_A(1)
  LOAD_B(1) LOAD_A(2)
  STAMP();
  asm volatile("s_waitcnt vmcnt(44)" ::: "memory");
  STAMP();
  READ_A(0)
  SCHED();
  WAIT_B(22, 0) STAMP(); READ_A(1) SCHED(); STEP_BODY(0, 1, 1)
  WAIT_B(22, 1) STAMP(); READ_A(2) SCHED(); STEP_BODY(1, 1, 1)
  WAIT_B(22, 0) STAMP(); READ_A(3) SCHED(); STEP_BODY(2, 1, 1)
  WAIT_B(22, 1) STAMP(); READ_A(4) SCHED(); STEP_BODY(3, 1, 1)
  WAIT_B(22, 0) STAMP(); READ_A(5) SCHED(); STEP_BODY(4, 1, 1)
  WAIT_B(22, 1) STAMP(); READ_A(6) SCHED(); STEP_BODY(5, 1, 0)
  WAIT_B(14, 0) STAMP(); READ_A(7) SCHED();
  v2u z1v[7];
  {
    const char* z1 = ws + WS_Z1 + (size_t)(4 * tp + wid) * 28 * 1024 + lane * 16;
#pragma unroll
    for (int n = 0; n < 7; ++n) { const int ft = fp * 7 + n; z1v[n] = *(const v2u*)(z1 + (ft >> 1) * 1024 + (ft & 1) * 8); }
  }
  const float bias = __builtin_bit_cast(float, (unsigned)bsp[tp * 64 + wid * 16 + (lane & 15)] << 16);

 STEP_BODY(6, 0, 0)
  WAIT_B(8, 1) STAMP(); SCHED();
  {
    char* mine = smem + 1024 + wid * 32768 + lane * 16;
#pragma unroll
    for (int mi = 1; mi <= 4; ++mi) {
      const int m = mi & 3;
#pragma unroll
      for (int ks = 0; ks < 2; ++ks)
#pragma unroll
        for (int n = 0; n < 7; ++n) acc[m][n] = mfma16(Bf[1][ks * 7 + n], Af[1][m * 2 + ks], acc[m][n]);
      SCHED();
      if (mi >= 2) {
#pragma unroll
        for (int n = 0; n < 7; ++n) *(f32x4*)(mine + ((mi - 1) * 7 + n) * 1024) = acc[mi - 1][n];
      }
      SCHED();
    }
  }
  STAMP();
  __syncthreads();
  STAMP();
  f32x4 o[7];
  {
    f32x4 pr[3][7];
#pragma unroll
    for (int d = 1; d < 4; ++d) {
      const char* src = smem + 1024 + ((wid - d) & 3) * 32768 + d * 7168 + lane * 16;
#pragma unroll
      for (int n = 0; n < 7; ++n) pr[d - 1][n] = *(const f32x4*)(src + n * 1024);
    }
    SCHED();
#pragma unroll
    for (int n = 0; n < 7; ++n) o[n] = (acc[0][n] + pr[0][n]) + (pr[1][n] + pr[2][n]);
  }
  char* hp = ws + WS_H + ((size_t)(4 * tp + wid) * 28 * 64 + (lane & 15) + 16 * (g >> 1)) * 16 + (size_t)fp * 7 * 512;
  v2u h[7];
#pragma unroll
  for (int n = 0; n < 7; ++n) {
    h[n][0] = pack_bf16(bf_lo(z1v[n][0]) * (o[n][0] + bias), bf_hi(z1v[n][0]) * (o[n][1] + bias));
    h[n][1] = pack_bf16(bf_lo(z1v[n][1]) * (o[n][2] + bias), bf_hi(z1v[n][1]) * (o[n][3] + bias));
  }
#pragma unroll
  for (int n = 0; n < 6; n += 2) st_wt16(pair16(h[n], h[n + 1]), hp + (n + (g & 1)) * 512);
  st_plain8(h[6], hp + 6 * 512 + (g & 1) * 8);
  STAMP(); STAMP_OUT(2);
}

#define K3_LDS (4 * 12 * 1024)
__global__ __launch_bounds__(256, 1) void k3_out(char* __restrict__ ws, unsigned short* __restrict__ out) {
  extern __shared__ __attribute__((aligned(16))) char smem[];
  const int lane = threadIdx.x & 63;
  const int wid = __builtin_amdgcn_readfirstlane(threadIdx.x >> 6);
  const int xcd = blockIdx.x & 7, idx = blockIdx.x >> 3;
  const int tp = xcd * 4 + (idx & 3);
  const int dp = idx >> 2;
  const int g = lane >> 4;
  STAMP_DECL STAMP();
  const v4u* hp = (const v4u*)(ws + WS_H) + ((size_t)(4 * tp) * 28 + wid * 7) * 64 + lane;
  const v4u* wp = (const v4u*)(ws + WS_WO) + ((size_t)(wid * 7) * 28 + dp * 4) * 64 + lane;
  v4u Hf[7][4], Wf[7][4];
#define K3_LD(k) { _Pragma("unroll") for (int n = 0; n < 4; ++n) Wf[k][n] = wp[((k) * 28 + n) * 64]; \
    _Pragma("unroll") for (int m = 0; m < 4; ++m) Hf[k][m] = hp[(((m + wid) & 3) * 28 + (k)) * 64]; SCHED(); }
#define K3_MM(k) { _Pragma("unroll") for (int m = 0; m < 4; ++m) _Pragma("unroll") for (int n = 0; n < 4; ++n) \
    acc[m][n] = mfma16(Wf[k][n], Hf[k][m], acc[m][n]); SCHED(); }
  f32x4 acc[4][4];
#pragma unroll
  for (int m = 0; m < 4; ++m)
#pragma unroll
    for (int n = 0; n < 4; ++n) acc[m][n] = (f32x4){0.f, 0.f, 0.f, 0.f};
  K3_LD(0) K3_LD(1) K3_LD(2) K3_LD(3)
  STAMP();
  K3_MM(0) K3_LD(4) K3_MM(1) K3_LD(5) K3_MM(2) K3_LD(6) K3_MM(3) K3_MM(4) K3_MM(5) K3_MM(6)
  STAMP();
#pragma unroll
  for (int m = 1; m < 4; ++m)
#pragma unroll
    for (int n = 0; n < 4; ++n) *(f32x4*)(smem + ((wid * 3 + m - 1) * 4 + n) * 1024 + lane * 16) = acc[m][n];
  __syncthreads();
  STAMP();
  f32x4 pr[3][4];
#pragma unroll
  for (int d = 1; d < 4; ++d)
#pragma unroll
    for (int n = 0; n < 4; ++n) pr[d - 1][n] = *(const f32x4*)(smem + ((((wid - d) & 3) * 3 + d - 1) * 4 + n) * 1024 + lane * 16);
  unsigned short* op = out + (size_t)(tp * 64 + wid * 16 + (lane & 15)) * D_ + dp * 64 + 16 * (g & 1) + 8 * (g >> 1);
  v2u h[4];
#pragma unroll
  for (int n = 0; n < 4; ++n) {
    const f32x4 o = (acc[0][n] + pr[0][n]) + (pr[1][n] + pr[2][n]);
    h[n][0] = pack_bf16(o[0], o[1]);
    h[n][1] = pack_bf16(o[2], o[3]);
  }
  st_wt16(pair16(h[0], h[1]), op);
  st_wt16(pair16(h[2], h[3]), op + 32);
  STAMP(); STAMP_OUT(0);
}

extern "C" void kernel_launch(void* const* d_in, const int* in_sizes, int n_in,
                              void* d_out, int out_size, void* d_ws, size_t ws_size,
                              hipStream_t stream) {
  const unsigned short* x = (const unsigned short*)d_in[0];
  const unsigned short* win = (const unsigned short*)d_in[1];
  const unsigned short* wsp = (const unsigned short*)d_in[2];
  const unsigned short* bsp = (const unsigned short*)d_in[3];
  const unsigned short* wout = (const unsigned short*)d_in[4];
  char* ws = (char*)d_ws + ((ws_size / 2) & ~(size_t)((2u << 20) - 1));
  (void)hipFuncSetAttribute((const void*)k1_gemm_gelu, hipFuncAttributeMaxDynamicSharedMemorySize, K1_LDS);
  (void)hipFuncSetAttribute((const void*)k2_spatial, hipFuncAttributeMaxDynamicSharedMemorySize, K2_LDS);
  (void)hipFuncSetAttribute((const void*)k3_out, hipFuncAttributeMaxDynamicSharedMemorySize, K3_LDS);
  k1_gemm_gelu<<<256, 640, K1_LDS, stream>>>(x, win, wsp, wout, ws, (const char*)d_out);
  k2_spatial<<<256, 256, K2_LDS, stream>>>(wsp, bsp, ws);
  k3_out<<<224, 256, K3_LDS, stream>>>(ws, (unsigned short*)d_out);
}
```

```cpp
#include <hip/hip_runtime.h>
#include <hip/hip_bf16.h>
#include <stdint.h>

typedef __attribute__((ext_vector_type(8))) short bf16x8;
typedef __attribute__((ext_vector_type(4))) short s16x4;
typedef __attribute__((ext_vector_type(4))) float f32x4;
typedef __attribute__((ext_vector_type(4))) unsigned v4u;
typedef __attribute__((ext_vector_type(2))) unsigned v2u;
typedef __attribute__((address_space(3))) s16x4* lds_s16x4_p;

#define S_ 2048
#define D_ 448
#define F_ 1792
#define FH_ 896

#define WS_Z1 0
#define WS_Z2 (WS_Z1 + 3670016)
#define WS_H  (WS_Z2 + 3670016)
#define WS_WO (WS_H + 3670016)

#define SCHED() __builtin_amdgcn_sched_barrier(0)

__device__ __forceinline__ f32x4 mfma16(v4u a, v4u b, f32x4 c) {
  return __builtin_amdgcn_mfma_f32_16x16x32_bf16(__builtin_bit_cast(bf16x8, a), __builtin_bit_cast(bf16x8, b), c, 0, 0, 0);
}
typedef __attribute__((ext_vector_type(2))) float f32x2;
typedef __attribute__((ext_vector_type(2))) __bf16 bf16x2;
__device__ __forceinline__ unsigned pack_bf16(float lo, float hi) {
  const bf16x2 r = __builtin_convertvector((f32x2){lo, hi}, bf16x2);
  return __builtin_bit_cast(unsigned, r);
}
__device__ __forceinline__ float asf(unsigned u) { return __builtin_bit_cast(float, u); }
__device__ __forceinline__ float bf_lo(unsigned u) { return __builtin_bit_cast(float, u << 16); }
__device__ __forceinline__ float bf_hi(unsigned u) { return __builtin_bit_cast(float, u & 0xffff0000u); }

__device__ __forceinline__ void glds16(unsigned voff, const void* sbase, unsigned lds) {
  asm volatile("s_mov_b32 m0, %2\n\ts_nop 0\n\tglobal_load_lds_dwordx4 %0, %1" ::"v"(voff), "s"(sbase), "s"(lds) : "memory");
}
__device__ __forceinline__ unsigned lds_addr(const void* p) { return (unsigned)(size_t)(__attribute__((address_space(3))) const char*)p; }

__device__ __forceinline__ void st_wt16(v4u d, void* p) { asm volatile("global_store_dwordx4 %0, %1, off sc1\n\ts_nop 1" ::"v"(p), "v"(d) : "memory"); }
__device__ __forceinline__ v4u pair16(v2u a, v2u b) {
  const auto r0 = __builtin_amdgcn_permlane16_swap(a[0], b[0], false, false);
  const auto r1 = __builtin_amdgcn_permlane16_swap(a[1], b[1], false, false);
  return (v4u){r0[0], r1[0], r0[1], r1[1]};
}
__device__ __forceinline__ void st_wt8(v2u d, void* p) { asm volatile("global_store_dwordx2 %0, %1, off sc1" ::"v"(p), "v"(d) : "memory"); }

__device__ __forceinline__ float gelu_f(float x) {
  const float c0 = -2.0f * 0.7978845608028654f * 1.4426950408889634f;
  const float c1 = c0 * 0.044715f;
  float t = __builtin_fmaf(x * x, c1, c0);
  float e = __builtin_amdgcn_exp2f(x * t);
  return x * __builtin_amdgcn_rcpf(1.0f + e);
}

#define K1_XS 16384
#define K1_WB (6 * K1_XS)
#define K1_LDS (K1_WB + 8 * 8192)
#define WS_EX (WS_WO + 802816)
#define EX_EC 4096
#define EX_CH 6144
#define EX_GRP 6400

__device__ __forceinline__ void ld8_sc1(v2u& d, unsigned voff, const void* sb) { asm volatile("global_load_dwordx2 %0, %1, %2 sc1" : "=v"(d) : "v"(voff), "s"(sb) : "memory"); }
__device__ __forceinline__ void ld4_sc1(unsigned& d, unsigned voff, const void* sb) { asm volatile("global_load_dword %0, %1, %2 sc1" : "=v"(d) : "v"(voff), "s"(sb) : "memory"); }
__device__ __forceinline__ void st8_sc1(v2u d, unsigned voff, const void* sb) { asm volatile("global_store_dwordx2 %0, %1, %2 sc1" ::"v"(voff), "v"(d), "s"(sb) : "memory"); }
__device__ __forceinline__ void st4_sc1(unsigned d, unsigned voff, const void* sb) { asm volatile("global_store_dword %0, %1, %2 sc1" ::"v"(voff), "v"(d), "s"(sb) : "memory"); }
template <int CTRL>
__device__ __forceinline__ float dpp_f(float v) {
  return __builtin_bit_cast(float, __builtin_amdgcn_update_dpp(0, __builtin_bit_cast(int, v), CTRL, 0xf, 0xf, true));
}
#define DPP_XOR1 0xB1
#define DPP_XOR3 0x1B
#define DPP_XOR7 0x141
#define DPP_XOR15 0x140
#define DPP_XOR8 0x128
#define DPP_BC(n) (0x150 + (n))

__global__ __launch_bounds__(640, 3) void k1_gemm_gelu(const unsigned short* __restrict__ x, const unsigned short* __restrict__ win,
                                                      const unsigned short* __restrict__ wsp, const unsigned short* __restrict__ wout,
                                                      char* __restrict__ ws, const char* __restrict__ outp) {
  extern __shared__ __attribute__((aligned(16))) char smem[];
  const int lane = threadIdx.x & 63;
  const int wid = __builtin_amdgcn_readfirstlane(threadIdx.x >> 6);
  const int xcd = blockIdx.x & 7, idx = blockIdx.x >> 3;
  const int g = lane >> 4, j = lane & 15;
  if (idx >= 28) {
    if (wid >= 8) return;
    const int hw = ((idx - 28) * 8 + xcd) * 8 + wid;
    const char* pb = (const char*)wsp + ((size_t)((xcd >> 1) * 512 + ((xcd & 1) * 4 + idx - 28) * 64 + wid * 8) * S_) * 2;
    unsigned tdst = 0;
    if (idx == 29) {
      const char* b = wid < 3 ? ws + wid * (64u << 16) : wid < 5 ? (const char*)wsp + (wid - 3) * (64u << 16) : outp;
      const unsigned off = wid < 5 ? lane << 16 : min(lane, 27) << 16;
      asm volatile("global_load_dword %0, %1, %2" : "+v"(tdst) : "v"(off), "s"(b) : "memory");
    }
    if (idx == 28 && wid < 4) {
      unsigned long long pc; int d0, len;
      asm volatile("s_getpc_b64 %0\n.Lpf_here_%=:\n\ts_mov_b32 %1, _Z12k1_gemm_geluPKtS0_S0_S0_PcPKc-.Lpf_here_%=\n\t"
                   "s_mov_b32 %2, _Z6k3_outPcPt-_Z12k1_gemm_geluPKtS0_S0_S0_PcPKc" : "=s"(pc), "=s"(d0), "=s"(len));
      const unsigned off = min((unsigned)(wid * 8192 + lane * 128), (unsigned)(len + 2560));
      asm volatile("global_load_dword %0, %1, %2" : "+v"(tdst) : "v"(off), "s"(pc + (long long)d0) : "memory");
    }
#pragma unroll
    for (int i = 0; i < 4; ++i)
      asm volatile("global_load_dword %0, %1, %2" : "+v"(tdst) : "v"(lane * 128), "s"(pb + i * 8192) : "memory");
    unsigned short v[4][8];
#pragma unroll
    for (int i = 0; i < 4; ++i) {
      const int b = min(hw + 256 * i, 783), kf = b / 28, dt = b % 28;
#pragma unroll
      for (int e = 0; e < 8; ++e) v[i][e] = wout[(size_t)(32 * kf + 8 * g + e) * D_ + 16 * dt + j];
    }
#pragma unroll
    for (int i = 0; i < 4; ++i) {
      const int b = min(hw + 256 * i, 783);
      v4u o;
#pragma unroll
      for (int q = 0; q < 4; ++q) o[q] = (unsigned)v[i][2 * q] | ((unsigned)v[i][2 * q + 1] << 16);
      *(v4u*)(ws + WS_WO + ((size_t)b * 64 + lane) * 16) = o;
    }
    asm volatile("s_waitcnt vmcnt(0)" : "+v"(tdst)::"memory");
    return;
  }
  const int rp = (xcd >> 1) * 4 + (idx & 3);
  const int cp = (xcd & 1) * 7 + (idx >> 2);

  const unsigned sm0 = lds_addr(smem);
#define BAR_L() asm volatile("s_waitcnt lgkmcnt(0)\n\ts_barrier" ::: "memory")
#define BAR_N() asm volatile("s_barrier" ::: "memory")
#define WAITBAR_V(N) asm volatile("s_waitcnt vmcnt(" #N ")\n\ts_barrier" ::: "memory")
  if (wid >= 8) {
    const int w0 = (wid - 8) * 4;
    const char* xb0 = (const char*)(x + (size_t)(rp * 128 + w0 * 16) * D_);
    const unsigned xvo0 = (lane >> 3) * (D_ * 2) + (((lane & 7) ^ g) * 16);
    const unsigned xvo1 = (lane >> 3) * (D_ * 2) + (((lane & 7) ^ (g + 4)) * 16) + 8 * D_ * 2;
    const char* w1 = (const char*)(win + cp * 64 + (size_t)(w0 * 8) * F_);
    const char* w2 = w1 + FH_ * 2;
    const unsigned wvoa = (lane >> 3) * (F_ * 2) + (((lane & 7) ^ (2 * (g & 1))) * 16);
    const unsigned wvob = wvoa ^ 64;
    const unsigned xl = sm0 + w0 * 2048, wl = sm0 + K1_WB + w0 * 1024;
#define K1_WL(base, c, dst, dstr) { _Pragma("unroll") for (int q = 0; q < 4; ++q) \
      glds16((q & 1) ? wvob : wvoa, (base) + (size_t)((c) * 64 + q * 8) * (F_ * 2), (dst) + q * (dstr)); }
#define K1_C(c) { _Pragma("unroll") for (int q = 0; q < 4; ++q) { \
      glds16(xvo0, xb0 + q * 16 * D_ * 2 + (c) * 128, xl + (c) * K1_XS + q * 2048); \
      glds16(xvo1, xb0 + q * 16 * D_ * 2 + (c) * 128, xl + (c) * K1_XS + q * 2048 + 1024); } \
      K1_WL(w2, c, wl + (c) * 8192, 1024) }
    K1_C(0) K1_C(1) K1_C(2)
    WAITBAR_V(24);
    K1_C(3)
    WAITBAR_V(24);
    K1_C(4)
    WAITBAR_V(24);
    K1_C(5)
    WAITBAR_V(24);
    K1_WL(w2, 6, wl + 6 * 8192, 1024) K1_WL(w1, 0, wl + 7 * 8192, 1024) K1_WL(w1, 1, xl, 2048)
    WAITBAR_V(24);
    K1_WL(w1, 2, xl + 1024, 2048) K1_WL(w1, 3, xl + K1_XS, 2048) K1_WL(w1, 4, xl + K1_XS + 1024, 2048)
    WAITBAR_V(24);
    K1_WL(w1, 5, xl + 2 * K1_XS, 2048) K1_WL(w1, 6, xl + 2 * K1_XS + 1024, 2048)
    WAITBAR_V(28);
    WAITBAR_V(0);
    return;
  }
  const char* ex = ws + WS_EX + (size_t)(rp * 8 + wid) * EX_GRP;
  const unsigned gr_off = (g * 8 + (j >> 1)) * 8;
  const unsigned in_vo = (j & 1) * 256 + gr_off;
  const unsigned pin_vo = EX_EC + cp * 128 + j * 8;
  const unsigned pout_vo = EX_EC + j * 128 + cp * 8;
  v2u in[7], pin;
#define K1_LDIN() { _Pragma("unroll") for (int k = 0; k < 7; ++k) ld8_sc1(in[k], in_vo, ex + k * 512); }
#define K1_INREGS "+v"(in[0]), "+v"(in[1]), "+v"(in[2]), "+v"(in[3]), "+v"(in[4]), "+v"(in[5]), "+v"(in[6])
  v4u xf[14];
  v2u scn;
  ld8_sc1(scn, lane < 32 ? cp * 256 + lane * 8 : pin_vo, ex);
  asm volatile("global_load_dwordx4 %0, %2, %3 offset:768\n\tglobal_load_dwordx4 %1, %2, %3 offset:832"
               : "=&v"(xf[12]), "=&v"(xf[13]) : "v"(j * (D_ * 2) + g * 16), "s"((const char*)(x + (size_t)(rp * 128 + wid * 16) * D_)) : "memory");
  const int xrd0 = wid * 2048 + j * 128 + ((g ^ (j >> 1)) * 16), xrd1 = xrd0 ^ 64;
  const int fsw = 2 * (((j >> 3) & 1) + 2 * (g & 1));
  const int tra = g * 1024 + (j >> 2) * 128 + (((j >> 1) & 1) * 16) + 8 * (j & 1);
  const int trb = g * 2048 + (j >> 2) * 128 + (((j >> 1) & 1) * 16) + 8 * (j & 1);
  v4u wf[2][8];
  f32x4 acc[4];
#pragma unroll
  for (int n = 0; n < 4; ++n) acc[n] = (f32x4){0.f, 0.f, 0.f, 0.f};
#define K1_STEP(RDX, c, RDW, b, off, tb, kstr, MM, cm, ZT) { \
    if (RDX) { xf[2 * (c)] = *(const v4u*)(smem + (c) * K1_XS + xrd0); xf[2 * (c) + 1] = *(const v4u*)(smem + (c) * K1_XS + xrd1); } \
    _Pragma("unroll") for (int i = 0; i < 8; ++i) { \
      const int k2 = i >> 2, n = i & 3; \
      if (RDW) { const char* sb = smem + (off) + (tb) + (((2 * n) ^ fsw) * 16) + k2 * (kstr); \
        s16x4 t0 = __builtin_amdgcn_ds_read_tr16_b64_v4i16((lds_s16x4_p)(sb)); \
        s16x4 t1 = __builtin_amdgcn_ds_read_tr16_b64_v4i16((lds_s16x4_p)(sb + 512)); \
        v2u a_ = __builtin_bit_cast(v2u, t0), b_ = __builtin_bit_cast(v2u, t1); \
        wf[b][i] = (v4u){a_[0], a_[1], b_[0], b_[1]}; } \
      if (MM) acc[n] = ZT ? mfma16(xf[2 * (cm) + k2], wf[(b) ^ 1][i], acc[n]) : mfma16(wf[(b) ^ 1][i], xf[2 * (cm) + k2], acc[n]); \
      SCHED(); } }

  BAR_N();
  K1_STEP(1, 0, 1, 0, K1_WB, tra, 4096, 0, 0, 1)
  asm volatile("s_waitcnt vmcnt(2)" : "+v"(scn)::"memory");
  unsigned nonce = 1;
  while (__any(scn[0] == nonce)) ++nonce;
  if (lane == 0) st4_sc1(nonce, EX_CH + cp * 4, ex);
  BAR_L();
  K1_STEP(1, 1, 1, 1, K1_WB + 8192, tra, 4096, 1, 0, 1)
  BAR_L();
  K1_STEP(1, 2, 1, 0, K1_WB + 2 * 8192, tra, 4096, 1, 1, 1)
  BAR_L();
  K1_STEP(1, 3, 1, 1, K1_WB + 3 * 8192, tra, 4096, 1, 2, 1)
  BAR_N();
  unsigned chal;
  ld4_sc1(chal, EX_CH + j * 4, ex);
  K1_STEP(1, 4, 1, 0, K1_WB + 4 * 8192, tra, 4096, 1, 3, 1)
  BAR_N();
  K1_STEP(1, 5, 1, 1, K1_WB + 5 * 8192, tra, 4096, 1, 4, 1)
  asm volatile("s_waitcnt vmcnt(2)\n\ts_barrier" : "+v"(xf[12]), "+v"(xf[13])::"memory");
  K1_STEP(0, 6, 1, 0, K1_WB + 6 * 8192, tra, 4096, 1, 5, 1)
  asm volatile("s_waitcnt vmcnt(0)" : "+v"(chal)::"memory");
  unsigned answered = chal;
  if (lane < 16) st8_sc1((v2u){answered, nonce}, pout_vo, ex);
  K1_STEP(0, 0, 0, 1, 0, trb, 8192, 1, 6, 1)
  float v[4][4];
  unsigned myval;
  {
    float sh[4], S[4], Q[4];
#pragma unroll
    for (int n = 0; n < 4; ++n)
#pragma unroll
      for (int r = 0; r < 4; ++r) { v[n][r] = gelu_f(acc[n][r]); acc[n][r] = 0.f; }
#pragma unroll
    for (int r = 0; r < 4; ++r) {
      sh[r] = dpp_f<DPP_BC(0)>(v[0][r]);
      const float d0 = v[0][r] - sh[r], d1 = v[1][r] - sh[r], d2 = v[2][r] - sh[r], d3 = v[3][r] - sh[r];
      S[r] = (d0 + d1) + (d2 + d3);
      Q[r] = (d0 * d0 + d1 * d1) + (d2 * d2 + d3 * d3);
    }
    const bool b3 = j & 8, b2 = j & 4, b1 = j & 2;
    float T[4];
#pragma unroll
    for (int r = 0; r < 4; ++r) T[r] = (b3 ? Q[r] : S[r]) + dpp_f<DPP_XOR15>(b3 ? S[r] : Q[r]);
    const float U0 = (b2 ? T[2] : T[0]) + dpp_f<DPP_XOR7>(b2 ? T[0] : T[2]);
    const float U1 = (b2 ? T[3] : T[1]) + dpp_f<DPP_XOR7>(b2 ? T[1] : T[3]);
    float W = (b1 ? U1 : U0) + dpp_f<DPP_XOR3>(b1 ? U0 : U1);
    W += dpp_f<DPP_XOR1>(W);
    const float other = dpp_f<DPP_XOR8>(W);
    const float shr = b2 ? (b1 ? sh[3] : sh[2]) : (b1 ? sh[1] : sh[0]);
    myval = __builtin_bit_cast(unsigned, b3 ? W - other * other * (1.0f / 64.0f) : shr + W * (1.0f / 64.0f));
  }
  if (!(j & 1)) st8_sc1((v2u){nonce, myval}, cp * 256 + gr_off, ex);
  ld8_sc1(pin, pin_vo, ex);
  ld4_sc1(chal, EX_CH + j * 4, ex);
  BAR_N();
  K1_STEP(0, 0, 1, 0, K1_WB + 7 * 8192, tra, 4096, 0, 0, 0)
  K1_STEP(0, 0, 1, 1, 0, trb, 8192, 1, 0, 0)
  K1_STEP(0, 0, 1, 0, 1024, trb, 8192, 1, 1, 0)
  K1_STEP(0, 0, 1, 1, K1_XS, trb, 8192, 1, 2, 0)
  K1_STEP(0, 0, 1, 0, K1_XS + 1024, trb, 8192, 1, 3, 0)
  K1_STEP(0, 0, 1, 1, 2 * K1_XS, trb, 8192, 1, 4, 0)
  K1_STEP(0, 0, 1, 0, 2 * K1_XS + 1024, trb, 8192, 1, 5, 0)
  K1_STEP(0, 0, 0, 1, 0, trb, 8192, 1, 6, 0)
  asm volatile("s_waitcnt vmcnt(0)" : "+v"(pin), "+v"(chal)::"memory");
  for (unsigned spin = 0; spin < (1u << 16); ++spin) {
    const bool pv = pin[0] == nonce, live = j < 14;
    const unsigned cur = pv ? pin[1] : chal;
    if (__any(live && cur != answered)) { answered = cur; if (lane < 16) st8_sc1((v2u){answered, nonce}, pout_vo, ex); }
    if (__all(pv || !live)) break;
    ld8_sc1(pin, pin_vo, ex);
    ld4_sc1(chal, EX_CH + j * 4, ex);
    asm volatile("s_waitcnt vmcnt(0)" : "+v"(pin), "+v"(chal)::"memory");
  }
  K1_LDIN()
  unsigned tg[7];
#pragma unroll
  for (int k = 0; k < 7; ++k) tg[k] = __shfl(answered, 2 * k + (j & 1));
  {
    char* z1 = ws + WS_Z1 + ((size_t)((8 * rp + wid) * 28 + cp * 2) * 64 + lane) * 16;
#pragma unroll
    for (int n = 0; n < 4; n += 2) {
      v4u o;
      o[0] = pack_bf16(gelu_f(acc[n][0]), gelu_f(acc[n][1]));
      o[1] = pack_bf16(gelu_f(acc[n][2]), gelu_f(acc[n][3]));
      o[2] = pack_bf16(gelu_f(acc[n + 1][0]), gelu_f(acc[n + 1][1]));
      o[3] = pack_bf16(gelu_f(acc[n + 1][2]), gelu_f(acc[n + 1][3]));
      st_wt16(o, z1 + n * 512);
    }
  }
  asm volatile("s_waitcnt vmcnt(2)" : K1_INREGS::"memory");
  for (unsigned spin = 0; spin < (1u << 16); ++spin) {
    bool okd = true;
#pragma unroll
    for (int k = 0; k < 7; ++k) okd = okd && in[k][0] == tg[k];
    if (__all(okd)) break;
    K1_LDIN()
    asm volatile("s_waitcnt vmcnt(0)" : K1_INREGS::"memory");
  }
  {
    float sm = ((asf(in[0][1]) + asf(in[1][1])) + (asf(in[2][1]) + asf(in[3][1]))) + ((asf(in[4][1]) + asf(in[5][1])) + asf(in[6][1]));
    sm += dpp_f<DPP_XOR1>(sm);
    const float mean = sm * (1.0f / 14.0f);
    float bt = 0.f;
#pragma unroll
    for (int k = 0; k < 7; ++k) { const float e = asf(in[k][1]) - mean; bt = __builtin_fmaf(e, e, bt); }
    bt += dpp_f<DPP_XOR1>(bt);
    const float between = dpp_f<DPP_XOR8>(bt);
    const float rstd = rsqrtf((sm + 64.0f * between) * (1.0f / FH_) + 1e-5f);
    float mr[4], rr[4];
    mr[0] = dpp_f<DPP_BC(0)>(mean); mr[1] = dpp_f<DPP_BC(2)>(mean); mr[2] = dpp_f<DPP_BC(4)>(mean); mr[3] = dpp_f<DPP_BC(6)>(mean);
    rr[0] = dpp_f<DPP_BC(8)>(rstd); rr[1] = dpp_f<DPP_BC(10)>(rstd); rr[2] = dpp_f<DPP_BC(12)>(rstd); rr[3] = dpp_f<DPP_BC(14)>(rstd);
    char* z2 = ws + WS_Z2 + ((size_t)((4 * rp + (wid >> 1)) * 56 + cp * 4 + (g & 1)) * 64 + j + 16 * (2 * (wid & 1) + (g >> 1))) * 16;
    v2u o[4];
#pragma unroll
    for (int n = 0; n < 4; ++n) {
      o[n][0] = pack_bf16((v[n][0] - mr[0]) * rr[0], (v[n][1] - mr[1]) * rr[1]);
      o[n][1] = pack_bf16((v[n][2] - mr[2]) * rr[2], (v[n][3] - mr[3]) * rr[3]);
    }
    st_wt16(pair16(o[0], o[1]), z2);
    st_wt16(pair16(o[2], o[3]), z2 + 2048);
  }
}

#define K2_LDS (4 * 32768 + 1024)

template <int OFF>
__device__ __forceinline__ void gload_a(v4u& dst, unsigned voff, const void* sbase) {
  asm volatile("global_load_dwordx4 %0, %1, %2 offset:%3" : "=a"(dst) : "v"(voff), "s"(sbase), "i"(OFF) : "memory");
}

__global__ __launch_bounds__(256, 1) void k2_spatial(const unsigned short* __restrict__ wsp, const unsigned short* __restrict__ bsp,
                                                    char* __restrict__ ws) {
  extern __shared__ __attribute__((aligned(16))) char smem[];
  const int lane = threadIdx.x & 63;
  const int wid = __builtin_amdgcn_readfirstlane(threadIdx.x >> 6);
  const int xcd = blockIdx.x & 7, idx = blockIdx.x >> 3;
  const int tp = (xcd >> 1) * 8 + (idx & 7);
  const int fp = (xcd & 1) * 4 + (idx >> 3);
  const int g = lane >> 4;

  const char* abase = (const char*)(wsp + (size_t)(tp * 64) * S_ + wid * 64);
  const unsigned avo0 = (lane >> 3) * (S_ * 2) + (((lane & 7) ^ g) * 16);
  const unsigned avo1 = (lane >> 3) * (S_ * 2) + (((lane & 7) ^ (g + 4)) * 16);
  const unsigned ring = lds_addr(smem) + 1024 + wid * 32768;
  const char* stg = smem + 1024 + wid * 32768;
  const char* bbase = ws + WS_Z2 + ((size_t)(wid * 2) * 56 + fp * 7 + 3) * 1024;
  const unsigned bvo = lane * 16;
  const int rd0 = (lane & 15) * 128 + ((g ^ ((lane & 15) >> 1)) * 16);
  const int rd1 = rd0 ^ 64;

  v4u Bf[2][14];
  v4u Af[2][8];
  f32x4 acc[4][7];
#pragma unroll
  for (int m = 0; m < 4; ++m)
#pragma unroll
    for (int n = 0; n < 7; ++n) acc[m][n] = (f32x4){0.f, 0.f, 0.f, 0.f};

#define RJ(j) (4 * (j))
#define LOAD_A1(j, i) glds16(((i) & 1) ? avo1 : avo0, abase + (size_t)(i) * 8 * (S_ * 2) + RJ(j) * 128, ring + ((j) & 3) * 8192 + (i) * 1024);
#define LOAD_A(j) LOAD_A1(j, 0) LOAD_A1(j, 1) LOAD_A1(j, 2) LOAD_A1(j, 3) LOAD_A1(j, 4) LOAD_A1(j, 5) LOAD_A1(j, 6) LOAD_A1(j, 7)
#define LOAD_B1(j, ks, n) gload_a<((n) - 3) * 1024>(Bf[(j) & 1][(ks) * 7 + (n)], bvo, bbase + (size_t)(2 * RJ(j) + (ks)) * 56 * 1024);
#define LOAD_Bk(j, ks) LOAD_B1(j, ks, 0) LOAD_B1(j, ks, 1) LOAD_B1(j, ks, 2) LOAD_B1(j, ks, 3) LOAD_B1(j, ks, 4) LOAD_B1(j, ks, 5) LOAD_B1(j, ks, 6)
#define LOAD_B(j) LOAD_Bk(j, 0) LOAD_Bk(j, 1)
#define READ_A(j) _Pragma("unroll") for (int m = 0; m < 4; ++m) _Pragma("unroll") for (int ks = 0; ks < 2; ++ks) \
    Af[(j) & 1][m * 2 + ks] = *(const v4u*)(stg + ((j) & 3) * 8192 + ((m + wid) & 3) * 2048 + (ks ? rd1 : rd0));
#define WAIT_B(N, P) asm volatile("s_waitcnt vmcnt(" #N ")" : "+a"(Bf[P][0]), "+a"(Bf[P][1]), "+a"(Bf[P][2]), "+a"(Bf[P][3]), "+a"(Bf[P][4]), \
    "+a"(Bf[P][5]), "+a"(Bf[P][6]), "+a"(Bf[P][7]), "+a"(Bf[P][8]), "+a"(Bf[P][9]), "+a"(Bf[P][10]), "+a"(Bf[P][11]), "+a"(Bf[P][12]), "+a"(Bf[P][13]) :: "memory");
#define MMA(j, ks, n) _Pragma("unroll") for (int m = 0; m < 4; ++m) acc[m][n] = mfma16(Bf[(j) & 1][(ks) * 7 + (n)], Af[(j) & 1][m * 2 + (ks)], acc[m][n]);
#define STEP_BODY(j, LB, LA) \
  MMA(j, 0, 0) SCHED(); if (LB) { LOAD_B1((j) + 2, 0, 0) } if (LA) { LOAD_A1((j) + 3, 0) } \
  MMA(j, 0, 1) SCHED(); if (LB) { LOAD_B1((j) + 2, 0, 1) } if (LA) { LOAD_A1((j) + 3, 1) } \
  MMA(j, 0, 2) SCHED(); if (LB) { LOAD_B1((j) + 2, 0, 2) } if (LA) { LOAD_A1((j) + 3, 2) } \
  MMA(j, 0, 3) SCHED(); if (LB) { LOAD_B1((j) + 2, 0, 3) } if (LA) { LOAD_A1((j) + 3, 3) } \
  MMA(j, 0, 4) SCHED(); if (LB) { LOAD_B1((j) + 2, 0, 4) } if (LA) { LOAD_A1((j) + 3, 4) } \
  MMA(j, 0, 5) SCHED(); if (LB) { LOAD_B1((j) + 2, 0, 5) } if (LA) { LOAD_A1((j) + 3, 5) } \
  MMA(j, 0, 6) SCHED(); if (LB) { LOAD_B1((j) + 2, 0, 6) } if (LA) { LOAD_A1((j) + 3, 6) } \
  MMA(j, 1, 0) SCHED(); if (LB) { LOAD_B1((j) + 2, 1, 0) } if (LA) { LOAD_A1((j) + 3, 7) } \
  MMA(j, 1, 1) SCHED(); if (LB) { LOAD_B1((j) + 2, 1, 1) } \
  MMA(j, 1, 2) SCHED(); if (LB) { LOAD_B1((j) + 2, 1, 2) } \
  MMA(j, 1, 3) SCHED(); if (LB) { LOAD_B1((j) + 2, 1, 3) } \
  MMA(j, 1, 4) SCHED(); if (LB) { LOAD_B1((j) + 2, 1, 4) } \
  MMA(j, 1, 5) SCHED(); if (LB) { LOAD_B1((j) + 2, 1, 5) } \
  MMA(j, 1, 6) SCHED(); if (LB) { LOAD_B1((j) + 2, 1, 6) } \
  SCHED();
  LOAD_A(0) LOAD_B(0) LOAD_A(1)
  LOAD_B(1) LOAD_A(2)
  asm volatile("s_waitcnt vmcnt(44)" ::: "memory");
  READ_A(0)
  SCHED();
  WAIT_B(22, 0) READ_A(1) SCHED(); STEP_BODY(0, 1, 1)
  WAIT_B(22, 1) READ_A(2) SCHED(); STEP_BODY(1, 1, 1)
  WAIT_B(22, 0) READ_A(3) SCHED(); STEP_BODY(2, 1, 1)
  WAIT_B(22, 1) READ_A(4) SCHED(); STEP_BODY(3, 1, 1)
  WAIT_B(22, 0) READ_A(5) SCHED(); STEP_BODY(4, 1, 1)
  WAIT_B(22, 1) READ_A(6) SCHED(); STEP_BODY(5, 1, 0)
  WAIT_B(14, 0) READ_A(7) SCHED();
  v2u z1v[7];
  unsigned braw;
  {
    const char* z1 = ws + WS_Z1 + (size_t)(4 * tp + wid) * 28 * 1024;
#pragma unroll
    for (int n = 0; n < 7; ++n) {
      const int ft = fp * 7 + n;
      asm volatile("global_load_dwordx2 %0, %1, %2" : "=v"(z1v[n]) : "v"(bvo), "s"(z1 + (ft >> 1) * 1024 + (ft & 1) * 8) : "memory");
    }
    asm volatile("global_load_ushort %0, %1, %2" : "=v"(braw) : "v"((lane & 15) * 2), "s"(bsp + tp * 64 + wid * 16) : "memory");
  }
  STEP_BODY(6, 0, 0)
  WAIT_B(8, 1) SCHED();
  {
    char* mine = smem + 1024 + wid * 32768 + lane * 16;
#pragma unroll
    for (int mi = 1; mi <= 4; ++mi) {
      const int m = mi & 3;
#pragma unroll
      for (int ks = 0; ks < 2; ++ks)
#pragma unroll
        for (int n = 0; n < 7; ++n) acc[m][n] = mfma16(Bf[1][ks * 7 + n], Af[1][m * 2 + ks], acc[m][n]);
      SCHED();
      if (mi >= 2) {
#pragma unroll
        for (int n = 0; n < 7; ++n) *(f32x4*)(mine + ((mi - 1) * 7 + n) * 1024) = acc[mi - 1][n];
      }
      SCHED();
    }
  }
  __syncthreads();
  f32x4 o[7];
  {
    f32x4 pr[3][7];
#pragma unroll
    for (int d = 1; d < 4; ++d) {
      const char* src = smem + 1024 + ((wid - d) & 3) * 32768 + d * 7168 + lane * 16;
#pragma unroll
      for (int n = 0; n < 7; ++n) pr[d - 1][n] = *(const f32x4*)(src + n * 1024);
    }
    SCHED();
#pragma unroll
    for (int n = 0; n < 7; ++n) o[n] = (acc[0][n] + pr[0][n]) + (pr[1][n] + pr[2][n]);
  }
  asm volatile("s_waitcnt vmcnt(0)" : "+v"(z1v[0]), "+v"(z1v[1]), "+v"(z1v[2]), "+v"(z1v[3]), "+v"(z1v[4]), "+v"(z1v[5]), "+v"(z1v[6]), "+v"(braw)::"memory");
  const float bias = asf(braw << 16);
  char* hp = ws + WS_H + ((size_t)(4 * tp + wid) * 28 * 64 + (lane & 15) + 16 * (g >> 1)) * 16 + (size_t)fp * 7 * 512;
  v2u h[7];
#pragma unroll
  for (int n = 0; n < 7; ++n) {
    h[n][0] = pack_bf16(bf_lo(z1v[n][0]) * (o[n][0] + bias), bf_hi(z1v[n][0]) * (o[n][1] + bias));
    h[n][1] = pack_bf16(bf_lo(z1v[n][1]) * (o[n][2] + bias), bf_hi(z1v[n][1]) * (o[n][3] + bias));
  }
#pragma unroll
  for (int n = 0; n < 6; n += 2) st_wt16(pair16(h[n], h[n + 1]), hp + (n + (g & 1)) * 512);
  st_wt8(h[6], hp + 6 * 512 + (g & 1) * 8);
}

#define K3_LDS (4 * 12 * 1024)
__global__ __launch_bounds__(256, 1) void k3_out(char* __restrict__ ws, unsigned short* __restrict__ out) {
  extern __shared__ __attribute__((aligned(16))) char smem[];
  const int lane = threadIdx.x & 63;
  const int wid = __builtin_amdgcn_readfirstlane(threadIdx.x >> 6);
  const int xcd = blockIdx.x & 7, idx = blockIdx.x >> 3;
  const int tp = xcd * 4 + (idx & 3);
  const int dp = idx >> 2;
  const int g = lane >> 4;
  const v4u* hp = (const v4u*)(ws + WS_H) + ((size_t)(4 * tp) * 28 + wid * 7) * 64 + lane;
  const v4u* wp = (const v4u*)(ws + WS_WO) + ((size_t)(wid * 7) * 28 + dp * 4) * 64 + lane;
  v4u Hf[7][4], Wf[7][4];
#define K3_LD(k) { _Pragma("unroll") for (int n = 0; n < 4; ++n) Wf[k][n] = wp[((k) * 28 + n) * 64]; \
    _Pragma("unroll") for (int m = 0; m < 4; ++m) Hf[k][m] = hp[(((m + wid) & 3) * 28 + (k)) * 64]; SCHED(); }
#define K3_MM(k) { _Pragma("unroll") for (int m = 0; m < 4; ++m) _Pragma("unroll") for (int n = 0; n < 4; ++n) \
    acc[m][n] = mfma16(Wf[k][n], Hf[k][m], acc[m][n]); SCHED(); }
  f32x4 acc[4][4];
#pragma unroll
  for (int m = 0; m < 4; ++m)
#pragma unroll
    for (int n = 0; n < 4; ++n) acc[m][n] = (f32x4){0.f, 0.f, 0.f, 0.f};
  K3_LD(0) K3_LD(1) K3_LD(2) K3_LD(3)
  K3_MM(0) K3_LD(4) K3_MM(1) K3_LD(5) K3_MM(2) K3_LD(6) K3_MM(3) K3_MM(4) K3_MM(5) K3_MM(6)
#pragma unroll
  for (int m = 1; m < 4; ++m)
#pragma unroll
    for (int n = 0; n < 4; ++n) *(f32x4*)(smem + ((wid * 3 + m - 1) * 4 + n) * 1024 + lane * 16) = acc[m][n];
  __syncthreads();
  f32x4 pr[3][4];
#pragma unroll
  for (int d = 1; d < 4; ++d)
#pragma unroll
    for (int n = 0; n < 4; ++n) pr[d - 1][n] = *(const f32x4*)(smem + ((((wid - d) & 3) * 3 + d - 1) * 4 + n) * 1024 + lane * 16);
  unsigned short* op = out + (size_t)(tp * 64 + wid * 16 + (lane & 15)) * D_ + dp * 64 + 16 * (g & 1) + 8 * (g >> 1);
  v2u h[4];
#pragma unroll
  for (int n = 0; n < 4; ++n) {
    const f32x4 o = (acc[0][n] + pr[0][n]) + (pr[1][n] + pr[2][n]);
    h[n][0] = pack_bf16(o[0], o[1]);
    h[n][1] = pack_bf16(o[2], o[3]);
  }
  st_wt16(pair16(h[0], h[1]), op);
  st_wt16(pair16(h[2], h[3]), op + 32);
}

extern "C" void kernel_launch(void* const* d_in, const int* in_sizes, int n_in,
                              void* d_out, int out_size, void* d_ws, size_t ws_size,
                              hipStream_t stream) {
  const unsigned short* x = (const unsigned short*)d_in[0];
  const unsigned short* win = (const unsigned short*)d_in[1];
  const unsigned short* wsp = (const unsigned short*)d_in[2];
  const unsigned short* bsp = (const unsigned short*)d_in[3];
  const unsigned short* wout = (const unsigned short*)d_in[4];
  char* ws = (char*)d_ws;
  (void)hipFuncSetAttribute((const void*)k1_gemm_gelu, hipFuncAttributeMaxDynamicSharedMemorySize, K1_LDS);
  (void)hipFuncSetAttribute((const void*)k2_spatial, hipFuncAttributeMaxDynamicSharedMemorySize, K2_LDS);
  (void)hipFuncSetAttribute((const void*)k3_out, hipFuncAttributeMaxDynamicSharedMemorySize, K3_LDS);
  k1_gemm_gelu<<<256, 640, K1_LDS, stream>>>(x, win, wsp, wout, ws, (const char*)d_out);
  k2_spatial<<<256, 256, K2_LDS, stream>>>(wsp, bsp, ws);
  k3_out<<<224, 256, K3_LDS, stream>>>(ws, (unsigned short*)d_out);
}
```
